```python
import torch

def _softmax(x, axis=-1):
    return torch.softmax(x, dim=axis)
def _gelu(x):
    return torch.nn.functional.gelu(x, approximate="tanh")
def _silu(x):
    return torch.nn.functional.silu(x)
def _sigmoid(x):
    return torch.sigmoid(x)
def _layernorm(x, axis=-1, eps=1e-5):
    m = x.mean(dim=axis, keepdim=True)
    v = x.var(dim=axis, keepdim=True, unbiased=False)
    return (x - m) / torch.sqrt(v + eps)
def _scatter_add(tgt, idx, src, axis=0):
    return tgt.clone().index_add_(axis, idx, src)
def _segment_sum(src, seg, n):
    out = torch.zeros((n,) + src.shape[1:], dtype=src.dtype)
    return out.index_add_(0, seg, src)
def _pad(x, pw):
    flat = tuple(v for pair in reversed(pw) for v in pair)
    return torch.nn.functional.pad(x, flat)

kernel_name = 'recurrent_scan__sliding_local_dilated__swiglu_ffn__prompt'

def setup_inputs():

    _w = (lambda a: (lambda t: t.to(torch.bfloat16) if t.is_floating_point() else t)(torch.as_tensor(a)))
    def _A_setup():
        B, S, D = 1, 2048, 1024
        return {
            "x": _w((lambda *s: torch.randn(*s, dtype=torch.bfloat16) * 0.05)(B, S, D)),
            "wqkv": _w((lambda *s: torch.randn(*s, dtype=torch.bfloat16) * 0.05)(D, 3 * D)),
            "wo": _w((lambda *s: torch.randn(*s, dtype=torch.bfloat16) * 0.05)(D, D)),
        }
    _A_in = _A_setup()

    def _B_setup():
        B, S, D, F = 1, 2048, 1024, 3584
        return {
            "x": _w((lambda *s: torch.randn(*s, dtype=torch.bfloat16) * 0.05)(B, S, D)),
            "w_gate": _w((lambda *s: torch.randn(*s, dtype=torch.bfloat16) * 0.05)(D, F)),
            "w_up": _w((lambda *s: torch.randn(*s, dtype=torch.bfloat16) * 0.05)(D, F)),
            "w_down": _w((lambda *s: torch.randn(*s, dtype=torch.bfloat16) * 0.05)(F, D)),
        }
    _B_in = _B_setup()

    return {
        "A_x": _A_in["x"],
        "A_wqkv": _A_in["wqkv"],
        "A_wo": _A_in["wo"],
        "B_x": _B_in["x"],
        "B_w_gate": _B_in["w_gate"],
        "B_w_up": _B_in["w_up"],
        "B_w_down": _B_in["w_down"],
    }

def reference(A_x, A_wqkv, A_wo, B_x, B_w_gate, B_w_up, B_w_down):
    def _A_ref(x, wqkv, wo):
        B, S, D = x.shape
        H = 32; Dh = D // H
        W, dil = 8, 2
        qkv = (lambda x, *d: x.permute(*d))((x @ wqkv).reshape(B, S, 3, H, Dh), 2, 0, 3, 1, 4)
        q, k, v = qkv[0], qkv[1], qkv[2]
        scale = Dh ** -0.5
        pos = torch.arange(S)
        off = torch.arange(W) * dil
        idx = pos[:, None] - off[None, :]
        mask = idx >= 0
        idx_c = torch.where(mask, idx, 0).reshape(-1)
        kg = k[:, :, idx_c, :].reshape(B, H, S, W, Dh)
        vg = v[:, :, idx_c, :].reshape(B, H, S, W, Dh)
        logits = torch.einsum("bhsd,bhswd->bhsw", q, kg) * scale
        logits = torch.where(mask[None, None, :, :], logits, -1e9)
        attn = _softmax(logits, axis=-1)
        o = torch.einsum("bhsw,bhswd->bhsd", attn, vg)
        return (lambda x, *d: x.permute(*d))(o, 0, 2, 1, 3).reshape(B, S, D) @ wo

    def _B_ref(x, w_gate, w_up, w_down):
        g = _silu(x @ w_gate)
        u = x @ w_up
        return (g * u) @ w_down

    x = A_x
    xn = _layernorm(x, axis=-1)
    s = _A_ref(xn, A_wqkv, A_wo)
    g = _silu(_B_ref(xn, B_w_gate, B_w_up, B_w_down))
    return x + s * g
```

```cpp
#include <hip/hip_runtime.h>
#include <hip/hip_bf16.h>
typedef unsigned short u16;
__device__ inline float bf2f(u16 b){ return __uint_as_float(((unsigned)b)<<16); }
__device__ inline u16 f2bf(float f){ unsigned u=__float_as_uint(f); u += 0x7FFFu + ((u>>16)&1u); return (u16)(u>>16); }
__device__ inline float rbf(float f){ return bf2f(f2bf(f)); }
constexpr int S=2048, D=1024, F=3584, H=32, DH=32;

__global__ void ln_k(const u16* x, u16* xn){
  int row=blockIdx.x; int t=threadIdx.x;
  __shared__ float red[256];
  float v[4]; float s=0;
  for(int i=0;i<4;i++){ v[i]=bf2f(x[row*D+t*4+i]); s+=v[i]; }
  red[t]=s; __syncthreads();
  for(int o=128;o>0;o>>=1){ if(t<o) red[t]+=red[t+o]; __syncthreads(); }
  float m=red[0]/D; __syncthreads();
  float q=0; for(int i=0;i<4;i++){ float d=v[i]-m; q+=d*d; }
  red[t]=q; __syncthreads();
  for(int o=128;o>0;o>>=1){ if(t<o) red[t]+=red[t+o]; __syncthreads(); }
  float var=red[0]/D;
  float r=1.0f/sqrtf(var+1e-5f);
  for(int i=0;i<4;i++) xn[row*D+t*4+i]=f2bf((v[i]-m)*r);
}
__global__ void gemm_k(const u16* A,const u16* B,u16* C,int M,int N,int K){
  __shared__ float As[32][33], Bs[32][33];
  int tx=threadIdx.x&31, ty=threadIdx.x>>5;
  int r0=blockIdx.y*32, c0=blockIdx.x*32;
  float acc[4]={0,0,0,0};
  for(int k0=0;k0<K;k0+=32){
    for(int i=0;i<4;i++){ As[ty+8*i][tx]=bf2f(A[(size_t)(r0+ty+8*i)*K+k0+tx]); Bs[ty+8*i][tx]=bf2f(B[(size_t)(k0+ty+8*i)*N+c0+tx]); }
    __syncthreads();
    for(int k=0;k<32;k++){ float b=Bs[k][tx]; for(int i=0;i<4;i++) acc[i]+=As[ty+8*i][k]*b; }
    __syncthreads();
  }
  for(int i=0;i<4;i++) C[(size_t)(r0+ty+8*i)*N+c0+tx]=f2bf(acc[i]);
}
__global__ void attn_k(const u16* qkv,u16* o){
  int idx=blockIdx.x*blockDim.x+threadIdx.x;
  int s=idx/H,h=idx%H;
  float lg[8]; float mx=-1e30f;
  for(int w=0;w<8;w++){ int j=s-2*w; float a=-1e9f; if(j>=0){ a=0; for(int d=0;d<DH;d++) a+=bf2f(qkv[(size_t)s*3*D+h*DH+d])*bf2f(qkv[(size_t)j*3*D+D+h*DH+d]); a*=0.17677669529663687f;} lg[w]=a; mx=fmaxf(mx,a);}
  float sum=0; for(int w=0;w<8;w++){ lg[w]=__expf(lg[w]-mx); sum+=lg[w]; }
  for(int d=0;d<DH;d++){ float a=0; for(int w=0;w<8;w++){ int j=s-2*w; if(j<0) j=0; a+=lg[w]/sum*bf2f(qkv[(size_t)j*3*D+2*D+h*DH+d]); } o[(size_t)s*D+h*DH+d]=f2bf(a);}
}
__global__ void swiglu_k(const u16* g,const u16* u,u16* h,int n){ int i=blockIdx.x*blockDim.x+threadIdx.x; if(i<n){ float a=bf2f(g[i]); float s=a/(1.f+__expf(-a)); h[i]=f2bf(s*bf2f(u[i])); } }
__global__ void final_k(const u16* x,const u16* s,const u16* d,u16* out,int n){ int i=blockIdx.x*blockDim.x+threadIdx.x; if(i<n){ float a=bf2f(d[i]); float g=a/(1.f+__expf(-a)); out[i]=f2bf(bf2f(x[i])+bf2f(s[i])*g);} }

extern "C" void kernel_launch(void* const* d_in, const int* in_sizes, int n_in, void* d_out, int out_size, void* d_ws, size_t ws_size, hipStream_t stream){
  const u16* x=(const u16*)d_in[0]; const u16* wqkv=(const u16*)d_in[1]; const u16* wo=(const u16*)d_in[2];
  const u16* wg=(const u16*)d_in[4]; const u16* wu=(const u16*)d_in[5]; const u16* wd=(const u16*)d_in[6];
  u16* ws=(u16*)d_ws;
  u16* xn=ws; u16* qkv=xn+S*D; u16* o=qkv+S*3*D; u16* g=o+S*D; u16* u=g+S*F; u16* h=u+S*F; u16* sb=h+S*F; u16* db=sb+S*D;
  ln_k<<<S,256,0,stream>>>(x,xn);
  gemm_k<<<dim3(3*D/32,S/32),256,0,stream>>>(xn,wqkv,qkv,S,3*D,D);
  attn_k<<<S*H/256,256,0,stream>>>(qkv,o);
  gemm_k<<<dim3(F/32,S/32),256,0,stream>>>(xn,wg,g,S,F,D);
  gemm_k<<<dim3(F/32,S/32),256,0,stream>>>(xn,wu,u,S,F,D);
  swiglu_k<<<S*F/256,256,0,stream>>>(g,u,h,S*F);
  gemm_k<<<dim3(D/32,S/32),256,0,stream>>>(o,wo,sb,S,D,D);
  gemm_k<<<dim3(D/32,S/32),256,0,stream>>>(h,wd,db,S,D,F);
  final_k<<<S*D/256,256,0,stream>>>(x,sb,db,(u16*)d_out,S*D);
}
```

```cpp
#include <hip/hip_runtime.h>
#include <hip/hip_bf16.h>
#include <stdint.h>

typedef unsigned short u16;
typedef __bf16 bf16x8 __attribute__((ext_vector_type(8)));
typedef __bf16 bf16x4 __attribute__((ext_vector_type(4)));
typedef __bf16 bf16x2 __attribute__((ext_vector_type(2)));
typedef float f32x4 __attribute__((ext_vector_type(4)));
typedef float f32x2 __attribute__((ext_vector_type(2)));
typedef unsigned u32x4 __attribute__((ext_vector_type(4)));
typedef unsigned u32x2 __attribute__((ext_vector_type(2)));
#define LAS __attribute__((address_space(3)))
#define DI __device__ __forceinline__
#define SCHED() __builtin_amdgcn_sched_barrier(0)

constexpr int S_ = 2048, D_ = 1024, F_ = 3584;
constexpr size_t WS_XN = 0;
constexpr size_t WS_H = WS_XN + (size_t)128 * 64 * 512;
constexpr size_t WS_O = WS_H + (size_t)128 * 224 * 512;
constexpr size_t WS_EX = WS_O + (size_t)128 * 64 * 512;
constexpr size_t WS_FLAG = WS_EX + (size_t)256 * 32768;
constexpr size_t WS_STAMP = WS_FLAG + (size_t)256 * 4 * 64;

DI unsigned cvtpk(float lo, float hi) { f32x2 v = {lo, hi}; bf16x2 b = __builtin_convertvector(v, bf16x2); return __builtin_bit_cast(unsigned, b); }
DI float bflo(unsigned u) { return __uint_as_float(u << 16); }
DI float bfhi(unsigned u) { return __uint_as_float(u & 0xffff0000u); }
DI float rbf(float x) { return bflo(cvtpk(x, x)); }
DI unsigned rfl(unsigned x) { return __builtin_amdgcn_readfirstlane(x); }
DI const char* uni_ptr(const char* p) { uint64_t a = (uint64_t)p; return (const char*)(((uint64_t)rfl((unsigned)(a >> 32)) << 32) | rfl((unsigned)a)); }
DI int lane_id() { int l; asm volatile("v_mbcnt_lo_u32_b32 %0, -1, 0\n\tv_mbcnt_hi_u32_b32 %0, -1, %0" : "=v"(l)); return l; }
DI float silu_f(float x) { return x * __builtin_amdgcn_rcpf(1.0f + __builtin_amdgcn_exp2f(-1.4426950408889634f * x)); }

DI void glds_s(unsigned ldsaddr, unsigned voff, const char* sbase) {
  asm volatile("s_mov_b32 m0, %0\n\ts_nop 0\n\tglobal_load_lds_dwordx4 %1, %2" :: "s"(ldsaddr), "v"(voff), "s"(sbase) : "memory");
}
DI void glds_v(unsigned ldsaddr, const char* vptr) {
  asm volatile("s_mov_b32 m0, %0\n\ts_nop 0\n\tglobal_load_lds_dwordx4 %1, off" :: "s"(ldsaddr), "v"(vptr) : "memory");
}
DI void glds_sm(unsigned ldsaddr, unsigned voff, const char* sbase, uint64_t mask) {
  asm volatile("s_mov_b32 m0, %0\n\ts_mov_b64 exec, %3\n\tglobal_load_lds_dwordx4 %1, %2\n\ts_mov_b64 exec, -1" :: "s"(ldsaddr), "v"(voff), "s"(sbase), "s"(mask) : "memory");
}
#define WAIT_V(n) asm volatile("s_waitcnt vmcnt(" #n ")" ::: "memory")
#define BAR() do { asm volatile("" ::: "memory"); __builtin_amdgcn_s_barrier(); asm volatile("" ::: "memory"); } while (0)
DI bf16x8 lds_b128(LAS char* p) { return *(LAS bf16x8*)p; }
DI bf16x8 lds_tr(LAS char* p, int off1) {
  bf16x4 lo = __builtin_amdgcn_ds_read_tr16_b64_v4bf16((LAS bf16x4*)p);
  bf16x4 hi = __builtin_amdgcn_ds_read_tr16_b64_v4bf16((LAS bf16x4*)(p + off1));
  return __builtin_shufflevector(lo, hi, 0, 1, 2, 3, 4, 5, 6, 7);
}
#define MFMA(a, b, c) __builtin_amdgcn_mfma_f32_16x16x32_bf16((a), (b), (c), 0, 0, 0)

__global__ __launch_bounds__(128) void k_ln(const u16* __restrict__ x, char* __restrict__ ws) {
  const int lane = threadIdx.x & 63, w = blockIdx.x * 2 + (threadIdx.x >> 6);
  const int row = w * 4 + (lane >> 4), c16 = lane & 15;
  u32x4 v[8];
#pragma unroll
  for (int i = 0; i < 8; ++i) v[i] = *(const u32x4*)(x + (size_t)row * D_ + (c16 + 16 * i) * 8);
  if (threadIdx.x < 16) *(u32x4*)(ws + WS_FLAG + blockIdx.x * 256 + threadIdx.x * 16) = (u32x4){0, 0, 0, 0};
  float s = 0.f;
#pragma unroll
  for (int i = 0; i < 8; ++i)
#pragma unroll
    for (int j = 0; j < 4; ++j) s += bflo(v[i][j]) + bfhi(v[i][j]);
#pragma unroll
  for (int o = 1; o < 16; o <<= 1) s += __shfl_xor(s, o);
  const float m = s * (1.0f / D_);
  float q = 0.f;
#pragma unroll
  for (int i = 0; i < 8; ++i)
#pragma unroll
    for (int j = 0; j < 4; ++j) { float a = bflo(v[i][j]) - m, b = bfhi(v[i][j]) - m; q += a * a + b * b; }
#pragma unroll
  for (int o = 1; o < 16; o <<= 1) q += __shfl_xor(q, o);
  const float mb = rbf(m), vb = rbf(q * (1.0f / D_));
  const float sq = rbf(sqrtf(rbf(vb + 1e-5f)));
  const float rs = 1.0f / sq;
  char* dst = ws + WS_XN + (size_t)(row >> 4) * 32768 + (row & 15) * 32 + (c16 & 1) * 16 + (c16 >> 1) * 512;
#pragma unroll
  for (int i = 0; i < 8; ++i) {
    u32x4 o;
#pragma unroll
    for (int j = 0; j < 4; ++j) { float a = rbf(bflo(v[i][j]) - mb), b = rbf(bfhi(v[i][j]) - mb); o[j] = cvtpk(a / sq, b / sq); }
    *(u32x4*)(dst + i * 8 * 512) = o;
  }
  (void)rs;
}

constexpr int SLOT1 = 37888, XOFF = 0, GOFF = 17408, UOFF = 25600, KVOFF = 33792;
constexpr int LDS1 = 4 * SLOT1;

struct K1Ld {
  const char* xb0; const char* xb1; const char* b4;
  const char* pg; const char* pu;
  unsigned vx, v4, incgu; unsigned inc4; uint64_t mask4; unsigned l0, l4;
};
DI void k1_issue(K1Ld& L, unsigned slot_base, int part) {
  if (part == 0) { glds_s(slot_base + L.l0, L.vx, L.xb0); L.xb0 += 1024; }
  if (part == 1) { glds_s(slot_base + L.l0 + 8192, L.vx, L.xb1); L.xb1 += 1024; }
  if (part == 2) { glds_v(slot_base + GOFF + L.l0, L.pg); L.pg += L.incgu; }
  if (part == 3) { glds_v(slot_base + UOFF + L.l0, L.pu); L.pu += L.incgu; }
  if (part == 4) { glds_sm(slot_base + L.l4, L.v4, L.b4, L.mask4); L.b4 += L.inc4; }
}

template <bool HALO, bool LOAD, int WAITN>
DI void k1_step(f32x4 (&acc)[8][5], f32x4& hacc, bf16x8 (&Wc)[5], bf16x8 (&Wn)[5], bf16x8& X0, bf16x8& X1,
                LAS char* lds, unsigned& aX, unsigned& aG0, unsigned& aG1, unsigned& aKV, K1Ld& L, unsigned& slot_rd, unsigned& slot_ld) {
  bf16x8 Xa = X0, Xb = X1, Xc, Xh;
  const unsigned dn = (slot_rd == 3 * SLOT1) ? (unsigned)(-3 * SLOT1) : (unsigned)SLOT1;
  const unsigned ldb = slot_ld;
#define MM(m, X) do { _Pragma("unroll") for (int n = 0; n < 5; ++n) acc[m][n] = MFMA(Wc[n], X, acc[m][n]); } while (0)
  Xc = lds_b128(lds + aX + 3 * 1024); Wn[0] = lds_tr(lds + aG0, 1024); if (LOAD) k1_issue(L, ldb, 0); SCHED();
  MM(0, Xa); SCHED();
  Xa = lds_b128(lds + aX + 4 * 1024); if (HALO) Xh = lds_b128(lds + aX - 0 * 1024 + 0 - ((int)0)); Wn[1] = lds_tr(lds + aG1, 1024); SCHED();
  MM(1, Xb); SCHED();
  Xb = lds_b128(lds + aX + 5 * 1024); Wn[2] = lds_tr(lds + aG0 + 8192, 1024); if (LOAD) k1_issue(L, ldb, 1); SCHED();
  MM(2, Xc); if (HALO) hacc = MFMA(Wc[4], Xh, hacc); SCHED();
  Xc = lds_b128(lds + aX + 6 * 1024); Wn[3] = lds_tr(lds + aG1 + 8192, 1024); if (LOAD) k1_issue(L, ldb, 2); SCHED();
  MM(3, Xa); SCHED();
  Xa = lds_b128(lds + aX + 7 * 1024); Wn[4] = lds_tr(lds + aKV, 512); if (LOAD) k1_issue(L, ldb, 3); SCHED();
  MM(4, Xb); SCHED();
  Xb = lds_b128(lds + aX + 8 * 1024); if (LOAD) k1_issue(L, ldb, 4); SCHED();
  MM(5, Xc); SCHED();
  aX += dn;
  X0 = lds_b128(lds + aX + 1 * 1024); SCHED();
  MM(6, Xa); SCHED();
  X1 = lds_b128(lds + aX + 2 * 1024); SCHED();
  MM(7, Xb); SCHED();
#undef MM
  slot_rd = (slot_rd == 3 * SLOT1) ? 0 : slot_rd + SLOT1;
  const unsigned dn2 = (slot_rd == 3 * SLOT1) ? (unsigned)(-3 * SLOT1) : (unsigned)SLOT1;
  aG0 += dn2; aG1 += dn2; aKV += dn2;
  slot_ld = (slot_ld == 3 * SLOT1) ? 0 : slot_ld + SLOT1;
  if (WAITN == 5) WAIT_V(5); else if (WAITN == 0) WAIT_V(0);
  if (WAITN >= 0) BAR();
}

template <bool HALO>
DI void k1_loop(f32x4 (&acc)[8][5], f32x4& hacc, LAS char* lds, unsigned aX, unsigned aG0, unsigned aG1, unsigned aKV, K1Ld& L) {
  bf16x8 Wa[5], Wb[5], X0, X1;
  Wa[0] = lds_tr(lds + aG0, 1024); Wa[1] = lds_tr(lds + aG1, 1024); Wa[2] = lds_tr(lds + aG0 + 8192, 1024); Wa[3] = lds_tr(lds + aG1 + 8192, 1024); Wa[4] = lds_tr(lds + aKV, 512);
  X0 = lds_b128(lds + aX + 1024); X1 = lds_b128(lds + aX + 2048);
  aG0 += SLOT1; aG1 += SLOT1; aKV += SLOT1;
  unsigned slot_rd = 0, slot_ld = 3 * SLOT1;
  for (int s = 0; s < 28; s += 2) {
    k1_step<HALO, true, 5>(acc, hacc, Wa, Wb, X0, X1, lds, aX, aG0, aG1, aKV, L, slot_rd, slot_ld);
    k1_step<HALO, true, 5>(acc, hacc, Wb, Wa, X0, X1, lds, aX, aG0, aG1, aKV, L, slot_rd, slot_ld);
  }
  k1_step<HALO, true, 5>(acc, hacc, Wa, Wb, X0, X1, lds, aX, aG0, aG1, aKV, L, slot_rd, slot_ld);
  k1_step<HALO, false, 0>(acc, hacc, Wb, Wa, X0, X1, lds, aX, aG0, aG1, aKV, L, slot_rd, slot_ld);
  k1_step<HALO, false, 1>(acc, hacc, Wa, Wb, X0, X1, lds, aX, aG0, aG1, aKV, L, slot_rd, slot_ld);
  k1_step<HALO, false, -1>(acc, hacc, Wb, Wa, X0, X1, lds, aX, aG0, aG1, aKV, L, slot_rd, slot_ld);
}

DI void k1_epilogue(f32x4 (&acc)[8][5], f32x4& hacc, LAS char* lds, char* ws, const int wid, const int pm, const int pn);

__global__ __launch_bounds__(512, 2) void k_gemm1(const u16* __restrict__ wqkv, const u16* __restrict__ wg, const u16* __restrict__ wu, char* __restrict__ ws) {
  extern __shared__ __attribute__((aligned(16))) char shm[];
  LAS char* lds = (LAS char*)shm;
  const int tid = threadIdx.x, lane = tid & 63;
  const int wid = rfl(tid >> 6), wr = wid >> 2, wc = wid & 3;
  const int b = blockIdx.x;
  const int pn = (b & 7) * 4 + ((b >> 3) & 3), pm = b >> 5;
  const int g = lane >> 4, r = lane & 15, q = (lane & 15) >> 2, p = lane & 3;

  K1Ld L;
  const char* xn = ws + WS_XN;
  { int mb0 = 16 * pm - 1 + wid; if (mb0 < 0) mb0 = 0; L.xb0 = xn + (size_t)mb0 * 32768; L.xb1 = xn + (size_t)(16 * pm + 7 + wid) * 32768; }
  L.vx = lane * 16; L.l0 = wid * 1024;
  { const int row = 4 * wid + g, mask = ((row & 3) << 2) | (((row >> 3) & 1) << 1), ch = r ^ mask;
    if (ch < 14) { L.pg = (const char*)wg + (size_t)row * 7168 + pn * 224 + ch * 16; L.pu = (const char*)wu + (size_t)row * 7168 + pn * 224 + ch * 16; L.incgu = 32 * 7168; }
    else { L.pg = (const char*)wqkv + (size_t)row * 6144 + pn * 64 + (ch - 14) * 16; L.pu = L.pg + 32; L.incgu = 32 * 6144; } }
  if (wid < 4) { L.b4 = xn + (size_t)(16 * pm + 15) * 32768; L.v4 = lane * 16; L.inc4 = 1024; L.l4 = 16 * 1024; L.mask4 = wid == 0 ? ~0ull : 1ull; }
  else { const int z = wid - 4, row = 8 * z + (lane >> 3), mask = (((row >> 3) & 1) << 2) | (((row >> 1) & 1) << 1), ch = (lane & 7) ^ mask;
    L.b4 = (const char*)wqkv; L.v4 = row * 6144 + (ch < 4 ? 2048 + ch * 16 : 4096 + (ch - 4) * 16) + pn * 64; L.inc4 = 32 * 6144; L.l4 = KVOFF + z * 1024; L.mask4 = ~0ull; }
  L.b4 = uni_ptr(L.b4); L.inc4 = rfl(L.inc4); L.l4 = rfl(L.l4);
  L.mask4 = ((uint64_t)rfl((unsigned)(L.mask4 >> 32)) << 32) | rfl((unsigned)L.mask4);
  L.xb0 = uni_ptr(L.xb0); L.xb1 = uni_ptr(L.xb1);
#pragma unroll
  for (int s = 0; s < 3; ++s)
#pragma unroll
    for (int part = 0; part < 5; ++part) k1_issue(L, s * SLOT1, part);

  const unsigned aX = XOFF + wr * 8192 + (g >> 1) * 512 + r * 32 + (g & 1) * 16;
  const int maskG = (q << 2) | ((g & 1) << 1);
  const int rowb = 8 * g + q;
  const unsigned aG0 = GOFF + 256 * rowb + 16 * ((4 * wc + (p >> 1)) ^ maskG) + 8 * (p & 1);
  const unsigned aG1 = aG0 ^ 32;
  const int mask2 = ((g & 1) << 2) | (((q >> 1) & 1) << 1);
  const unsigned aKV = KVOFF + 128 * rowb + 16 * ((2 * wc + (p >> 1)) ^ mask2) + 8 * (p & 1);

  f32x4 acc[8][5]; f32x4 hacc = {0.f, 0.f, 0.f, 0.f};
#pragma unroll
  for (int m = 0; m < 8; ++m)
#pragma unroll
    for (int n = 0; n < 5; ++n) acc[m][n] = (f32x4){0.f, 0.f, 0.f, 0.f};

  WAIT_V(5); BAR();
  k1_loop<true>(acc, hacc, lds, aX, aG0, aG1, aKV, L);
  asm volatile("s_waitcnt lgkmcnt(0)" ::: "memory"); BAR();
  k1_epilogue(acc, hacc, lds, ws, wid, pm, pn);
}

DI void k1_epilogue(f32x4 (&acc)[8][5], f32x4& hacc, LAS char* lds, char* ws, const int wid, const int pm, const int pn) {
  const int lane = lane_id(), wr = wid >> 2, wc = wid & 3;
  const int g = lane >> 4, r = lane & 15, q = (lane & 15) >> 2, p = lane & 3;
  constexpr int QS = 0, KS = 16384, VS = KS + 17408;
  const unsigned wofs = r * 32 + g * 8;
  {
    const unsigned base4 = (wc < 2 ? KS : VS) + (wc & 1) * 8704 + wofs;
#pragma unroll
    for (int m = 0; m < 8; ++m) { u32x2 w2 = {cvtpk(acc[m][4][0], acc[m][4][1]), cvtpk(acc[m][4][2], acc[m][4][3])}; *(LAS u32x2*)(lds + base4 + (1 + 8 * wr + m) * 512) = w2; }
    if (wr == 0) { u32x2 w2 = {cvtpk(hacc[0], hacc[1]), cvtpk(hacc[2], hacc[3])}; *(LAS u32x2*)(lds + base4) = w2; }
    if (wc == 3) {
#pragma unroll
      for (int m = 0; m < 8; ++m) {
        u32x2 a = {cvtpk(acc[m][1][0], acc[m][1][1]), cvtpk(acc[m][1][2], acc[m][1][3])}, c = {cvtpk(acc[m][3][0], acc[m][3][1]), cvtpk(acc[m][3][2], acc[m][3][3])};
        *(LAS u32x2*)(lds + QS + (8 * wr + m) * 1024 + wofs) = a; *(LAS u32x2*)(lds + QS + (8 * wr + m) * 1024 + 512 + wofs) = c; }
    }
  }
  {
    char* H = ws + WS_H;
    auto hfrag = [&](const f32x4& ga, const f32x4& ua) -> u32x2 {
      float h0 = silu_f(ga[0]) * ua[0], h1 = silu_f(ga[1]) * ua[1], h2 = silu_f(ga[2]) * ua[2], h3 = silu_f(ga[3]) * ua[3];
      return (u32x2){cvtpk(h0, h1), cvtpk(h2, h3)}; };
    if (wc < 3) {
      const size_t lo = (size_t)(7 * pn + 2 * wc + (g & 1)) * 512 + r * 32 + (g >> 1) * 16;
#pragma unroll
      for (int m = 0; m < 8; ++m) {
        u32x2 A = hfrag(acc[m][0], acc[m][2]), B = hfrag(acc[m][1], acc[m][3]);
        auto s0 = __builtin_amdgcn_permlane16_swap(A[0], B[0], false, false); auto s1 = __builtin_amdgcn_permlane16_swap(A[1], B[1], false, false);
        u32x4 o = {s0[0], s1[0], s0[1], s1[1]};
        *(u32x4*)(H + (size_t)(16 * pm + 8 * wr + m) * (224 * 512) + lo) = o; }
    } else {
      const size_t lo = (size_t)(7 * pn + 6) * 512 + r * 32 + (g >> 1) * 16 + (size_t)(g & 1) * (224 * 512);
#pragma unroll
      for (int m = 0; m < 8; m += 2) {
        u32x2 A = hfrag(acc[m][0], acc[m][2]), B = hfrag(acc[m + 1][0], acc[m + 1][2]);
        auto s0 = __builtin_amdgcn_permlane16_swap(A[0], B[0], false, false); auto s1 = __builtin_amdgcn_permlane16_swap(A[1], B[1], false, false);
        u32x4 o = {s0[0], s1[0], s0[1], s1[1]};
        *(u32x4*)(H + (size_t)(16 * pm + 8 * wr + m) * (224 * 512) + lo) = o; }
    }
  }
  asm volatile("s_waitcnt lgkmcnt(0)" ::: "memory"); BAR();
#pragma unroll
  for (int ii = 0; ii < 2; ++ii) {
    const int i = 2 * wid + ii;
    const unsigned fo = (g >> 1) * 8704 + r * 32 + (g & 1) * 16;
    bf16x8 qf = lds_b128(lds + QS + i * 1024 + (g >> 1) * 512 + r * 32 + (g & 1) * 16);
    bf16x8 k0 = lds_b128(lds + KS + i * 512 + fo), k1 = lds_b128(lds + KS + (i + 1) * 512 + fo);
    f32x4 z = {0.f, 0.f, 0.f, 0.f};
    f32x4 s0 = MFMA(k0, qf, z), s1 = MFMA(k1, qf, z);
    const bool first = (pm == 0 && i == 0);
    float lg[8]; float mx = -3.0e38f;
#pragma unroll
    for (int j = 0; j < 8; ++j) { const int kap = 16 * (j >> 2) + 4 * g + (j & 3), dl = 16 + r - kap;
      const bool ok = dl >= 0 && dl <= 14 && !(dl & 1) && !(first && j < 4);
      float v = rbf(rbf(j < 4 ? s0[j & 3] : s1[j & 3]) * 0.17677669529663687f);
      lg[j] = ok ? v : -3.0e38f; mx = fmaxf(mx, lg[j]); }
    mx = fmaxf(mx, __shfl_xor(mx, 16)); mx = fmaxf(mx, __shfl_xor(mx, 32));
    float sum = 0.f;
#pragma unroll
    for (int j = 0; j < 8; ++j) { lg[j] = lg[j] > -1.0e38f ? __builtin_amdgcn_exp2f((lg[j] - mx) * 1.4426950408889634f) : 0.f; sum += lg[j]; }
    sum += __shfl_xor(sum, 16); sum += __shfl_xor(sum, 32);
    const float inv = 1.0f / sum;
    u32x4 pw = {cvtpk(lg[0] * inv, lg[1] * inv), cvtpk(lg[2] * inv, lg[3] * inv), cvtpk(lg[4] * inv, lg[5] * inv), cvtpk(lg[6] * inv, lg[7] * inv)};
    bf16x8 pf = __builtin_bit_cast(bf16x8, pw);
    const unsigned va = VS + (i * 16 + 4 * g + q) * 32 + 8 * p;
    bf16x8 v0 = lds_tr(lds + va, 512), v1 = lds_tr(lds + va + 8704, 512);
    f32x4 o0 = MFMA(v0, pf, z), o1 = MFMA(v1, pf, z);
    u32x2 A = {cvtpk(o0[0], o0[1]), cvtpk(o0[2], o0[3])}, B = {cvtpk(o1[0], o1[1]), cvtpk(o1[2], o1[3])};
    auto t0 = __builtin_amdgcn_permlane16_swap(A[0], B[0], false, false); auto t1 = __builtin_amdgcn_permlane16_swap(A[1], B[1], false, false);
    u32x4 o = {t0[0], t1[0], t0[1], t1[1]};
    *(u32x4*)(ws + WS_O + ((size_t)(16 * pm + i) * 64 + 2 * pn + (g & 1)) * 512 + r * 32 + (g >> 1) * 16) = o;
  }
}

constexpr int SLOT2 = 32768, A2 = 0, B2 = 16384;
constexpr int LDS2 = 4 * SLOT2;
constexpr int NST_D = 28, NST_S = 16, NST = NST_D + NST_S;

__global__ __launch_bounds__(512, 2) void k_gemm2(const u16* __restrict__ x, const u16* __restrict__ wo, const u16* __restrict__ wd, char* __restrict__ ws, u16* __restrict__ out) {
  extern __shared__ __attribute__((aligned(16))) char shm[];
  LAS char* lds = (LAS char*)shm;
  const int tid = threadIdx.x, lane = tid & 63;
  const int wid = rfl(tid >> 6);
  const int b = blockIdx.x, xc = b & 7, ib = b >> 3;
  const int kh = xc & 1, pm = ((xc >> 1) & 1) * 8 + (ib & 7), pn = (xc >> 2) * 4 + (ib >> 3);
  const int g = lane >> 4, r = lane & 15, q = (lane & 15) >> 2, p = lane & 3;

  if (wid >= 4) {
    const int lw = wid - 4;
    const unsigned vlin = lane * 16;
    const unsigned vb0 = g * 2048 + ((r ^ (g << 2)) * 16), vb1 = g * 2048 + ((r ^ ((g << 2) | 2)) * 16);
    const char* ha = uni_ptr(ws + WS_H + ((size_t)(8 * pm + 2 * lw) * 224 + 112 * kh) * 512);
    const char* wb = uni_ptr((const char*)wd + ((size_t)1792 * kh + 16 * lw) * 2048 + pn * 256);
    const char* oa = uni_ptr(ws + WS_O + ((size_t)(8 * pm + 4 * kh + lw) * 64) * 512);
    const char* ob = uni_ptr((const char*)wo + (size_t)(16 * lw) * 2048 + pn * 256);
#define K2_ISSUE(t, slot) do { \
      if ((t) < NST_D) { \
        glds_s((slot) + A2 + (2 * lw) * 2048, vlin, ha); glds_s((slot) + A2 + (2 * lw) * 2048 + 1024, vlin, ha + 1024); \
        glds_s((slot) + A2 + (2 * lw + 1) * 2048, vlin, ha + 224 * 512); glds_s((slot) + A2 + (2 * lw + 1) * 2048 + 1024, vlin, ha + 224 * 512 + 1024); \
        ha += 2048; \
        glds_s((slot) + B2 + (16 * lw) * 256, vb0, wb); glds_s((slot) + B2 + (16 * lw + 4) * 256, vb0, wb + 4 * 2048); \
        glds_s((slot) + B2 + (16 * lw + 8) * 256, vb1, wb + 8 * 2048); glds_s((slot) + B2 + (16 * lw + 12) * 256, vb1, wb + 12 * 2048); \
        wb += 64 * 2048; \
      } else if ((t) < NST) { \
        glds_s((slot) + A2 + lw * 2048, vlin, oa); glds_s((slot) + A2 + lw * 2048 + 1024, vlin, oa + 1024); \
        oa += 2048; \
        glds_s((slot) + B2 + (16 * lw) * 256, vb0, ob); glds_s((slot) + B2 + (16 * lw + 4) * 256, vb0, ob + 4 * 2048); \
        glds_s((slot) + B2 + (16 * lw + 8) * 256, vb1, ob + 8 * 2048); glds_s((slot) + B2 + (16 * lw + 12) * 256, vb1, ob + 12 * 2048); \
        ob += 64 * 2048; \
      } } while (0)
    K2_ISSUE(0, 0); K2_ISSUE(1, SLOT2); K2_ISSUE(2, 2 * SLOT2);
    WAIT_V(8); BAR();
    unsigned slot = 3 * SLOT2;
    for (int t = 0; t < NST; ++t) {
      K2_ISSUE(t + 3, slot); slot = slot == 3 * SLOT2 ? 0 : slot + SLOT2;
      if (t + 3 < NST_D) { WAIT_V(8); } else if (t + 3 < NST) { WAIT_V(6); } else { WAIT_V(0); }
      BAR();
    }
    return;
  }
  const int wr = wid >> 1, wc = wid & 1;
  const unsigned aA = A2 + (g >> 1) * 512 + r * 32 + (g & 1) * 16;
  const unsigned aAo = aA + (4 * kh + 2 * wr) * 2048, aAs = aA + (4 * (kh ^ 1) + 2 * wr) * 2048;
  const int maskG = (q << 2) | ((g & 1) << 1);
  unsigned aB[4];
#pragma unroll
  for (int n = 0; n < 4; ++n) aB[n] = B2 + 256 * (8 * g + q) + 16 * ((8 * wc + 2 * n + (p >> 1)) ^ maskG) + 8 * (p & 1);
  f32x4 dacc[2][2][4], sacc[2][4];
#pragma unroll
  for (int n = 0; n < 4; ++n) { dacc[0][0][n] = dacc[0][1][n] = dacc[1][0][n] = dacc[1][1][n] = sacc[0][n] = sacc[1][n] = (f32x4){0.f, 0.f, 0.f, 0.f}; }
  BAR();
  unsigned slot = 0;
  for (int t = 0; t < NST_D; ++t) {
#pragma unroll
    for (int ks = 0; ks < 2; ++ks) {
      bf16x8 Bf[4], Af[4];
#pragma unroll
      for (int n = 0; n < 4; ++n) Bf[n] = lds_tr(lds + slot + aB[n] + ks * 8192, 1024);
#pragma unroll
      for (int i = 0; i < 4; ++i) Af[i] = lds_b128(lds + slot + ((i >> 1) ? aAs : aAo) + (i & 1) * 2048 + ks * 1024);
#pragma unroll
      for (int i = 0; i < 4; ++i)
#pragma unroll
        for (int n = 0; n < 4; ++n) dacc[i >> 1][i & 1][n] = MFMA(Bf[n], Af[i], dacc[i >> 1][i & 1][n]);
    }
    slot = slot == 3 * SLOT2 ? 0 : slot + SLOT2;
    asm volatile("s_waitcnt lgkmcnt(0)" ::: "memory"); BAR();
  }
  const int partner = b ^ 1;
  {
    __amdgpu_buffer_rsrc_t rs = __builtin_amdgcn_make_buffer_rsrc((void*)(ws + WS_EX + (size_t)partner * 32768 + wid * 8192), 0, 8192, 0x00020000);
#pragma unroll
    for (int mm = 0; mm < 2; ++mm)
#pragma unroll
      for (int n = 0; n < 4; ++n) { f32x4 v = dacc[1][mm][n]; __builtin_amdgcn_raw_buffer_store_b128(__builtin_bit_cast(u32x4, v), rs, (mm * 4 + n) * 1024 + lane * 16, 0, 16); }
  }
  for (int t = 0; t < NST_S; ++t) {
#pragma unroll
    for (int ks = 0; ks < 2; ++ks) {
      bf16x8 Bf[4], Af[2];
#pragma unroll
      for (int n = 0; n < 4; ++n) Bf[n] = lds_tr(lds + slot + aB[n] + ks * 8192, 1024);
#pragma unroll
      for (int i = 0; i < 2; ++i) Af[i] = lds_b128(lds + slot + aA + (2 * wr + i) * 2048 + ks * 1024);
#pragma unroll
      for (int i = 0; i < 2; ++i)
#pragma unroll
        for (int n = 0; n < 4; ++n) sacc[i][n] = MFMA(Bf[n], Af[i], sacc[i][n]);
    }
    if (t == 2) {
      asm volatile("s_waitcnt vmcnt(0)" ::: "memory");
      if (lane == 0) __hip_atomic_store((unsigned*)(ws + WS_FLAG + ((size_t)partner * 4 + wid) * 64), 1u, __ATOMIC_RELAXED, __HIP_MEMORY_SCOPE_AGENT);
    }
    slot = slot == 3 * SLOT2 ? 0 : slot + SLOT2;
    asm volatile("s_waitcnt lgkmcnt(0)" ::: "memory"); BAR();
  }
  {
    unsigned* fl = (unsigned*)(ws + WS_FLAG + ((size_t)b * 4 + wid) * 64);
    unsigned spins = 0;
    while (rfl(__hip_atomic_load(fl, __ATOMIC_RELAXED, __HIP_MEMORY_SCOPE_AGENT)) == 0u) { __builtin_amdgcn_s_sleep(1); if (++spins > (1u << 22)) break; }
    __builtin_amdgcn_fence(__ATOMIC_ACQUIRE, "agent");
    __amdgpu_buffer_rsrc_t rs = __builtin_amdgcn_make_buffer_rsrc((void*)(ws + WS_EX + (size_t)b * 32768 + wid * 8192), 0, 8192, 0x00020000);
#pragma unroll
    for (int mm = 0; mm < 2; ++mm) {
      const int row = 128 * pm + 64 * kh + 32 * wr + 16 * mm + r;
#pragma unroll
      for (int np = 0; np < 2; ++np) {
        u32x2 pk[2];
        const int col = 128 * pn + 64 * wc + 32 * np + 16 * (g & 1) + 8 * (g >> 1);
        f32x4 sv[2], gv[2];
#pragma unroll
        for (int e = 0; e < 2; ++e) { const int n = 2 * np + e;
          f32x4 o = __builtin_bit_cast(f32x4, __builtin_amdgcn_raw_buffer_load_b128(rs, (mm * 4 + n) * 1024 + lane * 16, 0, 16));
          f32x4 d = dacc[0][mm][n] + o;
          f32x4 s = sacc[mm][n]; f32x4 y;
#pragma unroll
          for (int j = 0; j < 4; ++j) { float dd = rbf(d[j]); y[j] = rbf(rbf(s[j]) * rbf(silu_f(dd))); }
          pk[e] = (u32x2){cvtpk(y[0], y[1]), cvtpk(y[2], y[3])}; }
        auto t0 = __builtin_amdgcn_permlane16_swap(pk[0][0], pk[1][0], false, false); auto t1 = __builtin_amdgcn_permlane16_swap(pk[0][1], pk[1][1], false, false);
        u32x4 yv = {t0[0], t1[0], t0[1], t1[1]};
        u32x4 xv = *(const u32x4*)(x + (size_t)row * D_ + col); u32x4 ov;
#pragma unroll
        for (int j = 0; j < 4; ++j) ov[j] = cvtpk(bflo(xv[j]) + bflo(yv[j]), bfhi(xv[j]) + bfhi(yv[j]));
        *(u32x4*)(out + (size_t)row * D_ + col) = ov;
        (void)sv; (void)gv;
      }
    }
  }
}

extern "C" void kernel_launch(void* const* d_in, const int* in_sizes, int n_in, void* d_out, int out_size, void* d_ws, size_t ws_size, hipStream_t stream) {
  const u16* x = (const u16*)d_in[0]; const u16* wqkv = (const u16*)d_in[1]; const u16* wo = (const u16*)d_in[2];
  const u16* wg = (const u16*)d_in[4]; const u16* wu = (const u16*)d_in[5]; const u16* wd = (const u16*)d_in[6];
  char* ws = (char*)d_ws;
  hipFuncSetAttribute((const void*)k_gemm1, hipFuncAttributeMaxDynamicSharedMemorySize, LDS1);
  hipFuncSetAttribute((const void*)k_gemm2, hipFuncAttributeMaxDynamicSharedMemorySize, LDS2);
  k_ln<<<256, 128, 0, stream>>>(x, ws);
  k_gemm1<<<256, 512, LDS1, stream>>>(wqkv, wg, wu, ws);
  k_gemm2<<<256, 512, LDS2, stream>>>(x, wo, wd, ws, (u16*)d_out);
}
```

```cpp
#include <hip/hip_runtime.h>
#include <hip/hip_bf16.h>
#include <stdint.h>

typedef unsigned short u16;
typedef __bf16 bf16x8 __attribute__((ext_vector_type(8)));
typedef __bf16 bf16x4 __attribute__((ext_vector_type(4)));
typedef __bf16 bf16x2 __attribute__((ext_vector_type(2)));
typedef float f32x4 __attribute__((ext_vector_type(4)));
typedef float f32x2 __attribute__((ext_vector_type(2)));
typedef unsigned u32x4 __attribute__((ext_vector_type(4)));
typedef unsigned u32x2 __attribute__((ext_vector_type(2)));
#define LAS __attribute__((address_space(3)))
#define DI __device__ __forceinline__
#define SCHED() __builtin_amdgcn_sched_barrier(0)

constexpr int S_ = 2048, D_ = 1024, F_ = 3584;
constexpr size_t WS_H = 0;
constexpr size_t WS_O = WS_H + (size_t)128 * 224 * 512;
constexpr size_t WS_EX = WS_O + (size_t)128 * 64 * 512;
constexpr size_t WS_SYNC = WS_EX + (size_t)256 * 32768;
constexpr unsigned SY_F1 = 4096, SY_F2 = 4096 + 16384, SY_ST = 65536, SY_CSP = 131072, SY_F3 = 655360, SY_F4 = 655360 + 16384, SY_SCR = 786432;
constexpr size_t WS_STAMP = WS_SYNC + 2097152;

DI unsigned cvtpk(float lo, float hi) { f32x2 v = {lo, hi}; bf16x2 b = __builtin_convertvector(v, bf16x2); return __builtin_bit_cast(unsigned, b); }
DI float bflo(unsigned u) { return __uint_as_float(u << 16); }
DI float bfhi(unsigned u) { return __uint_as_float(u & 0xffff0000u); }
DI float rbf(float x) { return bflo(cvtpk(x, x)); }
DI unsigned rfl(unsigned x) { return __builtin_amdgcn_readfirstlane(x); }
DI const char* uni_ptr(const char* p) { uint64_t a = (uint64_t)p; return (const char*)(((uint64_t)rfl((unsigned)(a >> 32)) << 32) | rfl((unsigned)a)); }
DI int lane_id() { int l; asm volatile("v_mbcnt_lo_u32_b32 %0, -1, 0\n\tv_mbcnt_hi_u32_b32 %0, -1, %0" : "=v"(l)); return l; }
DI float silu_f(float x) { return x * __builtin_amdgcn_rcpf(1.0f + __builtin_amdgcn_exp2f(-1.4426950408889634f * x)); }

DI void glds_s(unsigned ldsaddr, unsigned voff, const char* sbase) {
  asm volatile("s_mov_b32 m0, %0\n\ts_nop 0\n\tglobal_load_lds_dwordx4 %1, %2" :: "s"(ldsaddr), "v"(voff), "s"(sbase) : "memory");
}
DI void glds_v(unsigned ldsaddr, const char* vptr) {
  asm volatile("s_mov_b32 m0, %0\n\ts_nop 0\n\tglobal_load_lds_dwordx4 %1, off" :: "s"(ldsaddr), "v"(vptr) : "memory");
}
DI void glds_sm(unsigned ldsaddr, unsigned voff, const char* sbase, uint64_t mask) {
  asm volatile("s_mov_b32 m0, %0\n\ts_mov_b64 exec, %3\n\tglobal_load_lds_dwordx4 %1, %2\n\ts_mov_b64 exec, -1" :: "s"(ldsaddr), "v"(voff), "s"(sbase), "s"(mask) : "memory");
}
#define WAIT_V(n) asm volatile("s_waitcnt vmcnt(" #n ")" ::: "memory")
#define BAR() do { asm volatile("" ::: "memory"); __builtin_amdgcn_s_barrier(); asm volatile("" ::: "memory"); } while (0)
DI bf16x8 lds_b128(LAS char* p) { return *(LAS bf16x8*)p; }
DI bf16x8 lds_tr(LAS char* p, int off1) {
  bf16x4 lo = __builtin_amdgcn_ds_read_tr16_b64_v4bf16((LAS bf16x4*)p);
  bf16x4 hi = __builtin_amdgcn_ds_read_tr16_b64_v4bf16((LAS bf16x4*)(p + off1));
  return __builtin_shufflevector(lo, hi, 0, 1, 2, 3, 4, 5, 6, 7);
}
#define MFMA(a, b, c) __builtin_amdgcn_mfma_f32_16x16x32_bf16((a), (b), (c), 0, 0, 0)

#define STAMP(kern, idx) do {} while (0)
#define XINC 64
constexpr int SLOT1 = 39936, XOFF = 0, GOFF = 17408, UOFF = 25600, ZOFF = 33792;
constexpr int LDS1 = 4 * SLOT1;
typedef float f32x32 __attribute__((ext_vector_type(32)));
typedef unsigned u32x16 __attribute__((ext_vector_type(16)));
typedef unsigned u32x8 __attribute__((ext_vector_type(8)));
#define K1_ASM_A \
"v_mbcnt_lo_u32_b32 v36, -1, 0\n" \
"v_mbcnt_hi_u32_b32 v36, -1, v36\n" \
"v_lshrrev_b32 v37, 5, v36\n" \
"v_lshlrev_b32 v37, 11, v37\n" \
"v_and_b32 v38, 31, v36\n" \
"v_lshl_add_u32 v37, v38, 4, v37\n" \
"v_mov_b32 v38, 0\n" \
"global_load_dwordx2 v[112:113], v38, s[74:75] offset:0 sc1\n" \
"global_load_dwordx4 v[20:23], v37, s[72:73] offset:0\n" \
"global_load_dwordx4 v[24:27], v37, s[72:73] offset:512\n" \
"global_load_dwordx4 v[28:31], v37, s[72:73] offset:1024\n" \
"global_load_dwordx4 v[32:35], v37, s[72:73] offset:1536\n" \
"s_add_u32 m0, s49, 0\n" \
"s_nop 0\n" \
"global_load_lds_dwordx4 v10, s[40:41] offset:0\n" \
"global_load_lds_dwordx4 v11, s[40:41] offset:1024\n" \
"global_load_lds_dwordx4 v12, s[40:41] offset:2048\n" \
"global_load_lds_dwordx4 v13, s[40:41] offset:3072\n" \
"s_add_u32 s40, s40, 64\n" \
"s_addc_u32 s41, s41, 0\n" \
"s_add_u32 m0, s50, 0\n" \
"s_nop 0\n" \
"global_load_lds_dwordx4 v14, s[42:43] offset:0\n" \
"global_load_lds_dwordx4 v15, s[42:43] offset:1024\n" \
"s_add_u32 s42, s42, 229376\n" \
"s_addc_u32 s43, s43, 0\n" \
"s_add_u32 m0, s51, 0\n" \
"s_nop 0\n" \
"global_load_lds_dwordx4 v14, s[44:45] offset:0\n" \
"global_load_lds_dwordx4 v15, s[44:45] offset:1024\n" \
"s_add_u32 s44, s44, 229376\n" \
"s_addc_u32 s45, s45, 0\n" \
"s_add_u32 m0, s52, 0\n" \
"s_nop 0\n" \
"global_load_lds_dwordx4 v16, s[46:47]\n" \
"s_cmp_eq_u32 s53, 0\n" \
"s_cbranch_scc1 LZA%=_10\n" \
"global_load_lds_dwordx4 v17, s[46:47] offset:1024\n" \
"LZA%=_10:\n" \
"s_add_u32 s46, s46, s48\n" \
"s_addc_u32 s47, s47, 0\n" \
"s_add_u32 m0, s49, 39936\n" \
"s_nop 0\n" \
"global_load_lds_dwordx4 v10, s[40:41] offset:0\n" \
"global_load_lds_dwordx4 v11, s[40:41] offset:1024\n" \
"global_load_lds_dwordx4 v12, s[40:41] offset:2048\n" \
"global_load_lds_dwordx4 v13, s[40:41] offset:3072\n" \
"s_add_u32 s40, s40, 64\n" \
"s_addc_u32 s41, s41, 0\n" \
"s_add_u32 m0, s50, 39936\n" \
"s_nop 0\n" \
"global_load_lds_dwordx4 v14, s[42:43] offset:0\n" \
"global_load_lds_dwordx4 v15, s[42:43] offset:1024\n" \
"s_add_u32 s42, s42, 229376\n" \
"s_addc_u32 s43, s43, 0\n" \
"s_add_u32 m0, s51, 39936\n" \
"s_nop 0\n" \
"global_load_lds_dwordx4 v14, s[44:45] offset:0\n" \
"global_load_lds_dwordx4 v15, s[44:45] offset:1024\n" \
"s_add_u32 s44, s44, 229376\n" \
"s_addc_u32 s45, s45, 0\n" \
"s_add_u32 m0, s52, 39936\n" \
"s_nop 0\n" \
"global_load_lds_dwordx4 v16, s[46:47]\n" \
"s_cmp_eq_u32 s53, 0\n" \
"s_cbranch_scc1 LZA%=_20\n" \
"global_load_lds_dwordx4 v17, s[46:47] offset:1024\n" \
"LZA%=_20:\n" \
"s_add_u32 s46, s46, s48\n" \
"s_addc_u32 s47, s47, 0\n" \
"v_mov_b32 %[lane], v36\n"
#define K1_ASM_BODY \
"v_mbcnt_lo_u32_b32 v36, -1, 0\n" \
"v_mbcnt_hi_u32_b32 v36, -1, v36\n" \
"s_waitcnt vmcnt(18)\n" \
"v_readfirstlane_b32 s84, v112\n" \
"v_readfirstlane_b32 s85, v113\n" \
"v_mov_b32 v52, 0x3f803f80\n" \
"v_mov_b32 v40, 0\n" \
"v_mov_b32 v41, 0\n" \
"s_nop 0\n" \
"v_dot2_f32_bf16 v40, v20, v52, v40\n" \
"v_dot2_f32_bf16 v41, v21, v52, v41\n" \
"v_dot2_f32_bf16 v40, v22, v52, v40\n" \
"v_dot2_f32_bf16 v41, v23, v52, v41\n" \
"v_dot2_f32_bf16 v40, v24, v52, v40\n" \
"v_dot2_f32_bf16 v41, v25, v52, v41\n" \
"v_dot2_f32_bf16 v40, v26, v52, v40\n" \
"v_dot2_f32_bf16 v41, v27, v52, v41\n" \
"v_dot2_f32_bf16 v40, v28, v52, v40\n" \
"v_dot2_f32_bf16 v41, v29, v52, v41\n" \
"v_dot2_f32_bf16 v40, v30, v52, v40\n" \
"v_dot2_f32_bf16 v41, v31, v52, v41\n" \
"v_dot2_f32_bf16 v40, v32, v52, v40\n" \
"v_dot2_f32_bf16 v41, v33, v52, v41\n" \
"v_dot2_f32_bf16 v40, v34, v52, v40\n" \
"v_dot2_f32_bf16 v41, v35, v52, v41\n" \
"s_nop 2\n" \
"v_add_f32 v40, v40, v41\n" \
"s_nop 1\n" \
"v_add_f32_dpp v40, v40, v40 row_ror:8 row_mask:0xf bank_mask:0xf\n" \
"s_nop 1\n" \
"v_add_f32_dpp v40, v40, v40 row_ror:4 row_mask:0xf bank_mask:0xf\n" \
"s_nop 1\n" \
"v_add_f32_dpp v40, v40, v40 row_ror:2 row_mask:0xf bank_mask:0xf\n" \
"s_nop 1\n" \
"v_add_f32_dpp v40, v40, v40 row_ror:1 row_mask:0xf bank_mask:0xf\n" \
"v_mov_b32 v41, v40\n" \
"s_nop 1\n" \
"v_permlane16_swap_b32 v40, v41\n" \
"v_add_f32 v40, v40, v41\n" \
"v_mul_f32 v42, 0x3a800000, v40\n" \
"v_mov_b32 v38, 0\n" \
"v_mov_b32 v39, 0\n" \
"v_lshlrev_b32 v44, 16, v20\n" \
"v_and_b32 v45, 0xffff0000, v20\n" \
"v_pk_add_f32 v[44:45], v[44:45], v[42:43] op_sel_hi:[1,0] neg_lo:[0,1] neg_hi:[0,1]\n" \
"v_pk_fma_f32 v[38:39], v[44:45], v[44:45], v[38:39]\n" \
"v_lshlrev_b32 v46, 16, v21\n" \
"v_and_b32 v47, 0xffff0000, v21\n" \
"v_pk_add_f32 v[46:47], v[46:47], v[42:43] op_sel_hi:[1,0] neg_lo:[0,1] neg_hi:[0,1]\n" \
"v_pk_fma_f32 v[38:39], v[46:47], v[46:47], v[38:39]\n" \
"v_lshlrev_b32 v48, 16, v22\n" \
"v_and_b32 v49, 0xffff0000, v22\n" \
"v_pk_add_f32 v[48:49], v[48:49], v[42:43] op_sel_hi:[1,0] neg_lo:[0,1] neg_hi:[0,1]\n" \
"v_pk_fma_f32 v[38:39], v[48:49], v[48:49], v[38:39]\n" \
"v_lshlrev_b32 v50, 16, v23\n" \
"v_and_b32 v51, 0xffff0000, v23\n" \
"v_pk_add_f32 v[50:51], v[50:51], v[42:43] op_sel_hi:[1,0] neg_lo:[0,1] neg_hi:[0,1]\n" \
"v_pk_fma_f32 v[38:39], v[50:51], v[50:51], v[38:39]\n" \
"v_lshlrev_b32 v44, 16, v24\n" \
"v_and_b32 v45, 0xffff0000, v24\n" \
"v_pk_add_f32 v[44:45], v[44:45], v[42:43] op_sel_hi:[1,0] neg_lo:[0,1] neg_hi:[0,1]\n" \
"v_pk_fma_f32 v[38:39], v[44:45], v[44:45], v[38:39]\n" \
"v_lshlrev_b32 v46, 16, v25\n" \
"v_and_b32 v47, 0xffff0000, v25\n" \
"v_pk_add_f32 v[46:47], v[46:47], v[42:43] op_sel_hi:[1,0] neg_lo:[0,1] neg_hi:[0,1]\n" \
"v_pk_fma_f32 v[38:39], v[46:47], v[46:47], v[38:39]\n" \
"v_lshlrev_b32 v48, 16, v26\n" \
"v_and_b32 v49, 0xffff0000, v26\n" \
"v_pk_add_f32 v[48:49], v[48:49], v[42:43] op_sel_hi:[1,0] neg_lo:[0,1] neg_hi:[0,1]\n" \
"v_pk_fma_f32 v[38:39], v[48:49], v[48:49], v[38:39]\n" \
"v_lshlrev_b32 v50, 16, v27\n" \
"v_and_b32 v51, 0xffff0000, v27\n" \
"v_pk_add_f32 v[50:51], v[50:51], v[42:43] op_sel_hi:[1,0] neg_lo:[0,1] neg_hi:[0,1]\n" \
"v_pk_fma_f32 v[38:39], v[50:51], v[50:51], v[38:39]\n" \
"v_lshlrev_b32 v44, 16, v28\n" \
"v_and_b32 v45, 0xffff0000, v28\n" \
"v_pk_add_f32 v[44:45], v[44:45], v[42:43] op_sel_hi:[1,0] neg_lo:[0,1] neg_hi:[0,1]\n" \
"v_pk_fma_f32 v[38:39], v[44:45], v[44:45], v[38:39]\n" \
"v_lshlrev_b32 v46, 16, v29\n" \
"v_and_b32 v47, 0xffff0000, v29\n" \
"v_pk_add_f32 v[46:47], v[46:47], v[42:43] op_sel_hi:[1,0] neg_lo:[0,1] neg_hi:[0,1]\n" \
"v_pk_fma_f32 v[38:39], v[46:47], v[46:47], v[38:39]\n" \
"v_lshlrev_b32 v48, 16, v30\n" \
"v_and_b32 v49, 0xffff0000, v30\n" \
"v_pk_add_f32 v[48:49], v[48:49], v[42:43] op_sel_hi:[1,0] neg_lo:[0,1] neg_hi:[0,1]\n" \
"v_pk_fma_f32 v[38:39], v[48:49], v[48:49], v[38:39]\n" \
"v_lshlrev_b32 v50, 16, v31\n" \
"v_and_b32 v51, 0xffff0000, v31\n" \
"v_pk_add_f32 v[50:51], v[50:51], v[42:43] op_sel_hi:[1,0] neg_lo:[0,1] neg_hi:[0,1]\n" \
"v_pk_fma_f32 v[38:39], v[50:51], v[50:51], v[38:39]\n" \
"v_lshlrev_b32 v44, 16, v32\n" \
"v_and_b32 v45, 0xffff0000, v32\n" \
"v_pk_add_f32 v[44:45], v[44:45], v[42:43] op_sel_hi:[1,0] neg_lo:[0,1] neg_hi:[0,1]\n" \
"v_pk_fma_f32 v[38:39], v[44:45], v[44:45], v[38:39]\n" \
"v_lshlrev_b32 v46, 16, v33\n" \
"v_and_b32 v47, 0xffff0000, v33\n" \
"v_pk_add_f32 v[46:47], v[46:47], v[42:43] op_sel_hi:[1,0] neg_lo:[0,1] neg_hi:[0,1]\n" \
"v_pk_fma_f32 v[38:39], v[46:47], v[46:47], v[38:39]\n" \
"v_lshlrev_b32 v48, 16, v34\n" \
"v_and_b32 v49, 0xffff0000, v34\n" \
"v_pk_add_f32 v[48:49], v[48:49], v[42:43] op_sel_hi:[1,0] neg_lo:[0,1] neg_hi:[0,1]\n" \
"v_pk_fma_f32 v[38:39], v[48:49], v[48:49], v[38:39]\n" \
"v_lshlrev_b32 v50, 16, v35\n" \
"v_and_b32 v51, 0xffff0000, v35\n" \
"v_pk_add_f32 v[50:51], v[50:51], v[42:43] op_sel_hi:[1,0] neg_lo:[0,1] neg_hi:[0,1]\n" \
"v_pk_fma_f32 v[38:39], v[50:51], v[50:51], v[38:39]\n" \
"v_add_f32 v38, v38, v39\n" \
"s_nop 1\n" \
"v_add_f32_dpp v38, v38, v38 row_ror:8 row_mask:0xf bank_mask:0xf\n" \
"s_nop 1\n" \
"v_add_f32_dpp v38, v38, v38 row_ror:4 row_mask:0xf bank_mask:0xf\n" \
"s_nop 1\n" \
"v_add_f32_dpp v38, v38, v38 row_ror:2 row_mask:0xf bank_mask:0xf\n" \
"s_nop 1\n" \
"v_add_f32_dpp v38, v38, v38 row_ror:1 row_mask:0xf bank_mask:0xf\n" \
"v_mov_b32 v39, v38\n" \
"s_nop 1\n" \
"v_permlane16_swap_b32 v38, v39\n" \
"v_add_f32 v38, v38, v39\n" \
"v_mul_f32 v38, 0x3a800000, v38\n" \
"v_cvt_pk_bf16_f32 v38, v38, v38\n" \
"v_lshlrev_b32 v38, 16, v38\n" \
"v_add_f32 v38, 0x3727c5ac, v38\n" \
"v_cvt_pk_bf16_f32 v38, v38, v38\n" \
"v_lshlrev_b32 v38, 16, v38\n" \
"v_sqrt_f32 v38, v38\n" \
"s_nop 0\n" \
"v_cvt_pk_bf16_f32 v40, v42, v38\n" \
"v_lshrrev_b32 v37, 5, v36\n" \
"v_lshlrev_b32 v37, 2, v37\n" \
"v_add_u32 v37, s79, v37\n" \
"s_mov_b32 exec_lo, 1\n" \
"s_mov_b32 exec_hi, 1\n" \
"global_store_dword v37, v40, s[74:75] sc1\n" \
"s_mov_b64 exec, -1\n" \
"v_and_b32 v39, 15, v36\n" \
"v_mov_b32 v40, 0x3f803f80\n" \
"v_cmp_eq_u32 vcc, 0, v39\n" \
"s_nop 1\n" \
"v_cndmask_b32 v38, 0, v40, vcc\n" \
"v_accvgpr_write_b32 a196, v38\n" \
"v_accvgpr_write_b32 a197, v38\n" \
"v_accvgpr_write_b32 a198, v38\n" \
"v_accvgpr_write_b32 a199, v38\n" \
"v_cmp_eq_u32 vcc, 1, v39\n" \
"s_nop 1\n" \
"v_cndmask_b32 v38, 0, v40, vcc\n" \
"v_accvgpr_write_b32 a200, v38\n" \
"v_accvgpr_write_b32 a201, v38\n" \
"v_accvgpr_write_b32 a202, v38\n" \
"v_accvgpr_write_b32 a203, v38\n" \
"v_cmp_eq_u32 vcc, 2, v39\n" \
"s_nop 1\n" \
"v_cndmask_b32 v38, 0, v40, vcc\n" \
"v_accvgpr_write_b32 a204, v38\n" \
"v_accvgpr_write_b32 a205, v38\n" \
"v_accvgpr_write_b32 a206, v38\n" \
"v_accvgpr_write_b32 a207, v38\n" \
"v_cmp_eq_u32 vcc, 3, v39\n" \
"s_nop 1\n" \
"v_cndmask_b32 v38, 0, v40, vcc\n" \
"v_accvgpr_write_b32 a208, v38\n" \
"v_accvgpr_write_b32 a209, v38\n" \
"v_accvgpr_write_b32 a210, v38\n" \
"v_accvgpr_write_b32 a211, v38\n" \
"v_cmp_eq_u32 vcc, 4, v39\n" \
"s_nop 1\n" \
"v_cndmask_b32 v38, 0, v40, vcc\n" \
"v_accvgpr_write_b32 a212, v38\n" \
"v_accvgpr_write_b32 a213, v38\n" \
"v_accvgpr_write_b32 a214, v38\n" \
"v_accvgpr_write_b32 a215, v38\n" \
"s_mov_b32 s87, 0\n" \
"s_mov_b32 s92, %[scr]\n" \
"s_mov_b32 s93, %[wrapit]\n" \
"s_mov_b32 s90, %[wrapz]\n" \
"s_add_u32 s84, s84, 1\n" \
"s_addc_u32 s85, s85, 0\n" \
"s_waitcnt vmcnt(10)\n" \
"s_barrier\n" \
K1_STAMP1 \
"ds_read_b64_tr_b16 v[92:93], v1 offset:0\n" \
"ds_read_b64_tr_b16 v[94:95], v1 offset:1024\n" \
"ds_read_b128 v[20:23], v0 offset:1024\n" \
"ds_read_b128 v[24:27], v0 offset:2048\n" \
"ds_read_b128 v[28:31], v0 offset:3072\n" \
"ds_read_b128 v[32:35], v0 offset:4096\n" \
"ds_read_b128 v[36:39], v0 offset:5120\n" \
"ds_read_b128 v[40:43], v0 offset:6144\n" \
"ds_read_b128 v[44:47], v0 offset:7168\n" \
"ds_read_b128 v[48:51], v0 offset:8192\n" \
"ds_read_b128 v[52:55], v18 offset:0\n" \
"ds_read_b64_tr_b16 v[96:97], v2 offset:0\n" \
"ds_read_b64_tr_b16 v[98:99], v2 offset:1024\n" \
"ds_read_b64_tr_b16 v[100:101], v3 offset:0\n" \
"ds_read_b64_tr_b16 v[102:103], v3 offset:1024\n" \
"s_cmp_lg_u32 s66, 0\n" \
"s_cbranch_scc1 LKD%=\n" \
"s_waitcnt lgkmcnt(9)\n" \
"v_mfma_f32_16x16x32_bf16 a[0:3], v[92:95], v[20:23], 0\n" \
"ds_read_b64_tr_b16 v[104:105], v4 offset:0\n" \
"v_mfma_f32_16x16x32_bf16 a[40:43], v[92:95], v[24:27], 0\n" \
"s_add_u32 m0, s49, 79872\n" \
"v_mfma_f32_16x16x32_bf16 a[80:83], v[92:95], v[28:31], 0\n" \
"ds_read_b64_tr_b16 v[106:107], v5 offset:0\n" \
"global_load_lds_dwordx4 v10, s[40:41] offset:0\n" \
"v_mfma_f32_16x16x32_bf16 a[120:123], v[92:95], v[32:35], 0\n" \
"s_waitcnt lgkmcnt(7)\n" \
"v_mfma_f32_16x16x32_bf16 a[160:163], v[92:95], v[36:39], 0\n" \
"v_mfma_f32_16x16x32_bf16 v[124:127], v[92:95], v[40:43], 0\n" \
"global_load_lds_dwordx4 v11, s[40:41] offset:1024\n" \
"v_mfma_f32_16x16x32_bf16 v[164:167], v[92:95], v[44:47], 0\n" \
"v_mfma_f32_16x16x32_bf16 v[204:207], v[92:95], v[48:51], 0\n" \
"global_load_lds_dwordx4 v12, s[40:41] offset:2048\n" \
"v_mfma_f32_16x16x32_bf16 a[216:219], a[196:199], v[92:95], 0\n" \
"s_waitcnt lgkmcnt(4)\n" \
"v_mfma_f32_16x16x32_bf16 a[4:7], v[96:99], v[20:23], 0\n" \
"ds_read_b64_tr_b16 v[108:109], v1 offset:8192\n" \
"v_mfma_f32_16x16x32_bf16 a[44:47], v[96:99], v[24:27], 0\n" \
"v_mfma_f32_16x16x32_bf16 a[84:87], v[96:99], v[28:31], 0\n" \
"ds_read_b64_tr_b16 v[110:111], v1 offset:9216\n" \
"global_load_lds_dwordx4 v13, s[40:41] offset:3072\n" \
"v_mfma_f32_16x16x32_bf16 a[124:127], v[96:99], v[32:35], 0\n" \
"s_add_u32 s40, s40, 64\n" \
"s_addc_u32 s41, s41, 0\n" \
"v_mfma_f32_16x16x32_bf16 a[164:167], v[96:99], v[36:39], 0\n" \
"s_add_u32 m0, s50, 79872\n" \
"s_nop 0\n" \
"global_load_lds_dwordx4 v14, s[42:43] offset:0\n" \
"v_mfma_f32_16x16x32_bf16 v[128:131], v[96:99], v[40:43], 0\n" \
"v_mfma_f32_16x16x32_bf16 v[168:171], v[96:99], v[44:47], 0\n" \
"v_mfma_f32_16x16x32_bf16 v[208:211], v[96:99], v[48:51], 0\n" \
"global_load_lds_dwordx4 v15, s[42:43] offset:1024\n" \
"v_mfma_f32_16x16x32_bf16 a[216:219], a[200:203], v[96:99], a[216:219]\n" \
"s_waitcnt lgkmcnt(4)\n" \
"v_mfma_f32_16x16x32_bf16 a[8:11], v[100:103], v[20:23], 0\n" \
"s_add_u32 s42, s42, 229376\n" \
"s_addc_u32 s43, s43, 0\n" \
"v_mfma_f32_16x16x32_bf16 a[48:51], v[100:103], v[24:27], 0\n" \
"ds_read_b64_tr_b16 v[92:93], v2 offset:8192\n" \
"s_add_u32 m0, s51, 79872\n" \
"v_mfma_f32_16x16x32_bf16 a[88:91], v[100:103], v[28:31], 0\n" \
"global_load_lds_dwordx4 v14, s[44:45] offset:0\n" \
"ds_read_b64_tr_b16 v[94:95], v2 offset:9216\n" \
"v_mfma_f32_16x16x32_bf16 a[128:131], v[100:103], v[32:35], 0\n" \
"v_mfma_f32_16x16x32_bf16 a[168:171], v[100:103], v[36:39], 0\n" \
"global_load_lds_dwordx4 v15, s[44:45] offset:1024\n" \
"v_mfma_f32_16x16x32_bf16 v[132:135], v[100:103], v[40:43], 0\n" \
"s_add_u32 s44, s44, 229376\n" \
"s_addc_u32 s45, s45, 0\n" \
"v_mfma_f32_16x16x32_bf16 v[172:175], v[100:103], v[44:47], 0\n" \
"s_add_u32 m0, s52, 79872\n" \
"s_nop 0\n" \
"global_load_lds_dwordx4 v16, s[46:47]\n" \
"v_mfma_f32_16x16x32_bf16 v[212:215], v[100:103], v[48:51], 0\n" \
"v_mfma_f32_16x16x32_bf16 a[216:219], a[204:207], v[100:103], a[216:219]\n" \
"s_waitcnt lgkmcnt(4)\n" \
"v_mfma_f32_16x16x32_bf16 a[12:15], v[104:107], v[20:23], 0\n" \
"ds_read_b64_tr_b16 v[96:97], v3 offset:8192\n" \
"v_mfma_f32_16x16x32_bf16 a[52:55], v[104:107], v[24:27], 0\n" \
"s_cmp_eq_u32 s53, 0\n" \
"s_cbranch_scc1 LZ%=_31\n" \
"global_load_lds_dwordx4 v17, s[46:47] offset:1024\n" \
"LZ%=_31:\n" \
"v_mfma_f32_16x16x32_bf16 a[92:95], v[104:107], v[28:31], 0\n" \
"s_add_u32 s46, s46, s48\n" \
"s_addc_u32 s47, s47, 0\n" \
"v_mfma_f32_16x16x32_bf16 a[132:135], v[104:107], v[32:35], 0\n" \
"ds_read_b64_tr_b16 v[98:99], v3 offset:9216\n" \
"s_add_u32 m0, s49, 119808\n" \
"v_mfma_f32_16x16x32_bf16 a[172:175], v[104:107], v[36:39], 0\n" \
"global_load_lds_dwordx4 v10, s[40:41] offset:0\n" \
"v_mfma_f32_16x16x32_bf16 v[136:139], v[104:107], v[40:43], 0\n" \
"v_mfma_f32_16x16x32_bf16 v[176:179], v[104:107], v[44:47], 0\n" \
"global_load_lds_dwordx4 v11, s[40:41] offset:1024\n" \
"v_mfma_f32_16x16x32_bf16 v[216:219], v[104:107], v[48:51], 0\n" \
"v_mfma_f32_16x16x32_bf16 a[216:219], a[208:211], v[104:107], a[216:219]\n" \
"s_waitcnt lgkmcnt(4)\n" \
"v_mfma_f32_16x16x32_bf16 a[16:19], v[108:111], v[20:23], 0\n" \
"ds_read_b64_tr_b16 v[100:101], v6 offset:0\n" \
"global_load_lds_dwordx4 v12, s[40:41] offset:2048\n" \
"v_mfma_f32_16x16x32_bf16 a[56:59], v[108:111], v[24:27], 0\n" \
"v_mfma_f32_16x16x32_bf16 a[96:99], v[108:111], v[28:31], 0\n" \
"ds_read_b64_tr_b16 v[102:103], v7 offset:0\n" \
"v_mfma_f32_16x16x32_bf16 a[136:139], v[108:111], v[32:35], 0\n" \
"global_load_lds_dwordx4 v13, s[40:41] offset:3072\n" \
"v_mfma_f32_16x16x32_bf16 a[176:179], v[108:111], v[36:39], 0\n" \
"s_add_u32 s40, s40, 64\n" \
"s_addc_u32 s41, s41, 0\n" \
"v_mfma_f32_16x16x32_bf16 v[140:143], v[108:111], v[40:43], 0\n" \
"s_add_u32 m0, s50, 119808\n" \
"s_nop 0\n" \
"global_load_lds_dwordx4 v14, s[42:43] offset:0\n" \
"v_mfma_f32_16x16x32_bf16 v[180:183], v[108:111], v[44:47], 0\n" \
"v_mfma_f32_16x16x32_bf16 v[220:223], v[108:111], v[48:51], 0\n" \
"v_mfma_f32_16x16x32_bf16 a[216:219], a[212:215], v[108:111], a[216:219]\n" \
"s_waitcnt lgkmcnt(4)\n" \
"v_mfma_f32_16x16x32_bf16 a[20:23], v[92:95], v[20:23], 0\n" \
"ds_read_b64_tr_b16 v[104:105], v8 offset:0\n" \
"global_load_lds_dwordx4 v15, s[42:43] offset:1024\n" \
"v_mfma_f32_16x16x32_bf16 a[60:63], v[92:95], v[24:27], 0\n" \
"s_add_u32 s42, s42, 229376\n" \
"s_addc_u32 s43, s43, 0\n" \
"v_mfma_f32_16x16x32_bf16 a[100:103], v[92:95], v[28:31], 0\n" \
"s_add_u32 m0, s51, 119808\n" \
"ds_read_b64_tr_b16 v[106:107], v8 offset:768\n" \
"v_mfma_f32_16x16x32_bf16 a[140:143], v[92:95], v[32:35], 0\n" \
"global_load_lds_dwordx4 v14, s[44:45] offset:0\n" \
"v_mfma_f32_16x16x32_bf16 a[180:183], v[92:95], v[36:39], 0\n" \
"v_mfma_f32_16x16x32_bf16 v[144:147], v[92:95], v[40:43], 0\n" \
"global_load_lds_dwordx4 v15, s[44:45] offset:1024\n" \
"v_mfma_f32_16x16x32_bf16 v[184:187], v[92:95], v[44:47], 0\n" \
"s_add_u32 s44, s44, 229376\n" \
"s_addc_u32 s45, s45, 0\n" \
"v_mfma_f32_16x16x32_bf16 v[224:227], v[92:95], v[48:51], 0\n" \
"s_add_u32 m0, s52, 119808\n" \
"s_waitcnt lgkmcnt(4)\n" \
"v_mfma_f32_16x16x32_bf16 a[24:27], v[96:99], v[20:23], 0\n" \
"global_load_lds_dwordx4 v16, s[46:47]\n" \
"ds_read_b64_tr_b16 v[108:109], v9 offset:0\n" \
"v_mfma_f32_16x16x32_bf16 a[64:67], v[96:99], v[24:27], 0\n" \
"v_mfma_f32_16x16x32_bf16 a[104:107], v[96:99], v[28:31], 0\n" \
"ds_read_b64_tr_b16 v[110:111], v9 offset:768\n" \
"v_mfma_f32_16x16x32_bf16 a[144:147], v[96:99], v[32:35], 0\n" \
"s_cmp_eq_u32 s53, 0\n" \
"s_cbranch_scc1 LZ%=_41\n" \
"global_load_lds_dwordx4 v17, s[46:47] offset:1024\n" \
"LZ%=_41:\n" \
"v_mfma_f32_16x16x32_bf16 a[184:187], v[96:99], v[36:39], 0\n" \
"s_add_u32 s46, s46, s48\n" \
"s_addc_u32 s47, s47, 0\n" \
"v_mfma_f32_16x16x32_bf16 v[148:151], v[96:99], v[40:43], 0\n" \
"v_mfma_f32_16x16x32_bf16 v[188:191], v[96:99], v[44:47], 0\n" \
"v_mfma_f32_16x16x32_bf16 v[228:231], v[96:99], v[48:51], 0\n" \
"s_waitcnt lgkmcnt(0)\n" \
"s_waitcnt vmcnt(18)\n" \
"s_barrier\n" \
"v_mfma_f32_16x16x32_bf16 a[28:31], v[100:103], v[20:23], 0\n" \
"v_add_u32 v1, 39936, v1\n" \
"ds_read_b64_tr_b16 v[92:93], v1 offset:0\n" \
"v_mfma_f32_16x16x32_bf16 a[68:71], v[100:103], v[24:27], 0\n" \
"ds_read_b64_tr_b16 v[94:95], v1 offset:1024\n" \
"v_mfma_f32_16x16x32_bf16 a[108:111], v[100:103], v[28:31], 0\n" \
"v_add_u32 v0, 39936, v0\n" \
"ds_read_b128 v[56:59], v0 offset:1024\n" \
"v_mfma_f32_16x16x32_bf16 a[148:151], v[100:103], v[32:35], 0\n" \
"v_add_u32 v4, 39936, v4\n" \
"ds_read_b128 v[60:63], v0 offset:2048\n" \
"v_mfma_f32_16x16x32_bf16 a[188:191], v[100:103], v[36:39], 0\n" \
"ds_read_b128 v[64:67], v0 offset:3072\n" \
"v_add_u32 v5, 39936, v5\n" \
"v_mfma_f32_16x16x32_bf16 v[152:155], v[100:103], v[40:43], 0\n" \
"ds_read_b128 v[68:71], v0 offset:4096\n" \
"v_mfma_f32_16x16x32_bf16 v[192:195], v[100:103], v[44:47], 0\n" \
"ds_read_b128 v[72:75], v0 offset:5120\n" \
"v_add_u32 v6, 39936, v6\n" \
"v_mfma_f32_16x16x32_bf16 v[232:235], v[100:103], v[48:51], 0\n" \
"ds_read_b128 v[76:79], v0 offset:6144\n" \
"v_mfma_f32_16x16x32_bf16 a[32:35], v[104:107], v[20:23], 0\n" \
"ds_read_b128 v[80:83], v0 offset:7168\n" \
"v_add_u32 v7, 39936, v7\n" \
"v_mfma_f32_16x16x32_bf16 a[72:75], v[104:107], v[24:27], 0\n" \
"ds_read_b128 v[84:87], v0 offset:8192\n" \
"v_mfma_f32_16x16x32_bf16 a[112:115], v[104:107], v[28:31], 0\n" \
"v_add_u32 v18, 39936, v18\n" \
"ds_read_b128 v[88:91], v18 offset:0\n" \
"v_mfma_f32_16x16x32_bf16 a[152:155], v[104:107], v[32:35], 0\n" \
"v_add_u32 v8, 39936, v8\n" \
"v_add_u32 v2, 39936, v2\n" \
"v_mfma_f32_16x16x32_bf16 a[192:195], v[104:107], v[36:39], 0\n" \
"ds_read_b64_tr_b16 v[96:97], v2 offset:0\n" \
"ds_read_b64_tr_b16 v[98:99], v2 offset:1024\n" \
"v_mfma_f32_16x16x32_bf16 v[156:159], v[104:107], v[40:43], 0\n" \
"v_add_u32 v9, 39936, v9\n" \
"v_add_u32 v3, 39936, v3\n" \
"v_mfma_f32_16x16x32_bf16 v[196:199], v[104:107], v[44:47], 0\n" \
"ds_read_b64_tr_b16 v[100:101], v3 offset:0\n" \
"ds_read_b64_tr_b16 v[102:103], v3 offset:1024\n" \
"v_mfma_f32_16x16x32_bf16 v[236:239], v[104:107], v[48:51], 0\n" \
"v_mfma_f32_16x16x32_bf16 v[244:247], v[104:107], v[52:55], 0\n" \
"v_mfma_f32_16x16x32_bf16 a[36:39], v[108:111], v[20:23], 0\n" \
"v_mfma_f32_16x16x32_bf16 a[76:79], v[108:111], v[24:27], 0\n" \
"v_mfma_f32_16x16x32_bf16 a[116:119], v[108:111], v[28:31], 0\n" \
"v_mfma_f32_16x16x32_bf16 a[156:159], v[108:111], v[32:35], 0\n" \
"v_mfma_f32_16x16x32_bf16 v[120:123], v[108:111], v[36:39], 0\n" \
"v_mfma_f32_16x16x32_bf16 v[160:163], v[108:111], v[40:43], 0\n" \
"s_cmp_lg_u32 s49, 0\n" \
"s_cbranch_scc1 LPC%=_21\n" \
"v_mbcnt_lo_u32_b32 v113, -1, 0\n" \
"v_mbcnt_hi_u32_b32 v113, -1, v113\n" \
"v_lshlrev_b32 v113, 7, v113\n" \
"s_getpc_b64 s[88:89]\n" \
"LPD%=_21:\n" \
"s_mov_b32 s86, " CODE_END "-LPD%=_21-4\n" \
"v_min_u32 v114, s86, v113\n" \
"global_load_dword v255, v114, s[88:89]\n" \
"v_add_u32 v113, 8192, v113\n" \
"v_min_u32 v114, s86, v113\n" \
"global_load_dword v255, v114, s[88:89]\n" \
"v_add_u32 v113, 8192, v113\n" \
"v_min_u32 v114, s86, v113\n" \
"global_load_dword v255, v114, s[88:89]\n" \
"v_add_u32 v113, 8192, v113\n" \
"v_min_u32 v114, s86, v113\n" \
"global_load_dword v255, v114, s[88:89]\n" \
"v_add_u32 v113, 8192, v113\n" \
"v_min_u32 v114, s86, v113\n" \
"global_load_dword v255, v114, s[88:89]\n" \
"v_add_u32 v113, 8192, v113\n" \
"v_min_u32 v114, s86, v113\n" \
"global_load_dword v255, v114, s[88:89]\n" \
"v_add_u32 v113, 8192, v113\n" \
"v_min_u32 v114, s86, v113\n" \
"global_load_dword v255, v114, s[88:89]\n" \
"v_add_u32 v113, 8192, v113\n" \
"v_min_u32 v114, s86, v113\n" \
"global_load_dword v255, v114, s[88:89]\n" \
"v_add_u32 v113, 8192, v113\n" \
"LPC%=_21:\n" \
"v_mfma_f32_16x16x32_bf16 v[200:203], v[108:111], v[44:47], 0\n" \
"v_mfma_f32_16x16x32_bf16 v[240:243], v[108:111], v[48:51], 0\n" \
"s_waitcnt lgkmcnt(9)\n" \
"v_mfma_f32_16x16x32_bf16 a[0:3], v[92:95], v[56:59], a[0:3]\n" \
"ds_read_b64_tr_b16 v[104:105], v4 offset:0\n" \
"v_mfma_f32_16x16x32_bf16 a[40:43], v[92:95], v[60:63], a[40:43]\n" \
"s_cmp_eq_u32 s87, s93\n" \
"s_cbranch_scc1 LWR%=_52\n" \
"LWS%=_52:\n" \
"v_mfma_f32_16x16x32_bf16 a[80:83], v[92:95], v[64:67], a[80:83]\n" \
"ds_read_b64_tr_b16 v[106:107], v5 offset:0\n" \
"s_add_u32 m0, s49, 0\n" \
"v_mfma_f32_16x16x32_bf16 a[120:123], v[92:95], v[68:71], a[120:123]\n" \
"global_load_lds_dwordx4 v10, s[40:41] offset:0\n" \
"s_waitcnt lgkmcnt(7)\n" \
"v_mfma_f32_16x16x32_bf16 a[160:163], v[92:95], v[72:75], a[160:163]\n" \
"v_mfma_f32_16x16x32_bf16 v[124:127], v[92:95], v[76:79], v[124:127]\n" \
"v_mfma_f32_16x16x32_bf16 v[164:167], v[92:95], v[80:83], v[164:167]\n" \
"v_mfma_f32_16x16x32_bf16 v[204:207], v[92:95], v[84:87], v[204:207]\n" \
"v_mfma_f32_16x16x32_bf16 a[216:219], a[196:199], v[92:95], a[216:219]\n" \
"s_waitcnt lgkmcnt(4)\n" \
"v_mfma_f32_16x16x32_bf16 a[4:7], v[96:99], v[56:59], a[4:7]\n" \
"ds_read_b64_tr_b16 v[108:109], v1 offset:8192\n" \
"global_load_lds_dwordx4 v11, s[40:41] offset:1024\n" \
"v_mfma_f32_16x16x32_bf16 a[44:47], v[96:99], v[60:63], a[44:47]\n" \
"v_mfma_f32_16x16x32_bf16 a[84:87], v[96:99], v[64:67], a[84:87]\n" \
"ds_read_b64_tr_b16 v[110:111], v1 offset:9216\n" \
"v_mfma_f32_16x16x32_bf16 a[124:127], v[96:99], v[68:71], a[124:127]\n" \
"v_mfma_f32_16x16x32_bf16 a[164:167], v[96:99], v[72:75], a[164:167]\n" \
"v_mfma_f32_16x16x32_bf16 v[128:131], v[96:99], v[76:79], v[128:131]\n" \
"global_load_lds_dwordx4 v12, s[40:41] offset:2048\n" \
"v_mfma_f32_16x16x32_bf16 v[168:171], v[96:99], v[80:83], v[168:171]\n" \
"v_mfma_f32_16x16x32_bf16 v[208:211], v[96:99], v[84:87], v[208:211]\n" \
"v_mfma_f32_16x16x32_bf16 a[216:219], a[200:203], v[96:99], a[216:219]\n" \
"s_waitcnt lgkmcnt(4)\n" \
"v_mfma_f32_16x16x32_bf16 a[8:11], v[100:103], v[56:59], a[8:11]\n" \
"ds_read_b64_tr_b16 v[92:93], v2 offset:8192\n" \
"v_mfma_f32_16x16x32_bf16 a[48:51], v[100:103], v[60:63], a[48:51]\n" \
"v_mfma_f32_16x16x32_bf16 a[88:91], v[100:103], v[64:67], a[88:91]\n" \
"ds_read_b64_tr_b16 v[94:95], v2 offset:9216\n" \
"global_load_lds_dwordx4 v13, s[40:41] offset:3072\n" \
"v_mfma_f32_16x16x32_bf16 a[128:131], v[100:103], v[68:71], a[128:131]\n" \
"s_add_u32 s40, s40, 64\n" \
"s_addc_u32 s41, s41, 0\n" \
"v_mfma_f32_16x16x32_bf16 a[168:171], v[100:103], v[72:75], a[168:171]\n" \
"v_mfma_f32_16x16x32_bf16 v[132:135], v[100:103], v[76:79], v[132:135]\n" \
"v_mfma_f32_16x16x32_bf16 v[172:175], v[100:103], v[80:83], v[172:175]\n" \
"s_add_u32 m0, s50, 0\n" \
"v_mfma_f32_16x16x32_bf16 v[212:215], v[100:103], v[84:87], v[212:215]\n" \
"global_load_lds_dwordx4 v14, s[42:43] offset:0\n" \
"v_mfma_f32_16x16x32_bf16 a[216:219], a[204:207], v[100:103], a[216:219]\n" \
"s_waitcnt lgkmcnt(4)\n" \
"v_mfma_f32_16x16x32_bf16 a[12:15], v[104:107], v[56:59], a[12:15]\n" \
"ds_read_b64_tr_b16 v[96:97], v3 offset:8192\n" \
"v_mfma_f32_16x16x32_bf16 a[52:55], v[104:107], v[60:63], a[52:55]\n" \
"v_mfma_f32_16x16x32_bf16 a[92:95], v[104:107], v[64:67], a[92:95]\n" \
"ds_read_b64_tr_b16 v[98:99], v3 offset:9216\n" \
"v_mfma_f32_16x16x32_bf16 a[132:135], v[104:107], v[68:71], a[132:135]\n" \
"v_mfma_f32_16x16x32_bf16 a[172:175], v[104:107], v[72:75], a[172:175]\n" \
"global_load_lds_dwordx4 v15, s[42:43] offset:1024\n" \
"v_mfma_f32_16x16x32_bf16 v[136:139], v[104:107], v[76:79], v[136:139]\n" \
"s_add_u32 s42, s42, 229376\n" \
"s_addc_u32 s43, s43, 0\n" \
"v_mfma_f32_16x16x32_bf16 v[176:179], v[104:107], v[80:83], v[176:179]\n" \
"v_mfma_f32_16x16x32_bf16 v[216:219], v[104:107], v[84:87], v[216:219]\n" \
"v_mfma_f32_16x16x32_bf16 a[216:219], a[208:211], v[104:107], a[216:219]\n" \
"s_waitcnt lgkmcnt(4)\n" \
"v_mfma_f32_16x16x32_bf16 a[16:19], v[108:111], v[56:59], a[16:19]\n" \
"ds_read_b64_tr_b16 v[100:101], v6 offset:0\n" \
"s_add_u32 m0, s51, 0\n" \
"v_mfma_f32_16x16x32_bf16 a[56:59], v[108:111], v[60:63], a[56:59]\n" \
"global_load_lds_dwordx4 v14, s[44:45] offset:0\n" \
"v_mfma_f32_16x16x32_bf16 a[96:99], v[108:111], v[64:67], a[96:99]\n" \
"ds_read_b64_tr_b16 v[102:103], v7 offset:0\n" \
"v_mfma_f32_16x16x32_bf16 a[136:139], v[108:111], v[68:71], a[136:139]\n" \
"v_mfma_f32_16x16x32_bf16 a[176:179], v[108:111], v[72:75], a[176:179]\n" \
"v_mfma_f32_16x16x32_bf16 v[140:143], v[108:111], v[76:79], v[140:143]\n" \
"v_mfma_f32_16x16x32_bf16 v[180:183], v[108:111], v[80:83], v[180:183]\n" \
"global_load_lds_dwordx4 v15, s[44:45] offset:1024\n" \
"v_mfma_f32_16x16x32_bf16 v[220:223], v[108:111], v[84:87], v[220:223]\n" \
"s_add_u32 s44, s44, 229376\n" \
"s_addc_u32 s45, s45, 0\n" \
"v_mfma_f32_16x16x32_bf16 a[216:219], a[212:215], v[108:111], a[216:219]\n" \
"s_waitcnt lgkmcnt(4)\n" \
"v_mfma_f32_16x16x32_bf16 a[20:23], v[92:95], v[56:59], a[20:23]\n" \
"ds_read_b64_tr_b16 v[104:105], v8 offset:0\n" \
"v_mfma_f32_16x16x32_bf16 a[60:63], v[92:95], v[60:63], a[60:63]\n" \
"v_mfma_f32_16x16x32_bf16 a[100:103], v[92:95], v[64:67], a[100:103]\n" \
"ds_read_b64_tr_b16 v[106:107], v8 offset:768\n" \
"s_add_u32 m0, s52, 0\n" \
"v_mfma_f32_16x16x32_bf16 a[140:143], v[92:95], v[68:71], a[140:143]\n" \
"global_load_lds_dwordx4 v16, s[46:47]\n" \
"v_mfma_f32_16x16x32_bf16 a[180:183], v[92:95], v[72:75], a[180:183]\n" \
"v_mfma_f32_16x16x32_bf16 v[144:147], v[92:95], v[76:79], v[144:147]\n" \
"v_mfma_f32_16x16x32_bf16 v[184:187], v[92:95], v[80:83], v[184:187]\n" \
"v_mfma_f32_16x16x32_bf16 v[224:227], v[92:95], v[84:87], v[224:227]\n" \
"s_waitcnt lgkmcnt(4)\n" \
"v_mfma_f32_16x16x32_bf16 a[24:27], v[96:99], v[56:59], a[24:27]\n" \
"ds_read_b64_tr_b16 v[108:109], v9 offset:0\n" \
"v_mfma_f32_16x16x32_bf16 a[64:67], v[96:99], v[60:63], a[64:67]\n" \
"s_cmp_eq_u32 s53, 0\n" \
"s_cbranch_scc1 LZ%=_51\n" \
"global_load_lds_dwordx4 v17, s[46:47] offset:1024\n" \
"LZ%=_51:\n" \
"v_mfma_f32_16x16x32_bf16 a[104:107], v[96:99], v[64:67], a[104:107]\n" \
"s_add_u32 s46, s46, s48\n" \
"s_addc_u32 s47, s47, 0\n" \
"v_mfma_f32_16x16x32_bf16 a[144:147], v[96:99], v[68:71], a[144:147]\n" \
"ds_read_b64_tr_b16 v[110:111], v9 offset:768\n" \
"v_mfma_f32_16x16x32_bf16 a[184:187], v[96:99], v[72:75], a[184:187]\n" \
"v_mfma_f32_16x16x32_bf16 v[148:151], v[96:99], v[76:79], v[148:151]\n" \
"v_mfma_f32_16x16x32_bf16 v[188:191], v[96:99], v[80:83], v[188:191]\n" \
"v_mfma_f32_16x16x32_bf16 v[228:231], v[96:99], v[84:87], v[228:231]\n" \
"s_waitcnt lgkmcnt(0)\n" \
"s_waitcnt vmcnt(18)\n" \
"s_barrier\n" \
"v_mfma_f32_16x16x32_bf16 a[28:31], v[100:103], v[56:59], a[28:31]\n" \
"v_add_u32 v1, 39936, v1\n" \
"ds_read_b64_tr_b16 v[92:93], v1 offset:0\n" \
"v_mfma_f32_16x16x32_bf16 a[68:71], v[100:103], v[60:63], a[68:71]\n" \
"ds_read_b64_tr_b16 v[94:95], v1 offset:1024\n" \
"v_mfma_f32_16x16x32_bf16 a[108:111], v[100:103], v[64:67], a[108:111]\n" \
"v_add_u32 v0, 39936, v0\n" \
"ds_read_b128 v[20:23], v0 offset:1024\n" \
"v_mfma_f32_16x16x32_bf16 a[148:151], v[100:103], v[68:71], a[148:151]\n" \
"v_add_u32 v4, 39936, v4\n" \
"ds_read_b128 v[24:27], v0 offset:2048\n" \
"v_mfma_f32_16x16x32_bf16 a[188:191], v[100:103], v[72:75], a[188:191]\n" \
"ds_read_b128 v[28:31], v0 offset:3072\n" \
"v_add_u32 v5, 39936, v5\n" \
"v_mfma_f32_16x16x32_bf16 v[152:155], v[100:103], v[76:79], v[152:155]\n" \
"ds_read_b128 v[32:35], v0 offset:4096\n" \
"v_mfma_f32_16x16x32_bf16 v[192:195], v[100:103], v[80:83], v[192:195]\n" \
"ds_read_b128 v[36:39], v0 offset:5120\n" \
"v_add_u32 v6, 39936, v6\n" \
"v_mfma_f32_16x16x32_bf16 v[232:235], v[100:103], v[84:87], v[232:235]\n" \
"ds_read_b128 v[40:43], v0 offset:6144\n" \
"v_mfma_f32_16x16x32_bf16 a[32:35], v[104:107], v[56:59], a[32:35]\n" \
"ds_read_b128 v[44:47], v0 offset:7168\n" \
"v_add_u32 v7, 39936, v7\n" \
"v_mfma_f32_16x16x32_bf16 a[72:75], v[104:107], v[60:63], a[72:75]\n" \
"ds_read_b128 v[48:51], v0 offset:8192\n" \
"v_mfma_f32_16x16x32_bf16 a[112:115], v[104:107], v[64:67], a[112:115]\n" \
"v_add_u32 v18, 39936, v18\n" \
"ds_read_b128 v[52:55], v18 offset:0\n" \
"v_mfma_f32_16x16x32_bf16 a[152:155], v[104:107], v[68:71], a[152:155]\n" \
"v_add_u32 v8, 39936, v8\n" \
"v_add_u32 v2, 39936, v2\n" \
"v_mfma_f32_16x16x32_bf16 a[192:195], v[104:107], v[72:75], a[192:195]\n" \
"ds_read_b64_tr_b16 v[96:97], v2 offset:0\n" \
"ds_read_b64_tr_b16 v[98:99], v2 offset:1024\n" \
"v_mfma_f32_16x16x32_bf16 v[156:159], v[104:107], v[76:79], v[156:159]\n" \
"v_add_u32 v9, 39936, v9\n" \
"v_add_u32 v3, 39936, v3\n" \
"v_mfma_f32_16x16x32_bf16 v[196:199], v[104:107], v[80:83], v[196:199]\n" \
"ds_read_b64_tr_b16 v[100:101], v3 offset:0\n" \
"ds_read_b64_tr_b16 v[102:103], v3 offset:1024\n" \
"v_mfma_f32_16x16x32_bf16 v[236:239], v[104:107], v[84:87], v[236:239]\n" \
"v_mfma_f32_16x16x32_bf16 v[244:247], v[104:107], v[88:91], v[244:247]\n" \
"v_mfma_f32_16x16x32_bf16 a[36:39], v[108:111], v[56:59], a[36:39]\n" \
"v_mfma_f32_16x16x32_bf16 a[76:79], v[108:111], v[60:63], a[76:79]\n" \
"v_mfma_f32_16x16x32_bf16 a[116:119], v[108:111], v[64:67], a[116:119]\n" \
"v_mfma_f32_16x16x32_bf16 a[156:159], v[108:111], v[68:71], a[156:159]\n" \
"v_mfma_f32_16x16x32_bf16 v[120:123], v[108:111], v[72:75], v[120:123]\n" \
"v_mfma_f32_16x16x32_bf16 v[160:163], v[108:111], v[76:79], v[160:163]\n" \
"v_mfma_f32_16x16x32_bf16 v[200:203], v[108:111], v[80:83], v[200:203]\n" \
"v_mfma_f32_16x16x32_bf16 v[240:243], v[108:111], v[84:87], v[240:243]\n" \
"s_waitcnt lgkmcnt(9)\n" \
"v_mfma_f32_16x16x32_bf16 a[0:3], v[92:95], v[20:23], a[0:3]\n" \
"ds_read_b64_tr_b16 v[104:105], v4 offset:0\n" \
"v_mfma_f32_16x16x32_bf16 a[40:43], v[92:95], v[24:27], a[40:43]\n" \
"v_mfma_f32_16x16x32_bf16 a[80:83], v[92:95], v[28:31], a[80:83]\n" \
"ds_read_b64_tr_b16 v[106:107], v5 offset:0\n" \
"s_add_u32 m0, s49, 39936\n" \
"v_mfma_f32_16x16x32_bf16 a[120:123], v[92:95], v[32:35], a[120:123]\n" \
"global_load_lds_dwordx4 v10, s[40:41] offset:0\n" \
"s_waitcnt lgkmcnt(7)\n" \
"v_mfma_f32_16x16x32_bf16 a[160:163], v[92:95], v[36:39], a[160:163]\n" \
"v_mfma_f32_16x16x32_bf16 v[124:127], v[92:95], v[40:43], v[124:127]\n" \
"v_mfma_f32_16x16x32_bf16 v[164:167], v[92:95], v[44:47], v[164:167]\n" \
"v_mfma_f32_16x16x32_bf16 v[204:207], v[92:95], v[48:51], v[204:207]\n" \
"v_mfma_f32_16x16x32_bf16 a[216:219], a[196:199], v[92:95], a[216:219]\n" \
"s_waitcnt lgkmcnt(4)\n" \
"v_mfma_f32_16x16x32_bf16 a[4:7], v[96:99], v[20:23], a[4:7]\n" \
"ds_read_b64_tr_b16 v[108:109], v1 offset:8192\n" \
"global_load_lds_dwordx4 v11, s[40:41] offset:1024\n" \
"v_mfma_f32_16x16x32_bf16 a[44:47], v[96:99], v[24:27], a[44:47]\n" \
"v_mfma_f32_16x16x32_bf16 a[84:87], v[96:99], v[28:31], a[84:87]\n" \
"ds_read_b64_tr_b16 v[110:111], v1 offset:9216\n" \
"v_mfma_f32_16x16x32_bf16 a[124:127], v[96:99], v[32:35], a[124:127]\n" \
"v_mfma_f32_16x16x32_bf16 a[164:167], v[96:99], v[36:39], a[164:167]\n" \
"v_mfma_f32_16x16x32_bf16 v[128:131], v[96:99], v[40:43], v[128:131]\n" \
"global_load_lds_dwordx4 v12, s[40:41] offset:2048\n" \
"v_mfma_f32_16x16x32_bf16 v[168:171], v[96:99], v[44:47], v[168:171]\n" \
"v_mfma_f32_16x16x32_bf16 v[208:211], v[96:99], v[48:51], v[208:211]\n" \
"v_mfma_f32_16x16x32_bf16 a[216:219], a[200:203], v[96:99], a[216:219]\n" \
"s_waitcnt lgkmcnt(4)\n" \
"v_mfma_f32_16x16x32_bf16 a[8:11], v[100:103], v[20:23], a[8:11]\n" \
"ds_read_b64_tr_b16 v[92:93], v2 offset:8192\n" \
"v_mfma_f32_16x16x32_bf16 a[48:51], v[100:103], v[24:27], a[48:51]\n" \
"v_mfma_f32_16x16x32_bf16 a[88:91], v[100:103], v[28:31], a[88:91]\n" \
"ds_read_b64_tr_b16 v[94:95], v2 offset:9216\n" \
"global_load_lds_dwordx4 v13, s[40:41] offset:3072\n" \
"v_mfma_f32_16x16x32_bf16 a[128:131], v[100:103], v[32:35], a[128:131]\n" \
"s_add_u32 s40, s40, 64\n" \
"s_addc_u32 s41, s41, 0\n" \
"v_mfma_f32_16x16x32_bf16 a[168:171], v[100:103], v[36:39], a[168:171]\n" \
"v_mfma_f32_16x16x32_bf16 v[132:135], v[100:103], v[40:43], v[132:135]\n" \
"v_mfma_f32_16x16x32_bf16 v[172:175], v[100:103], v[44:47], v[172:175]\n" \
"s_add_u32 m0, s50, 39936\n" \
"v_mfma_f32_16x16x32_bf16 v[212:215], v[100:103], v[48:51], v[212:215]\n" \
"global_load_lds_dwordx4 v14, s[42:43] offset:0\n" \
"v_mfma_f32_16x16x32_bf16 a[216:219], a[204:207], v[100:103], a[216:219]\n" \
"s_waitcnt lgkmcnt(4)\n" \
"v_mfma_f32_16x16x32_bf16 a[12:15], v[104:107], v[20:23], a[12:15]\n" \
"ds_read_b64_tr_b16 v[96:97], v3 offset:8192\n" \
"v_mfma_f32_16x16x32_bf16 a[52:55], v[104:107], v[24:27], a[52:55]\n" \
"v_mfma_f32_16x16x32_bf16 a[92:95], v[104:107], v[28:31], a[92:95]\n" \
"ds_read_b64_tr_b16 v[98:99], v3 offset:9216\n" \
"v_mfma_f32_16x16x32_bf16 a[132:135], v[104:107], v[32:35], a[132:135]\n" \
"v_mfma_f32_16x16x32_bf16 a[172:175], v[104:107], v[36:39], a[172:175]\n" \
"global_load_lds_dwordx4 v15, s[42:43] offset:1024\n" \
"v_mfma_f32_16x16x32_bf16 v[136:139], v[104:107], v[40:43], v[136:139]\n" \
"s_add_u32 s42, s42, 229376\n" \
"s_addc_u32 s43, s43, 0\n" \
"v_mfma_f32_16x16x32_bf16 v[176:179], v[104:107], v[44:47], v[176:179]\n" \
"v_mfma_f32_16x16x32_bf16 v[216:219], v[104:107], v[48:51], v[216:219]\n" \
"v_mfma_f32_16x16x32_bf16 a[216:219], a[208:211], v[104:107], a[216:219]\n" \
"s_waitcnt lgkmcnt(4)\n" \
"v_mfma_f32_16x16x32_bf16 a[16:19], v[108:111], v[20:23], a[16:19]\n" \
"ds_read_b64_tr_b16 v[100:101], v6 offset:0\n" \
"s_add_u32 m0, s51, 39936\n" \
"v_mfma_f32_16x16x32_bf16 a[56:59], v[108:111], v[24:27], a[56:59]\n" \
"global_load_lds_dwordx4 v14, s[44:45] offset:0\n" \
"v_mfma_f32_16x16x32_bf16 a[96:99], v[108:111], v[28:31], a[96:99]\n" \
"ds_read_b64_tr_b16 v[102:103], v7 offset:0\n" \
"v_mfma_f32_16x16x32_bf16 a[136:139], v[108:111], v[32:35], a[136:139]\n" \
"v_mfma_f32_16x16x32_bf16 a[176:179], v[108:111], v[36:39], a[176:179]\n" \
"v_mfma_f32_16x16x32_bf16 v[140:143], v[108:111], v[40:43], v[140:143]\n" \
"v_mfma_f32_16x16x32_bf16 v[180:183], v[108:111], v[44:47], v[180:183]\n" \
"global_load_lds_dwordx4 v15, s[44:45] offset:1024\n" \
"v_mfma_f32_16x16x32_bf16 v[220:223], v[108:111], v[48:51], v[220:223]\n" \
"s_add_u32 s44, s44, 229376\n" \
"s_addc_u32 s45, s45, 0\n" \
"v_mfma_f32_16x16x32_bf16 a[216:219], a[212:215], v[108:111], a[216:219]\n" \
"s_waitcnt lgkmcnt(4)\n" \
"v_mfma_f32_16x16x32_bf16 a[20:23], v[92:95], v[20:23], a[20:23]\n" \
"ds_read_b64_tr_b16 v[104:105], v8 offset:0\n" \
"v_mfma_f32_16x16x32_bf16 a[60:63], v[92:95], v[24:27], a[60:63]\n" \
"v_mfma_f32_16x16x32_bf16 a[100:103], v[92:95], v[28:31], a[100:103]\n" \
"ds_read_b64_tr_b16 v[106:107], v8 offset:768\n" \
"s_add_u32 m0, s52, 39936\n" \
"v_mfma_f32_16x16x32_bf16 a[140:143], v[92:95], v[32:35], a[140:143]\n" \
"global_load_lds_dwordx4 v16, s[46:47]\n" \
"v_mfma_f32_16x16x32_bf16 a[180:183], v[92:95], v[36:39], a[180:183]\n" \
"v_mfma_f32_16x16x32_bf16 v[144:147], v[92:95], v[40:43], v[144:147]\n" \
"v_mfma_f32_16x16x32_bf16 v[184:187], v[92:95], v[44:47], v[184:187]\n" \
"v_mfma_f32_16x16x32_bf16 v[224:227], v[92:95], v[48:51], v[224:227]\n" \
"s_waitcnt lgkmcnt(4)\n" \
"v_mfma_f32_16x16x32_bf16 a[24:27], v[96:99], v[20:23], a[24:27]\n" \
"ds_read_b64_tr_b16 v[108:109], v9 offset:0\n" \
"v_mfma_f32_16x16x32_bf16 a[64:67], v[96:99], v[24:27], a[64:67]\n" \
"s_cmp_eq_u32 s53, 0\n" \
"s_cbranch_scc1 LZ%=_62\n" \
"global_load_lds_dwordx4 v17, s[46:47] offset:1024\n" \
"LZ%=_62:\n" \
"v_mfma_f32_16x16x32_bf16 a[104:107], v[96:99], v[28:31], a[104:107]\n" \
"s_add_u32 s46, s46, s48\n" \
"s_addc_u32 s47, s47, 0\n" \
"v_mfma_f32_16x16x32_bf16 a[144:147], v[96:99], v[32:35], a[144:147]\n" \
"ds_read_b64_tr_b16 v[110:111], v9 offset:768\n" \
"v_mfma_f32_16x16x32_bf16 a[184:187], v[96:99], v[36:39], a[184:187]\n" \
"v_mfma_f32_16x16x32_bf16 v[148:151], v[96:99], v[40:43], v[148:151]\n" \
"v_mfma_f32_16x16x32_bf16 v[188:191], v[96:99], v[44:47], v[188:191]\n" \
"v_mfma_f32_16x16x32_bf16 v[228:231], v[96:99], v[48:51], v[228:231]\n" \
"s_waitcnt lgkmcnt(0)\n" \
"s_waitcnt vmcnt(18)\n" \
"s_barrier\n" \
"v_mfma_f32_16x16x32_bf16 a[28:31], v[100:103], v[20:23], a[28:31]\n" \
"v_add_u32 v1, 39936, v1\n" \
"ds_read_b64_tr_b16 v[92:93], v1 offset:0\n" \
"v_mfma_f32_16x16x32_bf16 a[68:71], v[100:103], v[24:27], a[68:71]\n" \
"ds_read_b64_tr_b16 v[94:95], v1 offset:1024\n" \
"v_mfma_f32_16x16x32_bf16 a[108:111], v[100:103], v[28:31], a[108:111]\n" \
"v_add_u32 v0, 39936, v0\n" \
"ds_read_b128 v[56:59], v0 offset:1024\n" \
"v_mfma_f32_16x16x32_bf16 a[148:151], v[100:103], v[32:35], a[148:151]\n" \
"v_add_u32 v4, 39936, v4\n" \
"ds_read_b128 v[60:63], v0 offset:2048\n" \
"v_mfma_f32_16x16x32_bf16 a[188:191], v[100:103], v[36:39], a[188:191]\n" \
"s_add_u32 s88, s78, 16384\n" \
"ds_read_b128 v[64:67], v0 offset:3072\n" \
"v_mfma_f32_16x16x32_bf16 v[152:155], v[100:103], v[40:43], v[152:155]\n" \
"v_add_u32 v5, 39936, v5\n" \
"ds_read_b128 v[68:71], v0 offset:4096\n" \
"v_mfma_f32_16x16x32_bf16 v[192:195], v[100:103], v[44:47], v[192:195]\n" \
"s_cmp_eq_u32 s87, 0\n" \
"s_cselect_b32 s88, s78, s88\n" \
"v_mfma_f32_16x16x32_bf16 v[232:235], v[100:103], v[48:51], v[232:235]\n" \
"ds_read_b128 v[72:75], v0 offset:5120\n" \
"v_mfma_f32_16x16x32_bf16 a[32:35], v[104:107], v[20:23], a[32:35]\n" \
"v_add_u32 v6, 39936, v6\n" \
"ds_read_b128 v[76:79], v0 offset:6144\n" \
"v_mfma_f32_16x16x32_bf16 a[72:75], v[104:107], v[24:27], a[72:75]\n" \
"s_bitcmp1_b32 s54, s87\n" \
"s_cselect_b32 s88, s88, s92\n" \
"v_mfma_f32_16x16x32_bf16 a[112:115], v[104:107], v[28:31], a[112:115]\n" \
"ds_read_b128 v[80:83], v0 offset:7168\n" \
"v_add_u32 v7, 39936, v7\n" \
"v_mfma_f32_16x16x32_bf16 a[152:155], v[104:107], v[32:35], a[152:155]\n" \
"ds_read_b128 v[84:87], v0 offset:8192\n" \
"v_mov_b32 v116, s88\n" \
"v_mfma_f32_16x16x32_bf16 a[192:195], v[104:107], v[36:39], a[192:195]\n" \
"v_add_u32 v18, 39936, v18\n" \
"ds_read_b128 v[88:91], v18 offset:0\n" \
"v_mfma_f32_16x16x32_bf16 v[156:159], v[104:107], v[40:43], v[156:159]\n" \
"v_add_u32 v8, 39936, v8\n" \
"v_add_u32 v2, 39936, v2\n" \
"v_mfma_f32_16x16x32_bf16 v[196:199], v[104:107], v[44:47], v[196:199]\n" \
"ds_read_b64_tr_b16 v[96:97], v2 offset:0\n" \
"v_mov_b32 v118, s84\n" \
"v_mfma_f32_16x16x32_bf16 v[236:239], v[104:107], v[48:51], v[236:239]\n" \
"ds_read_b64_tr_b16 v[98:99], v2 offset:1024\n" \
"v_add_u32 v9, 39936, v9\n" \
"v_mfma_f32_16x16x32_bf16 v[244:247], v[104:107], v[52:55], v[244:247]\n" \
"v_add_u32 v3, 39936, v3\n" \
"v_mfma_f32_16x16x32_bf16 a[36:39], v[108:111], v[20:23], a[36:39]\n" \
"ds_read_b64_tr_b16 v[100:101], v3 offset:0\n" \
"v_mov_b32 v119, s85\n" \
"v_mfma_f32_16x16x32_bf16 a[76:79], v[108:111], v[24:27], a[76:79]\n" \
"ds_read_b64_tr_b16 v[102:103], v3 offset:1024\n" \
"global_store_dwordx2 v116, v[118:119], s[74:75] sc1\n" \
"v_mfma_f32_16x16x32_bf16 a[116:119], v[108:111], v[28:31], a[116:119]\n" \
"v_mfma_f32_16x16x32_bf16 a[156:159], v[108:111], v[32:35], a[156:159]\n" \
"v_mfma_f32_16x16x32_bf16 v[120:123], v[108:111], v[36:39], v[120:123]\n" \
"v_mfma_f32_16x16x32_bf16 v[160:163], v[108:111], v[40:43], v[160:163]\n" \
"v_mfma_f32_16x16x32_bf16 v[200:203], v[108:111], v[44:47], v[200:203]\n" \
"v_mfma_f32_16x16x32_bf16 v[240:243], v[108:111], v[48:51], v[240:243]\n" \
"s_waitcnt lgkmcnt(9)\n" \
"v_mfma_f32_16x16x32_bf16 a[0:3], v[92:95], v[56:59], a[0:3]\n" \
"ds_read_b64_tr_b16 v[104:105], v4 offset:0\n" \
"v_mfma_f32_16x16x32_bf16 a[40:43], v[92:95], v[60:63], a[40:43]\n" \
"v_mfma_f32_16x16x32_bf16 a[80:83], v[92:95], v[64:67], a[80:83]\n" \
"ds_read_b64_tr_b16 v[106:107], v5 offset:0\n" \
"s_add_u32 m0, s49, 79872\n" \
"v_mfma_f32_16x16x32_bf16 a[120:123], v[92:95], v[68:71], a[120:123]\n" \
"global_load_lds_dwordx4 v10, s[40:41] offset:0\n" \
"s_waitcnt lgkmcnt(7)\n" \
"v_mfma_f32_16x16x32_bf16 a[160:163], v[92:95], v[72:75], a[160:163]\n" \
"v_mfma_f32_16x16x32_bf16 v[124:127], v[92:95], v[76:79], v[124:127]\n" \
"v_mfma_f32_16x16x32_bf16 v[164:167], v[92:95], v[80:83], v[164:167]\n" \
"v_mfma_f32_16x16x32_bf16 v[204:207], v[92:95], v[84:87], v[204:207]\n" \
"v_mfma_f32_16x16x32_bf16 a[216:219], a[196:199], v[92:95], a[216:219]\n" \
"s_waitcnt lgkmcnt(4)\n" \
"v_mfma_f32_16x16x32_bf16 a[4:7], v[96:99], v[56:59], a[4:7]\n" \
"ds_read_b64_tr_b16 v[108:109], v1 offset:8192\n" \
"global_load_lds_dwordx4 v11, s[40:41] offset:1024\n" \
"v_mfma_f32_16x16x32_bf16 a[44:47], v[96:99], v[60:63], a[44:47]\n" \
"v_mfma_f32_16x16x32_bf16 a[84:87], v[96:99], v[64:67], a[84:87]\n" \
"ds_read_b64_tr_b16 v[110:111], v1 offset:9216\n" \
"v_mfma_f32_16x16x32_bf16 a[124:127], v[96:99], v[68:71], a[124:127]\n" \
"v_mfma_f32_16x16x32_bf16 a[164:167], v[96:99], v[72:75], a[164:167]\n" \
"v_mfma_f32_16x16x32_bf16 v[128:131], v[96:99], v[76:79], v[128:131]\n" \
"global_load_lds_dwordx4 v12, s[40:41] offset:2048\n" \
"v_mfma_f32_16x16x32_bf16 v[168:171], v[96:99], v[80:83], v[168:171]\n" \
"v_mfma_f32_16x16x32_bf16 v[208:211], v[96:99], v[84:87], v[208:211]\n" \
"v_mfma_f32_16x16x32_bf16 a[216:219], a[200:203], v[96:99], a[216:219]\n" \
"s_waitcnt lgkmcnt(4)\n" \
"v_mfma_f32_16x16x32_bf16 a[8:11], v[100:103], v[56:59], a[8:11]\n" \
"ds_read_b64_tr_b16 v[92:93], v2 offset:8192\n" \
"v_mfma_f32_16x16x32_bf16 a[48:51], v[100:103], v[60:63], a[48:51]\n" \
"v_mfma_f32_16x16x32_bf16 a[88:91], v[100:103], v[64:67], a[88:91]\n" \
"ds_read_b64_tr_b16 v[94:95], v2 offset:9216\n" \
"global_load_lds_dwordx4 v13, s[40:41] offset:3072\n" \
"v_mfma_f32_16x16x32_bf16 a[128:131], v[100:103], v[68:71], a[128:131]\n" \
"s_add_u32 s40, s40, 64\n" \
"s_addc_u32 s41, s41, 0\n" \
"v_mfma_f32_16x16x32_bf16 a[168:171], v[100:103], v[72:75], a[168:171]\n" \
"v_mfma_f32_16x16x32_bf16 v[132:135], v[100:103], v[76:79], v[132:135]\n" \
"v_mfma_f32_16x16x32_bf16 v[172:175], v[100:103], v[80:83], v[172:175]\n" \
"s_add_u32 m0, s50, 79872\n" \
"v_mfma_f32_16x16x32_bf16 v[212:215], v[100:103], v[84:87], v[212:215]\n" \
"global_load_lds_dwordx4 v14, s[42:43] offset:0\n" \
"v_mfma_f32_16x16x32_bf16 a[216:219], a[204:207], v[100:103], a[216:219]\n" \
"s_waitcnt lgkmcnt(4)\n" \
"v_mfma_f32_16x16x32_bf16 a[12:15], v[104:107], v[56:59], a[12:15]\n" \
"ds_read_b64_tr_b16 v[96:97], v3 offset:8192\n" \
"v_mfma_f32_16x16x32_bf16 a[52:55], v[104:107], v[60:63], a[52:55]\n" \
"v_mfma_f32_16x16x32_bf16 a[92:95], v[104:107], v[64:67], a[92:95]\n" \
"ds_read_b64_tr_b16 v[98:99], v3 offset:9216\n" \
"v_mfma_f32_16x16x32_bf16 a[132:135], v[104:107], v[68:71], a[132:135]\n" \
"v_mfma_f32_16x16x32_bf16 a[172:175], v[104:107], v[72:75], a[172:175]\n" \
"global_load_lds_dwordx4 v15, s[42:43] offset:1024\n" \
"v_mfma_f32_16x16x32_bf16 v[136:139], v[104:107], v[76:79], v[136:139]\n" \
"s_add_u32 s42, s42, 229376\n" \
"s_addc_u32 s43, s43, 0\n" \
"v_mfma_f32_16x16x32_bf16 v[176:179], v[104:107], v[80:83], v[176:179]\n" \
"v_mfma_f32_16x16x32_bf16 v[216:219], v[104:107], v[84:87], v[216:219]\n" \
"v_mfma_f32_16x16x32_bf16 a[216:219], a[208:211], v[104:107], a[216:219]\n" \
"s_waitcnt lgkmcnt(4)\n" \
"v_mfma_f32_16x16x32_bf16 a[16:19], v[108:111], v[56:59], a[16:19]\n" \
"ds_read_b64_tr_b16 v[100:101], v6 offset:0\n" \
"s_add_u32 m0, s51, 79872\n" \
"v_mfma_f32_16x16x32_bf16 a[56:59], v[108:111], v[60:63], a[56:59]\n" \
"global_load_lds_dwordx4 v14, s[44:45] offset:0\n" \
"v_mfma_f32_16x16x32_bf16 a[96:99], v[108:111], v[64:67], a[96:99]\n" \
"ds_read_b64_tr_b16 v[102:103], v7 offset:0\n" \
"v_mfma_f32_16x16x32_bf16 a[136:139], v[108:111], v[68:71], a[136:139]\n" \
"v_mfma_f32_16x16x32_bf16 a[176:179], v[108:111], v[72:75], a[176:179]\n" \
"v_mfma_f32_16x16x32_bf16 v[140:143], v[108:111], v[76:79], v[140:143]\n" \
"v_mfma_f32_16x16x32_bf16 v[180:183], v[108:111], v[80:83], v[180:183]\n" \
"global_load_lds_dwordx4 v15, s[44:45] offset:1024\n" \
"v_mfma_f32_16x16x32_bf16 v[220:223], v[108:111], v[84:87], v[220:223]\n" \
"s_add_u32 s44, s44, 229376\n" \
"s_addc_u32 s45, s45, 0\n" \
"v_mfma_f32_16x16x32_bf16 a[216:219], a[212:215], v[108:111], a[216:219]\n" \
"s_waitcnt lgkmcnt(4)\n" \
"v_mfma_f32_16x16x32_bf16 a[20:23], v[92:95], v[56:59], a[20:23]\n" \
"ds_read_b64_tr_b16 v[104:105], v8 offset:0\n" \
"v_mfma_f32_16x16x32_bf16 a[60:63], v[92:95], v[60:63], a[60:63]\n" \
"v_mfma_f32_16x16x32_bf16 a[100:103], v[92:95], v[64:67], a[100:103]\n" \
"ds_read_b64_tr_b16 v[106:107], v8 offset:768\n" \
"s_add_u32 m0, s52, 79872\n" \
"v_mfma_f32_16x16x32_bf16 a[140:143], v[92:95], v[68:71], a[140:143]\n" \
"global_load_lds_dwordx4 v16, s[46:47]\n" \
"v_mfma_f32_16x16x32_bf16 a[180:183], v[92:95], v[72:75], a[180:183]\n" \
"v_mfma_f32_16x16x32_bf16 v[144:147], v[92:95], v[76:79], v[144:147]\n" \
"v_mfma_f32_16x16x32_bf16 v[184:187], v[92:95], v[80:83], v[184:187]\n" \
"v_mfma_f32_16x16x32_bf16 v[224:227], v[92:95], v[84:87], v[224:227]\n" \
"s_waitcnt lgkmcnt(4)\n" \
"v_mfma_f32_16x16x32_bf16 a[24:27], v[96:99], v[56:59], a[24:27]\n" \
"ds_read_b64_tr_b16 v[108:109], v9 offset:0\n" \
"v_mfma_f32_16x16x32_bf16 a[64:67], v[96:99], v[60:63], a[64:67]\n" \
"s_cmp_eq_u32 s53, 0\n" \
"s_cbranch_scc1 LZ%=_72\n" \
"global_load_lds_dwordx4 v17, s[46:47] offset:1024\n" \
"LZ%=_72:\n" \
"v_mfma_f32_16x16x32_bf16 a[104:107], v[96:99], v[64:67], a[104:107]\n" \
"s_add_u32 s46, s46, s48\n" \
"s_addc_u32 s47, s47, 0\n" \
"v_mfma_f32_16x16x32_bf16 a[144:147], v[96:99], v[68:71], a[144:147]\n" \
"ds_read_b64_tr_b16 v[110:111], v9 offset:768\n" \
"v_mfma_f32_16x16x32_bf16 a[184:187], v[96:99], v[72:75], a[184:187]\n" \
"v_mfma_f32_16x16x32_bf16 v[148:151], v[96:99], v[76:79], v[148:151]\n" \
"v_mfma_f32_16x16x32_bf16 v[188:191], v[96:99], v[80:83], v[188:191]\n" \
"v_mfma_f32_16x16x32_bf16 v[228:231], v[96:99], v[84:87], v[228:231]\n" \
"s_waitcnt lgkmcnt(0)\n" \
"s_waitcnt vmcnt(18)\n" \
"s_barrier\n" \
"v_mfma_f32_16x16x32_bf16 a[28:31], v[100:103], v[56:59], a[28:31]\n" \
"v_add_u32 v1, 4294847488, v1\n" \
"ds_read_b64_tr_b16 v[92:93], v1 offset:0\n" \
"v_mfma_f32_16x16x32_bf16 a[68:71], v[100:103], v[60:63], a[68:71]\n" \
"ds_read_b64_tr_b16 v[94:95], v1 offset:1024\n" \
"v_mfma_f32_16x16x32_bf16 a[108:111], v[100:103], v[64:67], a[108:111]\n" \
"v_add_u32 v0, 4294847488, v0\n" \
"ds_read_b128 v[20:23], v0 offset:1024\n" \
"v_mfma_f32_16x16x32_bf16 a[148:151], v[100:103], v[68:71], a[148:151]\n" \
"v_add_u32 v4, 4294847488, v4\n" \
"ds_read_b128 v[24:27], v0 offset:2048\n" \
"v_mfma_f32_16x16x32_bf16 a[188:191], v[100:103], v[72:75], a[188:191]\n" \
"ds_read_b128 v[28:31], v0 offset:3072\n" \
"v_add_u32 v5, 4294847488, v5\n" \
"v_mfma_f32_16x16x32_bf16 v[152:155], v[100:103], v[76:79], v[152:155]\n" \
"ds_read_b128 v[32:35], v0 offset:4096\n" \
"v_mfma_f32_16x16x32_bf16 v[192:195], v[100:103], v[80:83], v[192:195]\n" \
"ds_read_b128 v[36:39], v0 offset:5120\n" \
"v_add_u32 v6, 4294847488, v6\n" \
"v_mfma_f32_16x16x32_bf16 v[232:235], v[100:103], v[84:87], v[232:235]\n" \
"ds_read_b128 v[40:43], v0 offset:6144\n" \
"v_mfma_f32_16x16x32_bf16 a[32:35], v[104:107], v[56:59], a[32:35]\n" \
"ds_read_b128 v[44:47], v0 offset:7168\n" \
"v_add_u32 v7, 4294847488, v7\n" \
"v_mfma_f32_16x16x32_bf16 a[72:75], v[104:107], v[60:63], a[72:75]\n" \
"ds_read_b128 v[48:51], v0 offset:8192\n" \
"v_mfma_f32_16x16x32_bf16 a[112:115], v[104:107], v[64:67], a[112:115]\n" \
"v_add_u32 v18, 4294847488, v18\n" \
"ds_read_b128 v[52:55], v18 offset:0\n" \
"v_mfma_f32_16x16x32_bf16 a[152:155], v[104:107], v[68:71], a[152:155]\n" \
"v_add_u32 v8, 4294847488, v8\n" \
"v_add_u32 v2, 4294847488, v2\n" \
"v_mfma_f32_16x16x32_bf16 a[192:195], v[104:107], v[72:75], a[192:195]\n" \
"ds_read_b64_tr_b16 v[96:97], v2 offset:0\n" \
"ds_read_b64_tr_b16 v[98:99], v2 offset:1024\n" \
"v_mfma_f32_16x16x32_bf16 v[156:159], v[104:107], v[76:79], v[156:159]\n" \
"v_add_u32 v9, 4294847488, v9\n" \
"v_add_u32 v3, 4294847488, v3\n" \
"v_mfma_f32_16x16x32_bf16 v[196:199], v[104:107], v[80:83], v[196:199]\n" \
"ds_read_b64_tr_b16 v[100:101], v3 offset:0\n" \
"ds_read_b64_tr_b16 v[102:103], v3 offset:1024\n" \
"v_mfma_f32_16x16x32_bf16 v[236:239], v[104:107], v[84:87], v[236:239]\n" \
"v_mfma_f32_16x16x32_bf16 v[244:247], v[104:107], v[88:91], v[244:247]\n" \
"v_mfma_f32_16x16x32_bf16 a[36:39], v[108:111], v[56:59], a[36:39]\n" \
"v_mfma_f32_16x16x32_bf16 a[76:79], v[108:111], v[60:63], a[76:79]\n" \
"v_mfma_f32_16x16x32_bf16 a[116:119], v[108:111], v[64:67], a[116:119]\n" \
"v_mfma_f32_16x16x32_bf16 a[156:159], v[108:111], v[68:71], a[156:159]\n" \
"v_mfma_f32_16x16x32_bf16 v[120:123], v[108:111], v[72:75], v[120:123]\n" \
"v_mfma_f32_16x16x32_bf16 v[160:163], v[108:111], v[76:79], v[160:163]\n" \
"v_mfma_f32_16x16x32_bf16 v[200:203], v[108:111], v[80:83], v[200:203]\n" \
"v_mfma_f32_16x16x32_bf16 v[240:243], v[108:111], v[84:87], v[240:243]\n" \
"LKE%=:\n" \
"s_mov_b32 s87, 1\n" \
"LK1%=:\n" \
"s_waitcnt lgkmcnt(9)\n" \
"v_mfma_f32_16x16x32_bf16 a[0:3], v[92:95], v[20:23], a[0:3]\n" \
"ds_read_b64_tr_b16 v[104:105], v4 offset:0\n" \
"v_mfma_f32_16x16x32_bf16 a[40:43], v[92:95], v[24:27], a[40:43]\n" \
"s_add_u32 s88, s71, 1\n" \
"s_cmp_eq_u32 s87, s88\n" \
"s_cselect_b32 s88, s82, s92\n" \
"v_mfma_f32_16x16x32_bf16 a[80:83], v[92:95], v[28:31], a[80:83]\n" \
"v_mbcnt_lo_u32_b32 v112, -1, 0\n" \
"v_mbcnt_hi_u32_b32 v112, -1, v112\n" \
"v_mfma_f32_16x16x32_bf16 a[120:123], v[92:95], v[32:35], a[120:123]\n" \
"ds_read_b64_tr_b16 v[106:107], v5 offset:0\n" \
"s_add_u32 m0, s49, 119808\n" \
"s_waitcnt lgkmcnt(7)\n" \
"v_mfma_f32_16x16x32_bf16 a[160:163], v[92:95], v[36:39], a[160:163]\n" \
"v_lshlrev_b32 v112, 4, v112\n" \
"global_load_lds_dwordx4 v10, s[40:41] offset:0\n" \
"v_mfma_f32_16x16x32_bf16 v[124:127], v[92:95], v[40:43], v[124:127]\n" \
"v_add_u32 v112, s88, v112\n" \
"v_mfma_f32_16x16x32_bf16 v[164:167], v[92:95], v[44:47], v[164:167]\n" \
"s_mov_b32 exec_hi, 0\n" \
"global_store_dwordx4 v112, a[216:219], s[74:75] sc1\n" \
"s_mov_b32 exec_hi, -1\n" \
"v_mfma_f32_16x16x32_bf16 v[204:207], v[92:95], v[48:51], v[204:207]\n" \
"s_waitcnt lgkmcnt(4)\n" \
"v_mfma_f32_16x16x32_bf16 a[4:7], v[96:99], v[20:23], a[4:7]\n" \
"ds_read_b64_tr_b16 v[108:109], v1 offset:8192\n" \
"global_load_lds_dwordx4 v11, s[40:41] offset:1024\n" \
"v_mfma_f32_16x16x32_bf16 a[44:47], v[96:99], v[24:27], a[44:47]\n" \
"v_mfma_f32_16x16x32_bf16 a[84:87], v[96:99], v[28:31], a[84:87]\n" \
"ds_read_b64_tr_b16 v[110:111], v1 offset:9216\n" \
"v_mfma_f32_16x16x32_bf16 a[124:127], v[96:99], v[32:35], a[124:127]\n" \
"v_mfma_f32_16x16x32_bf16 a[164:167], v[96:99], v[36:39], a[164:167]\n" \
"v_mfma_f32_16x16x32_bf16 v[128:131], v[96:99], v[40:43], v[128:131]\n" \
"global_load_lds_dwordx4 v12, s[40:41] offset:2048\n" \
"v_mfma_f32_16x16x32_bf16 v[168:171], v[96:99], v[44:47], v[168:171]\n" \
"v_mfma_f32_16x16x32_bf16 v[208:211], v[96:99], v[48:51], v[208:211]\n" \
"s_waitcnt lgkmcnt(4)\n" \
"v_mfma_f32_16x16x32_bf16 a[8:11], v[100:103], v[20:23], a[8:11]\n" \
"ds_read_b64_tr_b16 v[92:93], v2 offset:8192\n" \
"v_mfma_f32_16x16x32_bf16 a[48:51], v[100:103], v[24:27], a[48:51]\n" \
"v_mfma_f32_16x16x32_bf16 a[88:91], v[100:103], v[28:31], a[88:91]\n" \
"ds_read_b64_tr_b16 v[94:95], v2 offset:9216\n" \
"global_load_lds_dwordx4 v13, s[40:41] offset:3072\n" \
"v_mfma_f32_16x16x32_bf16 a[128:131], v[100:103], v[32:35], a[128:131]\n" \
"s_add_u32 s40, s40, 64\n" \
"s_addc_u32 s41, s41, 0\n" \
"v_mfma_f32_16x16x32_bf16 a[168:171], v[100:103], v[36:39], a[168:171]\n" \
"v_mfma_f32_16x16x32_bf16 v[132:135], v[100:103], v[40:43], v[132:135]\n" \
"v_mfma_f32_16x16x32_bf16 v[172:175], v[100:103], v[44:47], v[172:175]\n" \
"s_add_u32 m0, s50, 119808\n" \
"v_mfma_f32_16x16x32_bf16 v[212:215], v[100:103], v[48:51], v[212:215]\n" \
"global_load_lds_dwordx4 v14, s[42:43] offset:0\n" \
"s_waitcnt lgkmcnt(4)\n" \
"v_mfma_f32_16x16x32_bf16 a[12:15], v[104:107], v[20:23], a[12:15]\n" \
"ds_read_b64_tr_b16 v[96:97], v3 offset:8192\n" \
"v_mfma_f32_16x16x32_bf16 a[52:55], v[104:107], v[24:27], a[52:55]\n" \
"v_mfma_f32_16x16x32_bf16 a[92:95], v[104:107], v[28:31], a[92:95]\n" \
"ds_read_b64_tr_b16 v[98:99], v3 offset:9216\n" \
"v_mfma_f32_16x16x32_bf16 a[132:135], v[104:107], v[32:35], a[132:135]\n" \
"v_mfma_f32_16x16x32_bf16 a[172:175], v[104:107], v[36:39], a[172:175]\n" \
"global_load_lds_dwordx4 v15, s[42:43] offset:1024\n" \
"v_mfma_f32_16x16x32_bf16 v[136:139], v[104:107], v[40:43], v[136:139]\n" \
"s_add_u32 s42, s42, 229376\n" \
"s_addc_u32 s43, s43, 0\n" \
"v_mfma_f32_16x16x32_bf16 v[176:179], v[104:107], v[44:47], v[176:179]\n" \
"v_mfma_f32_16x16x32_bf16 v[216:219], v[104:107], v[48:51], v[216:219]\n" \
"s_waitcnt lgkmcnt(4)\n" \
"v_mfma_f32_16x16x32_bf16 a[16:19], v[108:111], v[20:23], a[16:19]\n" \
"ds_read_b64_tr_b16 v[100:101], v6 offset:0\n" \
"s_add_u32 m0, s51, 119808\n" \
"v_mfma_f32_16x16x32_bf16 a[56:59], v[108:111], v[24:27], a[56:59]\n" \
"global_load_lds_dwordx4 v14, s[44:45] offset:0\n" \
"v_mfma_f32_16x16x32_bf16 a[96:99], v[108:111], v[28:31], a[96:99]\n" \
"ds_read_b64_tr_b16 v[102:103], v7 offset:0\n" \
"v_mfma_f32_16x16x32_bf16 a[136:139], v[108:111], v[32:35], a[136:139]\n" \
"v_mfma_f32_16x16x32_bf16 a[176:179], v[108:111], v[36:39], a[176:179]\n" \
"v_mfma_f32_16x16x32_bf16 v[140:143], v[108:111], v[40:43], v[140:143]\n" \
"v_mfma_f32_16x16x32_bf16 v[180:183], v[108:111], v[44:47], v[180:183]\n" \
"global_load_lds_dwordx4 v15, s[44:45] offset:1024\n" \
"v_mfma_f32_16x16x32_bf16 v[220:223], v[108:111], v[48:51], v[220:223]\n" \
"s_add_u32 s44, s44, 229376\n" \
"s_addc_u32 s45, s45, 0\n" \
"s_waitcnt lgkmcnt(4)\n" \
"v_mfma_f32_16x16x32_bf16 a[20:23], v[92:95], v[20:23], a[20:23]\n" \
"ds_read_b64_tr_b16 v[104:105], v8 offset:0\n" \
"v_mfma_f32_16x16x32_bf16 a[60:63], v[92:95], v[24:27], a[60:63]\n" \
"v_mfma_f32_16x16x32_bf16 a[100:103], v[92:95], v[28:31], a[100:103]\n" \
"ds_read_b64_tr_b16 v[106:107], v8 offset:768\n" \
"s_add_u32 m0, s52, 119808\n" \
"v_mfma_f32_16x16x32_bf16 a[140:143], v[92:95], v[32:35], a[140:143]\n" \
"global_load_lds_dwordx4 v16, s[46:47]\n" \
"v_mfma_f32_16x16x32_bf16 a[180:183], v[92:95], v[36:39], a[180:183]\n" \
"v_mfma_f32_16x16x32_bf16 v[144:147], v[92:95], v[40:43], v[144:147]\n" \
"v_mfma_f32_16x16x32_bf16 v[184:187], v[92:95], v[44:47], v[184:187]\n" \
"v_mfma_f32_16x16x32_bf16 v[224:227], v[92:95], v[48:51], v[224:227]\n" \
"s_waitcnt lgkmcnt(4)\n" \
"v_mfma_f32_16x16x32_bf16 a[24:27], v[96:99], v[20:23], a[24:27]\n" \
"ds_read_b64_tr_b16 v[108:109], v9 offset:0\n" \
"v_mfma_f32_16x16x32_bf16 a[64:67], v[96:99], v[24:27], a[64:67]\n" \
"s_cmp_eq_u32 s53, 0\n" \
"s_cbranch_scc1 LZ%=_134\n" \
"global_load_lds_dwordx4 v17, s[46:47] offset:1024\n" \
"LZ%=_134:\n" \
"v_mfma_f32_16x16x32_bf16 a[104:107], v[96:99], v[28:31], a[104:107]\n" \
"s_add_u32 s46, s46, s48\n" \
"s_addc_u32 s47, s47, 0\n" \
"v_mfma_f32_16x16x32_bf16 a[144:147], v[96:99], v[32:35], a[144:147]\n" \
"ds_read_b64_tr_b16 v[110:111], v9 offset:768\n" \
"v_mfma_f32_16x16x32_bf16 a[184:187], v[96:99], v[36:39], a[184:187]\n" \
"v_mfma_f32_16x16x32_bf16 v[148:151], v[96:99], v[40:43], v[148:151]\n" \
"v_mfma_f32_16x16x32_bf16 v[188:191], v[96:99], v[44:47], v[188:191]\n" \
"v_mfma_f32_16x16x32_bf16 v[228:231], v[96:99], v[48:51], v[228:231]\n" \
"s_waitcnt lgkmcnt(0)\n" \
"s_waitcnt vmcnt(18)\n" \
"s_barrier\n" \
"v_mfma_f32_16x16x32_bf16 a[28:31], v[100:103], v[20:23], a[28:31]\n" \
"v_add_u32 v1, 39936, v1\n" \
"ds_read_b64_tr_b16 v[92:93], v1 offset:0\n" \
"v_mfma_f32_16x16x32_bf16 a[68:71], v[100:103], v[24:27], a[68:71]\n" \
"ds_read_b64_tr_b16 v[94:95], v1 offset:1024\n" \
"v_mfma_f32_16x16x32_bf16 a[108:111], v[100:103], v[28:31], a[108:111]\n" \
"v_add_u32 v0, 39936, v0\n" \
"ds_read_b128 v[56:59], v0 offset:1024\n" \
"v_mfma_f32_16x16x32_bf16 a[148:151], v[100:103], v[32:35], a[148:151]\n" \
"v_add_u32 v4, 39936, v4\n" \
"ds_read_b128 v[60:63], v0 offset:2048\n" \
"v_mfma_f32_16x16x32_bf16 a[188:191], v[100:103], v[36:39], a[188:191]\n" \
"ds_read_b128 v[64:67], v0 offset:3072\n" \
"v_add_u32 v5, 39936, v5\n" \
"v_mfma_f32_16x16x32_bf16 v[152:155], v[100:103], v[40:43], v[152:155]\n" \
"ds_read_b128 v[68:71], v0 offset:4096\n" \
"v_mfma_f32_16x16x32_bf16 v[192:195], v[100:103], v[44:47], v[192:195]\n" \
"ds_read_b128 v[72:75], v0 offset:5120\n" \
"v_add_u32 v6, 39936, v6\n" \
"v_mfma_f32_16x16x32_bf16 v[232:235], v[100:103], v[48:51], v[232:235]\n" \
"ds_read_b128 v[76:79], v0 offset:6144\n" \
"v_mfma_f32_16x16x32_bf16 a[32:35], v[104:107], v[20:23], a[32:35]\n" \
"ds_read_b128 v[80:83], v0 offset:7168\n" \
"v_add_u32 v7, 39936, v7\n" \
"v_mfma_f32_16x16x32_bf16 a[72:75], v[104:107], v[24:27], a[72:75]\n" \
"ds_read_b128 v[84:87], v0 offset:8192\n" \
"v_mfma_f32_16x16x32_bf16 a[112:115], v[104:107], v[28:31], a[112:115]\n" \
"v_add_u32 v18, 39936, v18\n" \
"ds_read_b128 v[88:91], v18 offset:0\n" \
"v_mfma_f32_16x16x32_bf16 a[152:155], v[104:107], v[32:35], a[152:155]\n" \
"v_add_u32 v8, 39936, v8\n" \
"v_add_u32 v2, 39936, v2\n" \
"v_mfma_f32_16x16x32_bf16 a[192:195], v[104:107], v[36:39], a[192:195]\n" \
"ds_read_b64_tr_b16 v[96:97], v2 offset:0\n" \
"ds_read_b64_tr_b16 v[98:99], v2 offset:1024\n" \
"v_mfma_f32_16x16x32_bf16 v[156:159], v[104:107], v[40:43], v[156:159]\n" \
"v_add_u32 v9, 39936, v9\n" \
"v_add_u32 v3, 39936, v3\n" \
"v_mfma_f32_16x16x32_bf16 v[196:199], v[104:107], v[44:47], v[196:199]\n" \
"ds_read_b64_tr_b16 v[100:101], v3 offset:0\n" \
"ds_read_b64_tr_b16 v[102:103], v3 offset:1024\n" \
"v_mfma_f32_16x16x32_bf16 v[236:239], v[104:107], v[48:51], v[236:239]\n" \
"v_mfma_f32_16x16x32_bf16 v[244:247], v[104:107], v[52:55], v[244:247]\n" \
"v_mfma_f32_16x16x32_bf16 a[36:39], v[108:111], v[20:23], a[36:39]\n" \
"v_mfma_f32_16x16x32_bf16 a[76:79], v[108:111], v[24:27], a[76:79]\n" \
"v_mfma_f32_16x16x32_bf16 a[116:119], v[108:111], v[28:31], a[116:119]\n" \
"v_mfma_f32_16x16x32_bf16 a[156:159], v[108:111], v[32:35], a[156:159]\n" \
"v_mfma_f32_16x16x32_bf16 v[120:123], v[108:111], v[36:39], v[120:123]\n" \
"v_mfma_f32_16x16x32_bf16 v[160:163], v[108:111], v[40:43], v[160:163]\n" \
"v_mfma_f32_16x16x32_bf16 v[200:203], v[108:111], v[44:47], v[200:203]\n" \
"v_mfma_f32_16x16x32_bf16 v[240:243], v[108:111], v[48:51], v[240:243]\n" \
"s_waitcnt lgkmcnt(9)\n" \
"v_mfma_f32_16x16x32_bf16 a[0:3], v[92:95], v[56:59], a[0:3]\n" \
"ds_read_b64_tr_b16 v[104:105], v4 offset:0\n" \
"v_mfma_f32_16x16x32_bf16 a[40:43], v[92:95], v[60:63], a[40:43]\n" \
"s_cmp_eq_u32 s87, s93\n" \
"s_cbranch_scc1 LWR%=_145\n" \
"LWS%=_145:\n" \
"v_mfma_f32_16x16x32_bf16 a[80:83], v[92:95], v[64:67], a[80:83]\n" \
"ds_read_b64_tr_b16 v[106:107], v5 offset:0\n" \
"s_add_u32 m0, s49, 0\n" \
"v_mfma_f32_16x16x32_bf16 a[120:123], v[92:95], v[68:71], a[120:123]\n" \
"global_load_lds_dwordx4 v10, s[40:41] offset:0\n" \
"s_waitcnt lgkmcnt(7)\n" \
"v_mfma_f32_16x16x32_bf16 a[160:163], v[92:95], v[72:75], a[160:163]\n" \
"v_mfma_f32_16x16x32_bf16 v[124:127], v[92:95], v[76:79], v[124:127]\n" \
"v_mfma_f32_16x16x32_bf16 v[164:167], v[92:95], v[80:83], v[164:167]\n" \
"v_mfma_f32_16x16x32_bf16 v[204:207], v[92:95], v[84:87], v[204:207]\n" \
"s_waitcnt lgkmcnt(4)\n" \
"v_mfma_f32_16x16x32_bf16 a[4:7], v[96:99], v[56:59], a[4:7]\n" \
"ds_read_b64_tr_b16 v[108:109], v1 offset:8192\n" \
"global_load_lds_dwordx4 v11, s[40:41] offset:1024\n" \
"v_mfma_f32_16x16x32_bf16 a[44:47], v[96:99], v[60:63], a[44:47]\n" \
"v_mfma_f32_16x16x32_bf16 a[84:87], v[96:99], v[64:67], a[84:87]\n" \
"ds_read_b64_tr_b16 v[110:111], v1 offset:9216\n" \
"v_mfma_f32_16x16x32_bf16 a[124:127], v[96:99], v[68:71], a[124:127]\n" \
"v_mfma_f32_16x16x32_bf16 a[164:167], v[96:99], v[72:75], a[164:167]\n" \
"v_mfma_f32_16x16x32_bf16 v[128:131], v[96:99], v[76:79], v[128:131]\n" \
"global_load_lds_dwordx4 v12, s[40:41] offset:2048\n" \
"v_mfma_f32_16x16x32_bf16 v[168:171], v[96:99], v[80:83], v[168:171]\n" \
"v_mfma_f32_16x16x32_bf16 v[208:211], v[96:99], v[84:87], v[208:211]\n" \
"s_waitcnt lgkmcnt(4)\n" \
"v_mfma_f32_16x16x32_bf16 a[8:11], v[100:103], v[56:59], a[8:11]\n" \
"ds_read_b64_tr_b16 v[92:93], v2 offset:8192\n" \
"v_mfma_f32_16x16x32_bf16 a[48:51], v[100:103], v[60:63], a[48:51]\n" \
"v_mfma_f32_16x16x32_bf16 a[88:91], v[100:103], v[64:67], a[88:91]\n" \
"ds_read_b64_tr_b16 v[94:95], v2 offset:9216\n" \
"global_load_lds_dwordx4 v13, s[40:41] offset:3072\n" \
"v_mfma_f32_16x16x32_bf16 a[128:131], v[100:103], v[68:71], a[128:131]\n" \
"s_add_u32 s40, s40, 64\n" \
"s_addc_u32 s41, s41, 0\n" \
"v_mfma_f32_16x16x32_bf16 a[168:171], v[100:103], v[72:75], a[168:171]\n" \
"v_mfma_f32_16x16x32_bf16 v[132:135], v[100:103], v[76:79], v[132:135]\n" \
"v_mfma_f32_16x16x32_bf16 v[172:175], v[100:103], v[80:83], v[172:175]\n" \
"s_add_u32 m0, s50, 0\n" \
"v_mfma_f32_16x16x32_bf16 v[212:215], v[100:103], v[84:87], v[212:215]\n" \
"global_load_lds_dwordx4 v14, s[42:43] offset:0\n" \
"s_waitcnt lgkmcnt(4)\n" \
"v_mfma_f32_16x16x32_bf16 a[12:15], v[104:107], v[56:59], a[12:15]\n" \
"ds_read_b64_tr_b16 v[96:97], v3 offset:8192\n" \
"v_mfma_f32_16x16x32_bf16 a[52:55], v[104:107], v[60:63], a[52:55]\n" \
"v_mfma_f32_16x16x32_bf16 a[92:95], v[104:107], v[64:67], a[92:95]\n" \
"ds_read_b64_tr_b16 v[98:99], v3 offset:9216\n" \
"v_mfma_f32_16x16x32_bf16 a[132:135], v[104:107], v[68:71], a[132:135]\n" \
"v_mfma_f32_16x16x32_bf16 a[172:175], v[104:107], v[72:75], a[172:175]\n" \
"global_load_lds_dwordx4 v15, s[42:43] offset:1024\n" \
"v_mfma_f32_16x16x32_bf16 v[136:139], v[104:107], v[76:79], v[136:139]\n" \
"s_add_u32 s42, s42, 229376\n" \
"s_addc_u32 s43, s43, 0\n" \
"v_mfma_f32_16x16x32_bf16 v[176:179], v[104:107], v[80:83], v[176:179]\n" \
"v_mfma_f32_16x16x32_bf16 v[216:219], v[104:107], v[84:87], v[216:219]\n" \
"s_waitcnt lgkmcnt(4)\n" \
"v_mfma_f32_16x16x32_bf16 a[16:19], v[108:111], v[56:59], a[16:19]\n" \
"ds_read_b64_tr_b16 v[100:101], v6 offset:0\n" \
"s_add_u32 m0, s51, 0\n" \
"v_mfma_f32_16x16x32_bf16 a[56:59], v[108:111], v[60:63], a[56:59]\n" \
"global_load_lds_dwordx4 v14, s[44:45] offset:0\n" \
"v_mfma_f32_16x16x32_bf16 a[96:99], v[108:111], v[64:67], a[96:99]\n" \
"ds_read_b64_tr_b16 v[102:103], v7 offset:0\n" \
"v_mfma_f32_16x16x32_bf16 a[136:139], v[108:111], v[68:71], a[136:139]\n" \
"v_mfma_f32_16x16x32_bf16 a[176:179], v[108:111], v[72:75], a[176:179]\n" \
"v_mfma_f32_16x16x32_bf16 v[140:143], v[108:111], v[76:79], v[140:143]\n" \
"v_mfma_f32_16x16x32_bf16 v[180:183], v[108:111], v[80:83], v[180:183]\n" \
"global_load_lds_dwordx4 v15, s[44:45] offset:1024\n" \
"v_mfma_f32_16x16x32_bf16 v[220:223], v[108:111], v[84:87], v[220:223]\n" \
"s_add_u32 s44, s44, 229376\n" \
"s_addc_u32 s45, s45, 0\n" \
"s_waitcnt lgkmcnt(4)\n" \
"v_mfma_f32_16x16x32_bf16 a[20:23], v[92:95], v[56:59], a[20:23]\n" \
"ds_read_b64_tr_b16 v[104:105], v8 offset:0\n" \
"v_mfma_f32_16x16x32_bf16 a[60:63], v[92:95], v[60:63], a[60:63]\n" \
"v_mfma_f32_16x16x32_bf16 a[100:103], v[92:95], v[64:67], a[100:103]\n" \
"ds_read_b64_tr_b16 v[106:107], v8 offset:768\n" \
"s_add_u32 m0, s52, 0\n" \
"v_mfma_f32_16x16x32_bf16 a[140:143], v[92:95], v[68:71], a[140:143]\n" \
"global_load_lds_dwordx4 v16, s[46:47]\n" \
"v_mfma_f32_16x16x32_bf16 a[180:183], v[92:95], v[72:75], a[180:183]\n" \
"v_mfma_f32_16x16x32_bf16 v[144:147], v[92:95], v[76:79], v[144:147]\n" \
"v_mfma_f32_16x16x32_bf16 v[184:187], v[92:95], v[80:83], v[184:187]\n" \
"v_mfma_f32_16x16x32_bf16 v[224:227], v[92:95], v[84:87], v[224:227]\n" \
"s_waitcnt lgkmcnt(4)\n" \
"v_mfma_f32_16x16x32_bf16 a[24:27], v[96:99], v[56:59], a[24:27]\n" \
"ds_read_b64_tr_b16 v[108:109], v9 offset:0\n" \
"v_mfma_f32_16x16x32_bf16 a[64:67], v[96:99], v[60:63], a[64:67]\n" \
"s_cmp_eq_u32 s53, 0\n" \
"s_cbranch_scc1 LZ%=_144\n" \
"global_load_lds_dwordx4 v17, s[46:47] offset:1024\n" \
"LZ%=_144:\n" \
"v_mfma_f32_16x16x32_bf16 a[104:107], v[96:99], v[64:67], a[104:107]\n" \
"s_add_u32 s46, s46, s48\n" \
"s_addc_u32 s47, s47, 0\n" \
"v_mfma_f32_16x16x32_bf16 a[144:147], v[96:99], v[68:71], a[144:147]\n" \
"ds_read_b64_tr_b16 v[110:111], v9 offset:768\n" \
"v_mfma_f32_16x16x32_bf16 a[184:187], v[96:99], v[72:75], a[184:187]\n" \
"v_mfma_f32_16x16x32_bf16 v[148:151], v[96:99], v[76:79], v[148:151]\n" \
"v_mfma_f32_16x16x32_bf16 v[188:191], v[96:99], v[80:83], v[188:191]\n" \
"v_mfma_f32_16x16x32_bf16 v[228:231], v[96:99], v[84:87], v[228:231]\n" \
"s_waitcnt lgkmcnt(0)\n" \
"s_waitcnt vmcnt(18)\n" \
"s_barrier\n" \
"v_mfma_f32_16x16x32_bf16 a[28:31], v[100:103], v[56:59], a[28:31]\n" \
"v_add_u32 v1, 39936, v1\n" \
"ds_read_b64_tr_b16 v[92:93], v1 offset:0\n" \
"v_mfma_f32_16x16x32_bf16 a[68:71], v[100:103], v[60:63], a[68:71]\n" \
"ds_read_b64_tr_b16 v[94:95], v1 offset:1024\n" \
"v_mfma_f32_16x16x32_bf16 a[108:111], v[100:103], v[64:67], a[108:111]\n" \
"v_add_u32 v0, 39936, v0\n" \
"ds_read_b128 v[20:23], v0 offset:1024\n" \
"v_mfma_f32_16x16x32_bf16 a[148:151], v[100:103], v[68:71], a[148:151]\n" \
"v_add_u32 v4, 39936, v4\n" \
"ds_read_b128 v[24:27], v0 offset:2048\n" \
"v_mfma_f32_16x16x32_bf16 a[188:191], v[100:103], v[72:75], a[188:191]\n" \
"ds_read_b128 v[28:31], v0 offset:3072\n" \
"v_add_u32 v5, 39936, v5\n" \
"v_mfma_f32_16x16x32_bf16 v[152:155], v[100:103], v[76:79], v[152:155]\n" \
"ds_read_b128 v[32:35], v0 offset:4096\n" \
"v_mfma_f32_16x16x32_bf16 v[192:195], v[100:103], v[80:83], v[192:195]\n" \
"ds_read_b128 v[36:39], v0 offset:5120\n" \
"v_add_u32 v6, 39936, v6\n" \
"v_mfma_f32_16x16x32_bf16 v[232:235], v[100:103], v[84:87], v[232:235]\n" \
"ds_read_b128 v[40:43], v0 offset:6144\n" \
"v_mfma_f32_16x16x32_bf16 a[32:35], v[104:107], v[56:59], a[32:35]\n" \
"ds_read_b128 v[44:47], v0 offset:7168\n" \
"v_add_u32 v7, 39936, v7\n" \
"v_mfma_f32_16x16x32_bf16 a[72:75], v[104:107], v[60:63], a[72:75]\n" \
"ds_read_b128 v[48:51], v0 offset:8192\n" \
"v_mfma_f32_16x16x32_bf16 a[112:115], v[104:107], v[64:67], a[112:115]\n" \
"v_add_u32 v18, 39936, v18\n" \
"ds_read_b128 v[52:55], v18 offset:0\n" \
"v_mfma_f32_16x16x32_bf16 a[152:155], v[104:107], v[68:71], a[152:155]\n" \
"v_add_u32 v8, 39936, v8\n" \
"v_add_u32 v2, 39936, v2\n" \
"v_mfma_f32_16x16x32_bf16 a[192:195], v[104:107], v[72:75], a[192:195]\n" \
"ds_read_b64_tr_b16 v[96:97], v2 offset:0\n" \
"ds_read_b64_tr_b16 v[98:99], v2 offset:1024\n" \
"v_mfma_f32_16x16x32_bf16 v[156:159], v[104:107], v[76:79], v[156:159]\n" \
"v_add_u32 v9, 39936, v9\n" \
"v_add_u32 v3, 39936, v3\n" \
"v_mfma_f32_16x16x32_bf16 v[196:199], v[104:107], v[80:83], v[196:199]\n" \
"ds_read_b64_tr_b16 v[100:101], v3 offset:0\n" \
"ds_read_b64_tr_b16 v[102:103], v3 offset:1024\n" \
"v_mfma_f32_16x16x32_bf16 v[236:239], v[104:107], v[84:87], v[236:239]\n" \
"v_mfma_f32_16x16x32_bf16 v[244:247], v[104:107], v[88:91], v[244:247]\n" \
"v_mfma_f32_16x16x32_bf16 a[36:39], v[108:111], v[56:59], a[36:39]\n" \
"v_mfma_f32_16x16x32_bf16 a[76:79], v[108:111], v[60:63], a[76:79]\n" \
"v_mfma_f32_16x16x32_bf16 a[116:119], v[108:111], v[64:67], a[116:119]\n" \
"v_mfma_f32_16x16x32_bf16 a[156:159], v[108:111], v[68:71], a[156:159]\n" \
"v_mfma_f32_16x16x32_bf16 v[120:123], v[108:111], v[72:75], v[120:123]\n" \
"v_mfma_f32_16x16x32_bf16 v[160:163], v[108:111], v[76:79], v[160:163]\n" \
"v_mfma_f32_16x16x32_bf16 v[200:203], v[108:111], v[80:83], v[200:203]\n" \
"v_mfma_f32_16x16x32_bf16 v[240:243], v[108:111], v[84:87], v[240:243]\n" \
"s_waitcnt lgkmcnt(9)\n" \
"v_mfma_f32_16x16x32_bf16 a[0:3], v[92:95], v[20:23], a[0:3]\n" \
"ds_read_b64_tr_b16 v[104:105], v4 offset:0\n" \
"v_mfma_f32_16x16x32_bf16 a[40:43], v[92:95], v[24:27], a[40:43]\n" \
"v_mfma_f32_16x16x32_bf16 a[80:83], v[92:95], v[28:31], a[80:83]\n" \
"ds_read_b64_tr_b16 v[106:107], v5 offset:0\n" \
"s_add_u32 m0, s49, 39936\n" \
"v_mfma_f32_16x16x32_bf16 a[120:123], v[92:95], v[32:35], a[120:123]\n" \
"global_load_lds_dwordx4 v10, s[40:41] offset:0\n" \
"s_waitcnt lgkmcnt(7)\n" \
"v_mfma_f32_16x16x32_bf16 a[160:163], v[92:95], v[36:39], a[160:163]\n" \
"v_mfma_f32_16x16x32_bf16 v[124:127], v[92:95], v[40:43], v[124:127]\n" \
"v_mfma_f32_16x16x32_bf16 v[164:167], v[92:95], v[44:47], v[164:167]\n" \
"v_mfma_f32_16x16x32_bf16 v[204:207], v[92:95], v[48:51], v[204:207]\n" \
"s_waitcnt lgkmcnt(4)\n" \
"v_mfma_f32_16x16x32_bf16 a[4:7], v[96:99], v[20:23], a[4:7]\n" \
"ds_read_b64_tr_b16 v[108:109], v1 offset:8192\n" \
"global_load_lds_dwordx4 v11, s[40:41] offset:1024\n" \
"v_mfma_f32_16x16x32_bf16 a[44:47], v[96:99], v[24:27], a[44:47]\n" \
"v_mfma_f32_16x16x32_bf16 a[84:87], v[96:99], v[28:31], a[84:87]\n" \
"ds_read_b64_tr_b16 v[110:111], v1 offset:9216\n" \
"v_mfma_f32_16x16x32_bf16 a[124:127], v[96:99], v[32:35], a[124:127]\n" \
"v_mfma_f32_16x16x32_bf16 a[164:167], v[96:99], v[36:39], a[164:167]\n" \
"v_mfma_f32_16x16x32_bf16 v[128:131], v[96:99], v[40:43], v[128:131]\n" \
"global_load_lds_dwordx4 v12, s[40:41] offset:2048\n" \
"v_mfma_f32_16x16x32_bf16 v[168:171], v[96:99], v[44:47], v[168:171]\n" \
"v_mfma_f32_16x16x32_bf16 v[208:211], v[96:99], v[48:51], v[208:211]\n" \
"s_waitcnt lgkmcnt(4)\n" \
"v_mfma_f32_16x16x32_bf16 a[8:11], v[100:103], v[20:23], a[8:11]\n" \
"ds_read_b64_tr_b16 v[92:93], v2 offset:8192\n" \
"v_mfma_f32_16x16x32_bf16 a[48:51], v[100:103], v[24:27], a[48:51]\n" \
"v_mfma_f32_16x16x32_bf16 a[88:91], v[100:103], v[28:31], a[88:91]\n" \
"ds_read_b64_tr_b16 v[94:95], v2 offset:9216\n" \
"global_load_lds_dwordx4 v13, s[40:41] offset:3072\n" \
"v_mfma_f32_16x16x32_bf16 a[128:131], v[100:103], v[32:35], a[128:131]\n" \
"s_add_u32 s40, s40, 64\n" \
"s_addc_u32 s41, s41, 0\n" \
"v_mfma_f32_16x16x32_bf16 a[168:171], v[100:103], v[36:39], a[168:171]\n" \
"v_mfma_f32_16x16x32_bf16 v[132:135], v[100:103], v[40:43], v[132:135]\n" \
"v_mfma_f32_16x16x32_bf16 v[172:175], v[100:103], v[44:47], v[172:175]\n" \
"s_add_u32 m0, s50, 39936\n" \
"v_mfma_f32_16x16x32_bf16 v[212:215], v[100:103], v[48:51], v[212:215]\n" \
"global_load_lds_dwordx4 v14, s[42:43] offset:0\n" \
"s_waitcnt lgkmcnt(4)\n" \
"v_mfma_f32_16x16x32_bf16 a[12:15], v[104:107], v[20:23], a[12:15]\n" \
"ds_read_b64_tr_b16 v[96:97], v3 offset:8192\n" \
"v_mfma_f32_16x16x32_bf16 a[52:55], v[104:107], v[24:27], a[52:55]\n" \
"v_mfma_f32_16x16x32_bf16 a[92:95], v[104:107], v[28:31], a[92:95]\n" \
"ds_read_b64_tr_b16 v[98:99], v3 offset:9216\n" \
"v_mfma_f32_16x16x32_bf16 a[132:135], v[104:107], v[32:35], a[132:135]\n" \
"v_mfma_f32_16x16x32_bf16 a[172:175], v[104:107], v[36:39], a[172:175]\n" \
"global_load_lds_dwordx4 v15, s[42:43] offset:1024\n" \
"v_mfma_f32_16x16x32_bf16 v[136:139], v[104:107], v[40:43], v[136:139]\n" \
"s_add_u32 s42, s42, 229376\n" \
"s_addc_u32 s43, s43, 0\n" \
"v_mfma_f32_16x16x32_bf16 v[176:179], v[104:107], v[44:47], v[176:179]\n" \
"v_mfma_f32_16x16x32_bf16 v[216:219], v[104:107], v[48:51], v[216:219]\n" \
"s_waitcnt lgkmcnt(4)\n" \
"v_mfma_f32_16x16x32_bf16 a[16:19], v[108:111], v[20:23], a[16:19]\n" \
"ds_read_b64_tr_b16 v[100:101], v6 offset:0\n" \
"s_add_u32 m0, s51, 39936\n" \
"v_mfma_f32_16x16x32_bf16 a[56:59], v[108:111], v[24:27], a[56:59]\n" \
"global_load_lds_dwordx4 v14, s[44:45] offset:0\n" \
"v_mfma_f32_16x16x32_bf16 a[96:99], v[108:111], v[28:31], a[96:99]\n" \
"ds_read_b64_tr_b16 v[102:103], v7 offset:0\n" \
"v_mfma_f32_16x16x32_bf16 a[136:139], v[108:111], v[32:35], a[136:139]\n" \
"v_mfma_f32_16x16x32_bf16 a[176:179], v[108:111], v[36:39], a[176:179]\n" \
"v_mfma_f32_16x16x32_bf16 v[140:143], v[108:111], v[40:43], v[140:143]\n" \
"v_mfma_f32_16x16x32_bf16 v[180:183], v[108:111], v[44:47], v[180:183]\n" \
"global_load_lds_dwordx4 v15, s[44:45] offset:1024\n" \
"v_mfma_f32_16x16x32_bf16 v[220:223], v[108:111], v[48:51], v[220:223]\n" \
"s_add_u32 s44, s44, 229376\n" \
"s_addc_u32 s45, s45, 0\n" \
"s_waitcnt lgkmcnt(4)\n" \
"v_mfma_f32_16x16x32_bf16 a[20:23], v[92:95], v[20:23], a[20:23]\n" \
"ds_read_b64_tr_b16 v[104:105], v8 offset:0\n" \
"v_mfma_f32_16x16x32_bf16 a[60:63], v[92:95], v[24:27], a[60:63]\n" \
"v_mfma_f32_16x16x32_bf16 a[100:103], v[92:95], v[28:31], a[100:103]\n" \
"ds_read_b64_tr_b16 v[106:107], v8 offset:768\n" \
"s_add_u32 m0, s52, 39936\n" \
"v_mfma_f32_16x16x32_bf16 a[140:143], v[92:95], v[32:35], a[140:143]\n" \
"global_load_lds_dwordx4 v16, s[46:47]\n" \
"v_mfma_f32_16x16x32_bf16 a[180:183], v[92:95], v[36:39], a[180:183]\n" \
"v_mfma_f32_16x16x32_bf16 v[144:147], v[92:95], v[40:43], v[144:147]\n" \
"v_mfma_f32_16x16x32_bf16 v[184:187], v[92:95], v[44:47], v[184:187]\n" \
"v_mfma_f32_16x16x32_bf16 v[224:227], v[92:95], v[48:51], v[224:227]\n" \
"s_waitcnt lgkmcnt(4)\n" \
"v_mfma_f32_16x16x32_bf16 a[24:27], v[96:99], v[20:23], a[24:27]\n" \
"ds_read_b64_tr_b16 v[108:109], v9 offset:0\n" \
"v_mfma_f32_16x16x32_bf16 a[64:67], v[96:99], v[24:27], a[64:67]\n" \
"s_cmp_eq_u32 s53, 0\n" \
"s_cbranch_scc1 LZ%=_155\n" \
"global_load_lds_dwordx4 v17, s[46:47] offset:1024\n" \
"LZ%=_155:\n" \
"v_mfma_f32_16x16x32_bf16 a[104:107], v[96:99], v[28:31], a[104:107]\n" \
"s_add_u32 s46, s46, s48\n" \
"s_addc_u32 s47, s47, 0\n" \
"v_mfma_f32_16x16x32_bf16 a[144:147], v[96:99], v[32:35], a[144:147]\n" \
"ds_read_b64_tr_b16 v[110:111], v9 offset:768\n" \
"v_mfma_f32_16x16x32_bf16 a[184:187], v[96:99], v[36:39], a[184:187]\n" \
"v_mfma_f32_16x16x32_bf16 v[148:151], v[96:99], v[40:43], v[148:151]\n" \
"v_mfma_f32_16x16x32_bf16 v[188:191], v[96:99], v[44:47], v[188:191]\n" \
"v_mfma_f32_16x16x32_bf16 v[228:231], v[96:99], v[48:51], v[228:231]\n" \
"s_waitcnt lgkmcnt(0)\n" \
"s_waitcnt vmcnt(18)\n" \
"s_barrier\n" \
"v_mfma_f32_16x16x32_bf16 a[28:31], v[100:103], v[20:23], a[28:31]\n" \
"v_add_u32 v1, 39936, v1\n" \
"ds_read_b64_tr_b16 v[92:93], v1 offset:0\n" \
"v_mfma_f32_16x16x32_bf16 a[68:71], v[100:103], v[24:27], a[68:71]\n" \
"ds_read_b64_tr_b16 v[94:95], v1 offset:1024\n" \
"v_mfma_f32_16x16x32_bf16 a[108:111], v[100:103], v[28:31], a[108:111]\n" \
"v_add_u32 v0, 39936, v0\n" \
"ds_read_b128 v[56:59], v0 offset:1024\n" \
"v_mfma_f32_16x16x32_bf16 a[148:151], v[100:103], v[32:35], a[148:151]\n" \
"v_add_u32 v4, 39936, v4\n" \
"ds_read_b128 v[60:63], v0 offset:2048\n" \
"v_mfma_f32_16x16x32_bf16 a[188:191], v[100:103], v[36:39], a[188:191]\n" \
"s_add_u32 s88, s78, 16384\n" \
"ds_read_b128 v[64:67], v0 offset:3072\n" \
"v_mfma_f32_16x16x32_bf16 v[152:155], v[100:103], v[40:43], v[152:155]\n" \
"v_add_u32 v5, 39936, v5\n" \
"ds_read_b128 v[68:71], v0 offset:4096\n" \
"v_mfma_f32_16x16x32_bf16 v[192:195], v[100:103], v[44:47], v[192:195]\n" \
"s_cmp_eq_u32 s87, 0\n" \
"s_cselect_b32 s88, s78, s88\n" \
"v_mfma_f32_16x16x32_bf16 v[232:235], v[100:103], v[48:51], v[232:235]\n" \
"ds_read_b128 v[72:75], v0 offset:5120\n" \
"v_mfma_f32_16x16x32_bf16 a[32:35], v[104:107], v[20:23], a[32:35]\n" \
"v_add_u32 v6, 39936, v6\n" \
"ds_read_b128 v[76:79], v0 offset:6144\n" \
"v_mfma_f32_16x16x32_bf16 a[72:75], v[104:107], v[24:27], a[72:75]\n" \
"s_bitcmp1_b32 s54, s87\n" \
"s_cselect_b32 s88, s88, s92\n" \
"v_mfma_f32_16x16x32_bf16 a[112:115], v[104:107], v[28:31], a[112:115]\n" \
"ds_read_b128 v[80:83], v0 offset:7168\n" \
"v_add_u32 v7, 39936, v7\n" \
"v_mfma_f32_16x16x32_bf16 a[152:155], v[104:107], v[32:35], a[152:155]\n" \
"ds_read_b128 v[84:87], v0 offset:8192\n" \
"v_mov_b32 v116, s88\n" \
"v_mfma_f32_16x16x32_bf16 a[192:195], v[104:107], v[36:39], a[192:195]\n" \
"v_add_u32 v18, 39936, v18\n" \
"ds_read_b128 v[88:91], v18 offset:0\n" \
"v_mfma_f32_16x16x32_bf16 v[156:159], v[104:107], v[40:43], v[156:159]\n" \
"v_add_u32 v8, 39936, v8\n" \
"v_add_u32 v2, 39936, v2\n" \
"v_mfma_f32_16x16x32_bf16 v[196:199], v[104:107], v[44:47], v[196:199]\n" \
"ds_read_b64_tr_b16 v[96:97], v2 offset:0\n" \
"v_mov_b32 v118, s84\n" \
"v_mfma_f32_16x16x32_bf16 v[236:239], v[104:107], v[48:51], v[236:239]\n" \
"ds_read_b64_tr_b16 v[98:99], v2 offset:1024\n" \
"v_add_u32 v9, 39936, v9\n" \
"v_mfma_f32_16x16x32_bf16 v[244:247], v[104:107], v[52:55], v[244:247]\n" \
"v_add_u32 v3, 39936, v3\n" \
"v_mfma_f32_16x16x32_bf16 a[36:39], v[108:111], v[20:23], a[36:39]\n" \
"ds_read_b64_tr_b16 v[100:101], v3 offset:0\n" \
"v_mov_b32 v119, s85\n" \
"v_mfma_f32_16x16x32_bf16 a[76:79], v[108:111], v[24:27], a[76:79]\n" \
"ds_read_b64_tr_b16 v[102:103], v3 offset:1024\n" \
"global_store_dwordx2 v116, v[118:119], s[74:75] sc1\n" \
"v_mfma_f32_16x16x32_bf16 a[116:119], v[108:111], v[28:31], a[116:119]\n" \
"v_mfma_f32_16x16x32_bf16 a[156:159], v[108:111], v[32:35], a[156:159]\n" \
"v_mfma_f32_16x16x32_bf16 v[120:123], v[108:111], v[36:39], v[120:123]\n" \
"v_mfma_f32_16x16x32_bf16 v[160:163], v[108:111], v[40:43], v[160:163]\n" \
"v_mfma_f32_16x16x32_bf16 v[200:203], v[108:111], v[44:47], v[200:203]\n" \
"v_mfma_f32_16x16x32_bf16 v[240:243], v[108:111], v[48:51], v[240:243]\n" \
"s_waitcnt lgkmcnt(9)\n" \
"v_mfma_f32_16x16x32_bf16 a[0:3], v[92:95], v[56:59], a[0:3]\n" \
"ds_read_b64_tr_b16 v[104:105], v4 offset:0\n" \
"v_mfma_f32_16x16x32_bf16 a[40:43], v[92:95], v[60:63], a[40:43]\n" \
"v_mfma_f32_16x16x32_bf16 a[80:83], v[92:95], v[64:67], a[80:83]\n" \
"ds_read_b64_tr_b16 v[106:107], v5 offset:0\n" \
"s_add_u32 m0, s49, 79872\n" \
"v_mfma_f32_16x16x32_bf16 a[120:123], v[92:95], v[68:71], a[120:123]\n" \
"global_load_lds_dwordx4 v10, s[40:41] offset:0\n" \
"s_waitcnt lgkmcnt(7)\n" \
"v_mfma_f32_16x16x32_bf16 a[160:163], v[92:95], v[72:75], a[160:163]\n" \
"v_mfma_f32_16x16x32_bf16 v[124:127], v[92:95], v[76:79], v[124:127]\n" \
"v_mfma_f32_16x16x32_bf16 v[164:167], v[92:95], v[80:83], v[164:167]\n" \
"v_mfma_f32_16x16x32_bf16 v[204:207], v[92:95], v[84:87], v[204:207]\n" \
"s_waitcnt lgkmcnt(4)\n" \
"v_mfma_f32_16x16x32_bf16 a[4:7], v[96:99], v[56:59], a[4:7]\n" \
"ds_read_b64_tr_b16 v[108:109], v1 offset:8192\n" \
"global_load_lds_dwordx4 v11, s[40:41] offset:1024\n" \
"v_mfma_f32_16x16x32_bf16 a[44:47], v[96:99], v[60:63], a[44:47]\n" \
"v_mfma_f32_16x16x32_bf16 a[84:87], v[96:99], v[64:67], a[84:87]\n" \
"ds_read_b64_tr_b16 v[110:111], v1 offset:9216\n" \
"v_mfma_f32_16x16x32_bf16 a[124:127], v[96:99], v[68:71], a[124:127]\n" \
"v_mfma_f32_16x16x32_bf16 a[164:167], v[96:99], v[72:75], a[164:167]\n" \
"v_mfma_f32_16x16x32_bf16 v[128:131], v[96:99], v[76:79], v[128:131]\n" \
"global_load_lds_dwordx4 v12, s[40:41] offset:2048\n" \
"v_mfma_f32_16x16x32_bf16 v[168:171], v[96:99], v[80:83], v[168:171]\n" \
"v_mfma_f32_16x16x32_bf16 v[208:211], v[96:99], v[84:87], v[208:211]\n" \
"s_waitcnt lgkmcnt(4)\n" \
"v_mfma_f32_16x16x32_bf16 a[8:11], v[100:103], v[56:59], a[8:11]\n" \
"ds_read_b64_tr_b16 v[92:93], v2 offset:8192\n" \
"v_mfma_f32_16x16x32_bf16 a[48:51], v[100:103], v[60:63], a[48:51]\n" \
"v_mfma_f32_16x16x32_bf16 a[88:91], v[100:103], v[64:67], a[88:91]\n" \
"ds_read_b64_tr_b16 v[94:95], v2 offset:9216\n" \
"global_load_lds_dwordx4 v13, s[40:41] offset:3072\n" \
"v_mfma_f32_16x16x32_bf16 a[128:131], v[100:103], v[68:71], a[128:131]\n" \
"s_add_u32 s40, s40, 64\n" \
"s_addc_u32 s41, s41, 0\n" \
"v_mfma_f32_16x16x32_bf16 a[168:171], v[100:103], v[72:75], a[168:171]\n" \
"v_mfma_f32_16x16x32_bf16 v[132:135], v[100:103], v[76:79], v[132:135]\n" \
"v_mfma_f32_16x16x32_bf16 v[172:175], v[100:103], v[80:83], v[172:175]\n" \
"s_add_u32 m0, s50, 79872\n" \
"v_mfma_f32_16x16x32_bf16 v[212:215], v[100:103], v[84:87], v[212:215]\n" \
"global_load_lds_dwordx4 v14, s[42:43] offset:0\n" \
"s_waitcnt lgkmcnt(4)\n" \
"v_mfma_f32_16x16x32_bf16 a[12:15], v[104:107], v[56:59], a[12:15]\n" \
"ds_read_b64_tr_b16 v[96:97], v3 offset:8192\n" \
"v_mfma_f32_16x16x32_bf16 a[52:55], v[104:107], v[60:63], a[52:55]\n" \
"v_mfma_f32_16x16x32_bf16 a[92:95], v[104:107], v[64:67], a[92:95]\n" \
"ds_read_b64_tr_b16 v[98:99], v3 offset:9216\n" \
"v_mfma_f32_16x16x32_bf16 a[132:135], v[104:107], v[68:71], a[132:135]\n" \
"v_mfma_f32_16x16x32_bf16 a[172:175], v[104:107], v[72:75], a[172:175]\n" \
"global_load_lds_dwordx4 v15, s[42:43] offset:1024\n" \
"v_mfma_f32_16x16x32_bf16 v[136:139], v[104:107], v[76:79], v[136:139]\n" \
"s_add_u32 s42, s42, 229376\n" \
"s_addc_u32 s43, s43, 0\n" \
"v_mfma_f32_16x16x32_bf16 v[176:179], v[104:107], v[80:83], v[176:179]\n" \
"v_mfma_f32_16x16x32_bf16 v[216:219], v[104:107], v[84:87], v[216:219]\n" \
"s_waitcnt lgkmcnt(4)\n" \
"v_mfma_f32_16x16x32_bf16 a[16:19], v[108:111], v[56:59], a[16:19]\n" \
"ds_read_b64_tr_b16 v[100:101], v6 offset:0\n" \
"s_add_u32 m0, s51, 79872\n" \
"v_mfma_f32_16x16x32_bf16 a[56:59], v[108:111], v[60:63], a[56:59]\n" \
"global_load_lds_dwordx4 v14, s[44:45] offset:0\n" \
"v_mfma_f32_16x16x32_bf16 a[96:99], v[108:111], v[64:67], a[96:99]\n" \
"ds_read_b64_tr_b16 v[102:103], v7 offset:0\n" \
"v_mfma_f32_16x16x32_bf16 a[136:139], v[108:111], v[68:71], a[136:139]\n" \
"v_mfma_f32_16x16x32_bf16 a[176:179], v[108:111], v[72:75], a[176:179]\n" \
"v_mfma_f32_16x16x32_bf16 v[140:143], v[108:111], v[76:79], v[140:143]\n" \
"v_mfma_f32_16x16x32_bf16 v[180:183], v[108:111], v[80:83], v[180:183]\n" \
"global_load_lds_dwordx4 v15, s[44:45] offset:1024\n" \
"v_mfma_f32_16x16x32_bf16 v[220:223], v[108:111], v[84:87], v[220:223]\n" \
"s_add_u32 s44, s44, 229376\n" \
"s_addc_u32 s45, s45, 0\n" \
"s_waitcnt lgkmcnt(4)\n" \
"v_mfma_f32_16x16x32_bf16 a[20:23], v[92:95], v[56:59], a[20:23]\n" \
"ds_read_b64_tr_b16 v[104:105], v8 offset:0\n" \
"v_mfma_f32_16x16x32_bf16 a[60:63], v[92:95], v[60:63], a[60:63]\n" \
"v_mfma_f32_16x16x32_bf16 a[100:103], v[92:95], v[64:67], a[100:103]\n" \
"ds_read_b64_tr_b16 v[106:107], v8 offset:768\n" \
"s_add_u32 m0, s52, 79872\n" \
"v_mfma_f32_16x16x32_bf16 a[140:143], v[92:95], v[68:71], a[140:143]\n" \
"global_load_lds_dwordx4 v16, s[46:47]\n" \
"v_mfma_f32_16x16x32_bf16 a[180:183], v[92:95], v[72:75], a[180:183]\n" \
"v_mfma_f32_16x16x32_bf16 v[144:147], v[92:95], v[76:79], v[144:147]\n" \
"v_mfma_f32_16x16x32_bf16 v[184:187], v[92:95], v[80:83], v[184:187]\n" \
"v_mfma_f32_16x16x32_bf16 v[224:227], v[92:95], v[84:87], v[224:227]\n" \
"s_waitcnt lgkmcnt(4)\n" \
"v_mfma_f32_16x16x32_bf16 a[24:27], v[96:99], v[56:59], a[24:27]\n" \
"ds_read_b64_tr_b16 v[108:109], v9 offset:0\n" \
"v_mfma_f32_16x16x32_bf16 a[64:67], v[96:99], v[60:63], a[64:67]\n" \
"s_cmp_eq_u32 s53, 0\n" \
"s_cbranch_scc1 LZ%=_165\n" \
"global_load_lds_dwordx4 v17, s[46:47] offset:1024\n" \
"LZ%=_165:\n" \
"v_mfma_f32_16x16x32_bf16 a[104:107], v[96:99], v[64:67], a[104:107]\n" \
"s_add_u32 s46, s46, s48\n" \
"s_addc_u32 s47, s47, 0\n" \
"v_mfma_f32_16x16x32_bf16 a[144:147], v[96:99], v[68:71], a[144:147]\n" \
"ds_read_b64_tr_b16 v[110:111], v9 offset:768\n" \
"v_mfma_f32_16x16x32_bf16 a[184:187], v[96:99], v[72:75], a[184:187]\n" \
"v_mfma_f32_16x16x32_bf16 v[148:151], v[96:99], v[76:79], v[148:151]\n" \
"v_mfma_f32_16x16x32_bf16 v[188:191], v[96:99], v[80:83], v[188:191]\n" \
"v_mfma_f32_16x16x32_bf16 v[228:231], v[96:99], v[84:87], v[228:231]\n" \
"s_waitcnt lgkmcnt(0)\n" \
"s_waitcnt vmcnt(18)\n" \
"s_barrier\n" \
"v_mfma_f32_16x16x32_bf16 a[28:31], v[100:103], v[56:59], a[28:31]\n" \
"v_add_u32 v1, 4294847488, v1\n" \
"ds_read_b64_tr_b16 v[92:93], v1 offset:0\n" \
"v_mfma_f32_16x16x32_bf16 a[68:71], v[100:103], v[60:63], a[68:71]\n" \
"ds_read_b64_tr_b16 v[94:95], v1 offset:1024\n" \
"v_mfma_f32_16x16x32_bf16 a[108:111], v[100:103], v[64:67], a[108:111]\n" \
"v_add_u32 v0, 4294847488, v0\n" \
"ds_read_b128 v[20:23], v0 offset:1024\n" \
"v_mfma_f32_16x16x32_bf16 a[148:151], v[100:103], v[68:71], a[148:151]\n" \
"v_add_u32 v4, 4294847488, v4\n" \
"ds_read_b128 v[24:27], v0 offset:2048\n" \
"v_mfma_f32_16x16x32_bf16 a[188:191], v[100:103], v[72:75], a[188:191]\n" \
"ds_read_b128 v[28:31], v0 offset:3072\n" \
"v_add_u32 v5, 4294847488, v5\n" \
"v_mfma_f32_16x16x32_bf16 v[152:155], v[100:103], v[76:79], v[152:155]\n" \
"ds_read_b128 v[32:35], v0 offset:4096\n" \
"v_mfma_f32_16x16x32_bf16 v[192:195], v[100:103], v[80:83], v[192:195]\n" \
"ds_read_b128 v[36:39], v0 offset:5120\n" \
"v_add_u32 v6, 4294847488, v6\n" \
"v_mfma_f32_16x16x32_bf16 v[232:235], v[100:103], v[84:87], v[232:235]\n" \
"ds_read_b128 v[40:43], v0 offset:6144\n" \
"v_mfma_f32_16x16x32_bf16 a[32:35], v[104:107], v[56:59], a[32:35]\n" \
"ds_read_b128 v[44:47], v0 offset:7168\n" \
"v_add_u32 v7, 4294847488, v7\n" \
"v_mfma_f32_16x16x32_bf16 a[72:75], v[104:107], v[60:63], a[72:75]\n" \
"ds_read_b128 v[48:51], v0 offset:8192\n" \
"v_mfma_f32_16x16x32_bf16 a[112:115], v[104:107], v[64:67], a[112:115]\n" \
"v_add_u32 v18, 4294847488, v18\n" \
"ds_read_b128 v[52:55], v18 offset:0\n" \
"v_mfma_f32_16x16x32_bf16 a[152:155], v[104:107], v[68:71], a[152:155]\n" \
"v_add_u32 v8, 4294847488, v8\n" \
"v_add_u32 v2, 4294847488, v2\n" \
"v_mfma_f32_16x16x32_bf16 a[192:195], v[104:107], v[72:75], a[192:195]\n" \
"ds_read_b64_tr_b16 v[96:97], v2 offset:0\n" \
"ds_read_b64_tr_b16 v[98:99], v2 offset:1024\n" \
"v_mfma_f32_16x16x32_bf16 v[156:159], v[104:107], v[76:79], v[156:159]\n" \
"v_add_u32 v9, 4294847488, v9\n" \
"v_add_u32 v3, 4294847488, v3\n" \
"v_mfma_f32_16x16x32_bf16 v[196:199], v[104:107], v[80:83], v[196:199]\n" \
"ds_read_b64_tr_b16 v[100:101], v3 offset:0\n" \
"ds_read_b64_tr_b16 v[102:103], v3 offset:1024\n" \
"v_mfma_f32_16x16x32_bf16 v[236:239], v[104:107], v[84:87], v[236:239]\n" \
"v_mfma_f32_16x16x32_bf16 v[244:247], v[104:107], v[88:91], v[244:247]\n" \
"v_mfma_f32_16x16x32_bf16 a[36:39], v[108:111], v[56:59], a[36:39]\n" \
"v_mfma_f32_16x16x32_bf16 a[76:79], v[108:111], v[60:63], a[76:79]\n" \
"v_mfma_f32_16x16x32_bf16 a[116:119], v[108:111], v[64:67], a[116:119]\n" \
"v_mfma_f32_16x16x32_bf16 a[156:159], v[108:111], v[68:71], a[156:159]\n" \
"v_mfma_f32_16x16x32_bf16 v[120:123], v[108:111], v[72:75], v[120:123]\n" \
"v_mfma_f32_16x16x32_bf16 v[160:163], v[108:111], v[76:79], v[160:163]\n" \
"v_mfma_f32_16x16x32_bf16 v[200:203], v[108:111], v[80:83], v[200:203]\n" \
"v_mfma_f32_16x16x32_bf16 v[240:243], v[108:111], v[84:87], v[240:243]\n" \
"s_add_u32 s87, s87, 1\n" \
"s_cmp_lg_u32 s87, 7\n" \
"s_cbranch_scc1 LK1%=\n" \
"s_waitcnt lgkmcnt(9)\n" \
"v_mfma_f32_16x16x32_bf16 a[0:3], v[92:95], v[20:23], a[0:3]\n" \
"ds_read_b64_tr_b16 v[104:105], v4 offset:0\n" \
"v_mfma_f32_16x16x32_bf16 a[40:43], v[92:95], v[24:27], a[40:43]\n" \
"v_mfma_f32_16x16x32_bf16 a[80:83], v[92:95], v[28:31], a[80:83]\n" \
"ds_read_b64_tr_b16 v[106:107], v5 offset:0\n" \
"s_add_u32 m0, s49, 119808\n" \
"v_mfma_f32_16x16x32_bf16 a[120:123], v[92:95], v[32:35], a[120:123]\n" \
"global_load_lds_dwordx4 v10, s[40:41] offset:0\n" \
"s_waitcnt lgkmcnt(7)\n" \
"v_mfma_f32_16x16x32_bf16 a[160:163], v[92:95], v[36:39], a[160:163]\n" \
"v_mfma_f32_16x16x32_bf16 v[124:127], v[92:95], v[40:43], v[124:127]\n" \
"v_mfma_f32_16x16x32_bf16 v[164:167], v[92:95], v[44:47], v[164:167]\n" \
"v_mfma_f32_16x16x32_bf16 v[204:207], v[92:95], v[48:51], v[204:207]\n" \
"s_waitcnt lgkmcnt(4)\n" \
"v_mfma_f32_16x16x32_bf16 a[4:7], v[96:99], v[20:23], a[4:7]\n" \
"ds_read_b64_tr_b16 v[108:109], v1 offset:8192\n" \
"global_load_lds_dwordx4 v11, s[40:41] offset:1024\n" \
"v_mfma_f32_16x16x32_bf16 a[44:47], v[96:99], v[24:27], a[44:47]\n" \
"v_mfma_f32_16x16x32_bf16 a[84:87], v[96:99], v[28:31], a[84:87]\n" \
"ds_read_b64_tr_b16 v[110:111], v1 offset:9216\n" \
"v_mfma_f32_16x16x32_bf16 a[124:127], v[96:99], v[32:35], a[124:127]\n" \
"v_mfma_f32_16x16x32_bf16 a[164:167], v[96:99], v[36:39], a[164:167]\n" \
"v_mfma_f32_16x16x32_bf16 v[128:131], v[96:99], v[40:43], v[128:131]\n" \
"global_load_lds_dwordx4 v12, s[40:41] offset:2048\n" \
"v_mfma_f32_16x16x32_bf16 v[168:171], v[96:99], v[44:47], v[168:171]\n" \
"v_mfma_f32_16x16x32_bf16 v[208:211], v[96:99], v[48:51], v[208:211]\n" \
"s_waitcnt lgkmcnt(4)\n" \
"v_mfma_f32_16x16x32_bf16 a[8:11], v[100:103], v[20:23], a[8:11]\n" \
"ds_read_b64_tr_b16 v[92:93], v2 offset:8192\n" \
"v_mfma_f32_16x16x32_bf16 a[48:51], v[100:103], v[24:27], a[48:51]\n" \
"v_mfma_f32_16x16x32_bf16 a[88:91], v[100:103], v[28:31], a[88:91]\n" \
"ds_read_b64_tr_b16 v[94:95], v2 offset:9216\n" \
"global_load_lds_dwordx4 v13, s[40:41] offset:3072\n" \
"v_mfma_f32_16x16x32_bf16 a[128:131], v[100:103], v[32:35], a[128:131]\n" \
"s_add_u32 s40, s40, 64\n" \
"s_addc_u32 s41, s41, 0\n" \
"v_mfma_f32_16x16x32_bf16 a[168:171], v[100:103], v[36:39], a[168:171]\n" \
"v_mfma_f32_16x16x32_bf16 v[132:135], v[100:103], v[40:43], v[132:135]\n" \
"v_mfma_f32_16x16x32_bf16 v[172:175], v[100:103], v[44:47], v[172:175]\n" \
"s_add_u32 m0, s50, 119808\n" \
"v_mfma_f32_16x16x32_bf16 v[212:215], v[100:103], v[48:51], v[212:215]\n" \
"global_load_lds_dwordx4 v14, s[42:43] offset:0\n" \
"s_waitcnt lgkmcnt(4)\n" \
"v_mfma_f32_16x16x32_bf16 a[12:15], v[104:107], v[20:23], a[12:15]\n" \
"ds_read_b64_tr_b16 v[96:97], v3 offset:8192\n" \
"v_mfma_f32_16x16x32_bf16 a[52:55], v[104:107], v[24:27], a[52:55]\n" \
"v_mfma_f32_16x16x32_bf16 a[92:95], v[104:107], v[28:31], a[92:95]\n" \
"ds_read_b64_tr_b16 v[98:99], v3 offset:9216\n" \
"v_mfma_f32_16x16x32_bf16 a[132:135], v[104:107], v[32:35], a[132:135]\n" \
"v_mfma_f32_16x16x32_bf16 a[172:175], v[104:107], v[36:39], a[172:175]\n" \
"global_load_lds_dwordx4 v15, s[42:43] offset:1024\n" \
"v_mfma_f32_16x16x32_bf16 v[136:139], v[104:107], v[40:43], v[136:139]\n" \
"s_add_u32 s42, s42, 229376\n" \
"s_addc_u32 s43, s43, 0\n" \
"v_mfma_f32_16x16x32_bf16 v[176:179], v[104:107], v[44:47], v[176:179]\n" \
"v_mfma_f32_16x16x32_bf16 v[216:219], v[104:107], v[48:51], v[216:219]\n" \
"s_waitcnt lgkmcnt(4)\n" \
"v_mfma_f32_16x16x32_bf16 a[16:19], v[108:111], v[20:23], a[16:19]\n" \
"ds_read_b64_tr_b16 v[100:101], v6 offset:0\n" \
"s_add_u32 m0, s51, 119808\n" \
"v_mfma_f32_16x16x32_bf16 a[56:59], v[108:111], v[24:27], a[56:59]\n" \
"global_load_lds_dwordx4 v14, s[44:45] offset:0\n" \
"v_mfma_f32_16x16x32_bf16 a[96:99], v[108:111], v[28:31], a[96:99]\n" \
"ds_read_b64_tr_b16 v[102:103], v7 offset:0\n" \
"v_mfma_f32_16x16x32_bf16 a[136:139], v[108:111], v[32:35], a[136:139]\n" \
"v_mfma_f32_16x16x32_bf16 a[176:179], v[108:111], v[36:39], a[176:179]\n" \
"v_mfma_f32_16x16x32_bf16 v[140:143], v[108:111], v[40:43], v[140:143]\n" \
"v_mfma_f32_16x16x32_bf16 v[180:183], v[108:111], v[44:47], v[180:183]\n" \
"global_load_lds_dwordx4 v15, s[44:45] offset:1024\n" \
"v_mfma_f32_16x16x32_bf16 v[220:223], v[108:111], v[48:51], v[220:223]\n" \
"s_add_u32 s44, s44, 229376\n" \
"s_addc_u32 s45, s45, 0\n" \
"s_waitcnt lgkmcnt(4)\n" \
"v_mfma_f32_16x16x32_bf16 a[20:23], v[92:95], v[20:23], a[20:23]\n" \
"ds_read_b64_tr_b16 v[104:105], v8 offset:0\n" \
"v_mfma_f32_16x16x32_bf16 a[60:63], v[92:95], v[24:27], a[60:63]\n" \
"v_mfma_f32_16x16x32_bf16 a[100:103], v[92:95], v[28:31], a[100:103]\n" \
"ds_read_b64_tr_b16 v[106:107], v8 offset:768\n" \
"s_add_u32 m0, s52, 119808\n" \
"v_mfma_f32_16x16x32_bf16 a[140:143], v[92:95], v[32:35], a[140:143]\n" \
"global_load_lds_dwordx4 v16, s[46:47]\n" \
"v_mfma_f32_16x16x32_bf16 a[180:183], v[92:95], v[36:39], a[180:183]\n" \
"v_mfma_f32_16x16x32_bf16 v[144:147], v[92:95], v[40:43], v[144:147]\n" \
"v_mfma_f32_16x16x32_bf16 v[184:187], v[92:95], v[44:47], v[184:187]\n" \
"v_mfma_f32_16x16x32_bf16 v[224:227], v[92:95], v[48:51], v[224:227]\n" \
"s_waitcnt lgkmcnt(4)\n" \
"v_mfma_f32_16x16x32_bf16 a[24:27], v[96:99], v[20:23], a[24:27]\n" \
"ds_read_b64_tr_b16 v[108:109], v9 offset:0\n" \
"v_mfma_f32_16x16x32_bf16 a[64:67], v[96:99], v[24:27], a[64:67]\n" \
"s_cmp_eq_u32 s53, 0\n" \
"s_cbranch_scc1 LZ%=_175\n" \
"global_load_lds_dwordx4 v17, s[46:47] offset:1024\n" \
"LZ%=_175:\n" \
"v_mfma_f32_16x16x32_bf16 a[104:107], v[96:99], v[28:31], a[104:107]\n" \
"s_add_u32 s46, s46, s48\n" \
"s_addc_u32 s47, s47, 0\n" \
"v_mfma_f32_16x16x32_bf16 a[144:147], v[96:99], v[32:35], a[144:147]\n" \
"ds_read_b64_tr_b16 v[110:111], v9 offset:768\n" \
"v_mfma_f32_16x16x32_bf16 a[184:187], v[96:99], v[36:39], a[184:187]\n" \
"v_mfma_f32_16x16x32_bf16 v[148:151], v[96:99], v[40:43], v[148:151]\n" \
"v_mfma_f32_16x16x32_bf16 v[188:191], v[96:99], v[44:47], v[188:191]\n" \
"v_mfma_f32_16x16x32_bf16 v[228:231], v[96:99], v[48:51], v[228:231]\n" \
"s_waitcnt lgkmcnt(0)\n" \
"s_waitcnt vmcnt(18)\n" \
"s_barrier\n" \
"v_mfma_f32_16x16x32_bf16 a[28:31], v[100:103], v[20:23], a[28:31]\n" \
"v_add_u32 v1, 39936, v1\n" \
"ds_read_b64_tr_b16 v[92:93], v1 offset:0\n" \
"v_mfma_f32_16x16x32_bf16 a[68:71], v[100:103], v[24:27], a[68:71]\n" \
"ds_read_b64_tr_b16 v[94:95], v1 offset:1024\n" \
"v_mfma_f32_16x16x32_bf16 a[108:111], v[100:103], v[28:31], a[108:111]\n" \
"v_add_u32 v0, 39936, v0\n" \
"ds_read_b128 v[56:59], v0 offset:1024\n" \
"v_mfma_f32_16x16x32_bf16 a[148:151], v[100:103], v[32:35], a[148:151]\n" \
"v_add_u32 v4, 39936, v4\n" \
"ds_read_b128 v[60:63], v0 offset:2048\n" \
"v_mfma_f32_16x16x32_bf16 a[188:191], v[100:103], v[36:39], a[188:191]\n" \
"ds_read_b128 v[64:67], v0 offset:3072\n" \
"v_add_u32 v5, 39936, v5\n" \
"v_mfma_f32_16x16x32_bf16 v[152:155], v[100:103], v[40:43], v[152:155]\n" \
"ds_read_b128 v[68:71], v0 offset:4096\n" \
"v_mfma_f32_16x16x32_bf16 v[192:195], v[100:103], v[44:47], v[192:195]\n" \
"ds_read_b128 v[72:75], v0 offset:5120\n" \
"v_add_u32 v6, 39936, v6\n" \
"v_mfma_f32_16x16x32_bf16 v[232:235], v[100:103], v[48:51], v[232:235]\n" \
"ds_read_b128 v[76:79], v0 offset:6144\n" \
"v_mfma_f32_16x16x32_bf16 a[32:35], v[104:107], v[20:23], a[32:35]\n" \
"ds_read_b128 v[80:83], v0 offset:7168\n" \
"v_add_u32 v7, 39936, v7\n" \
"v_mfma_f32_16x16x32_bf16 a[72:75], v[104:107], v[24:27], a[72:75]\n" \
"ds_read_b128 v[84:87], v0 offset:8192\n" \
"v_mfma_f32_16x16x32_bf16 a[112:115], v[104:107], v[28:31], a[112:115]\n" \
"v_add_u32 v18, 39936, v18\n" \
"ds_read_b128 v[88:91], v18 offset:0\n" \
"v_mfma_f32_16x16x32_bf16 a[152:155], v[104:107], v[32:35], a[152:155]\n" \
"v_add_u32 v8, 39936, v8\n" \
"v_add_u32 v2, 39936, v2\n" \
"v_mfma_f32_16x16x32_bf16 a[192:195], v[104:107], v[36:39], a[192:195]\n" \
"ds_read_b64_tr_b16 v[96:97], v2 offset:0\n" \
"ds_read_b64_tr_b16 v[98:99], v2 offset:1024\n" \
"v_mfma_f32_16x16x32_bf16 v[156:159], v[104:107], v[40:43], v[156:159]\n" \
"v_add_u32 v9, 39936, v9\n" \
"v_add_u32 v3, 39936, v3\n" \
"v_mfma_f32_16x16x32_bf16 v[196:199], v[104:107], v[44:47], v[196:199]\n" \
"ds_read_b64_tr_b16 v[100:101], v3 offset:0\n" \
"ds_read_b64_tr_b16 v[102:103], v3 offset:1024\n" \
"v_mfma_f32_16x16x32_bf16 v[236:239], v[104:107], v[48:51], v[236:239]\n" \
"v_mfma_f32_16x16x32_bf16 v[244:247], v[104:107], v[52:55], v[244:247]\n" \
"v_mfma_f32_16x16x32_bf16 a[36:39], v[108:111], v[20:23], a[36:39]\n" \
"v_mfma_f32_16x16x32_bf16 a[76:79], v[108:111], v[24:27], a[76:79]\n" \
"v_mfma_f32_16x16x32_bf16 a[116:119], v[108:111], v[28:31], a[116:119]\n" \
"v_mfma_f32_16x16x32_bf16 a[156:159], v[108:111], v[32:35], a[156:159]\n" \
"v_mfma_f32_16x16x32_bf16 v[120:123], v[108:111], v[36:39], v[120:123]\n" \
"v_mfma_f32_16x16x32_bf16 v[160:163], v[108:111], v[40:43], v[160:163]\n" \
"v_mfma_f32_16x16x32_bf16 v[200:203], v[108:111], v[44:47], v[200:203]\n" \
"v_mfma_f32_16x16x32_bf16 v[240:243], v[108:111], v[48:51], v[240:243]\n" \
"s_waitcnt lgkmcnt(9)\n" \
"v_mfma_f32_16x16x32_bf16 a[0:3], v[92:95], v[56:59], a[0:3]\n" \
"ds_read_b64_tr_b16 v[104:105], v4 offset:0\n" \
"v_mfma_f32_16x16x32_bf16 a[40:43], v[92:95], v[60:63], a[40:43]\n" \
"global_load_dwordx2 a[220:221], v254, s[74:75] sc1\n" \
"v_mfma_f32_16x16x32_bf16 a[80:83], v[92:95], v[64:67], a[80:83]\n" \
"ds_read_b64_tr_b16 v[106:107], v5 offset:0\n" \
"v_mfma_f32_16x16x32_bf16 a[120:123], v[92:95], v[68:71], a[120:123]\n" \
"s_cmp_eq_u32 s81, 0\n" \
"s_cbranch_scc1 LA1%=\n" \
"v_mbcnt_lo_u32_b32 v255, -1, 0\n" \
"v_mbcnt_hi_u32_b32 v255, -1, v255\n" \
"v_lshlrev_b32 v255, 6, v255\n" \
"v_add_u32 v255, 4096, v255\n" \
"global_load_dword a222, v255, s[74:75] sc1\n" \
"v_add_u32 v255, 4096, v255\n" \
"global_load_dword a223, v255, s[74:75] sc1\n" \
"v_add_u32 v255, 4096, v255\n" \
"global_load_dword a224, v255, s[74:75] sc1\n" \
"v_add_u32 v255, 4096, v255\n" \
"global_load_dword a225, v255, s[74:75] sc1\n" \
"v_add_u32 v255, 4096, v255\n" \
"LA1%=:\n" \
"s_waitcnt lgkmcnt(7)\n" \
"v_mfma_f32_16x16x32_bf16 a[160:163], v[92:95], v[72:75], a[160:163]\n" \
"v_mfma_f32_16x16x32_bf16 v[124:127], v[92:95], v[76:79], v[124:127]\n" \
"v_mfma_f32_16x16x32_bf16 v[164:167], v[92:95], v[80:83], v[164:167]\n" \
"v_mfma_f32_16x16x32_bf16 v[204:207], v[92:95], v[84:87], v[204:207]\n" \
"s_waitcnt lgkmcnt(4)\n" \
"v_mfma_f32_16x16x32_bf16 a[4:7], v[96:99], v[56:59], a[4:7]\n" \
"ds_read_b64_tr_b16 v[108:109], v1 offset:8192\n" \
"v_mfma_f32_16x16x32_bf16 a[44:47], v[96:99], v[60:63], a[44:47]\n" \
"v_mfma_f32_16x16x32_bf16 a[84:87], v[96:99], v[64:67], a[84:87]\n" \
"ds_read_b64_tr_b16 v[110:111], v1 offset:9216\n" \
"v_mfma_f32_16x16x32_bf16 a[124:127], v[96:99], v[68:71], a[124:127]\n" \
"v_mfma_f32_16x16x32_bf16 a[164:167], v[96:99], v[72:75], a[164:167]\n" \
"v_mfma_f32_16x16x32_bf16 v[128:131], v[96:99], v[76:79], v[128:131]\n" \
"v_mfma_f32_16x16x32_bf16 v[168:171], v[96:99], v[80:83], v[168:171]\n" \
"v_mfma_f32_16x16x32_bf16 v[208:211], v[96:99], v[84:87], v[208:211]\n" \
"s_waitcnt lgkmcnt(4)\n" \
"v_mfma_f32_16x16x32_bf16 a[8:11], v[100:103], v[56:59], a[8:11]\n" \
"ds_read_b64_tr_b16 v[92:93], v2 offset:8192\n" \
"v_mfma_f32_16x16x32_bf16 a[48:51], v[100:103], v[60:63], a[48:51]\n" \
"v_mfma_f32_16x16x32_bf16 a[88:91], v[100:103], v[64:67], a[88:91]\n" \
"ds_read_b64_tr_b16 v[94:95], v2 offset:9216\n" \
"v_mfma_f32_16x16x32_bf16 a[128:131], v[100:103], v[68:71], a[128:131]\n" \
"v_mfma_f32_16x16x32_bf16 a[168:171], v[100:103], v[72:75], a[168:171]\n" \
"v_mfma_f32_16x16x32_bf16 v[132:135], v[100:103], v[76:79], v[132:135]\n" \
"v_mfma_f32_16x16x32_bf16 v[172:175], v[100:103], v[80:83], v[172:175]\n" \
"v_mfma_f32_16x16x32_bf16 v[212:215], v[100:103], v[84:87], v[212:215]\n" \
"s_waitcnt lgkmcnt(4)\n" \
"v_mfma_f32_16x16x32_bf16 a[12:15], v[104:107], v[56:59], a[12:15]\n" \
"ds_read_b64_tr_b16 v[96:97], v3 offset:8192\n" \
"v_mfma_f32_16x16x32_bf16 a[52:55], v[104:107], v[60:63], a[52:55]\n" \
"v_mfma_f32_16x16x32_bf16 a[92:95], v[104:107], v[64:67], a[92:95]\n" \
"ds_read_b64_tr_b16 v[98:99], v3 offset:9216\n" \
"v_mfma_f32_16x16x32_bf16 a[132:135], v[104:107], v[68:71], a[132:135]\n" \
"v_mfma_f32_16x16x32_bf16 a[172:175], v[104:107], v[72:75], a[172:175]\n" \
"v_mfma_f32_16x16x32_bf16 v[136:139], v[104:107], v[76:79], v[136:139]\n" \
"v_mfma_f32_16x16x32_bf16 v[176:179], v[104:107], v[80:83], v[176:179]\n" \
"v_mfma_f32_16x16x32_bf16 v[216:219], v[104:107], v[84:87], v[216:219]\n" \
"s_waitcnt lgkmcnt(4)\n" \
"v_mfma_f32_16x16x32_bf16 a[16:19], v[108:111], v[56:59], a[16:19]\n" \
"ds_read_b64_tr_b16 v[100:101], v6 offset:0\n" \
"v_mfma_f32_16x16x32_bf16 a[56:59], v[108:111], v[60:63], a[56:59]\n" \
"v_mfma_f32_16x16x32_bf16 a[96:99], v[108:111], v[64:67], a[96:99]\n" \
"ds_read_b64_tr_b16 v[102:103], v7 offset:0\n" \
"v_mfma_f32_16x16x32_bf16 a[136:139], v[108:111], v[68:71], a[136:139]\n" \
"v_mfma_f32_16x16x32_bf16 a[176:179], v[108:111], v[72:75], a[176:179]\n" \
"v_mfma_f32_16x16x32_bf16 v[140:143], v[108:111], v[76:79], v[140:143]\n" \
"v_mfma_f32_16x16x32_bf16 v[180:183], v[108:111], v[80:83], v[180:183]\n" \
"v_mfma_f32_16x16x32_bf16 v[220:223], v[108:111], v[84:87], v[220:223]\n" \
"s_waitcnt lgkmcnt(4)\n" \
"v_mfma_f32_16x16x32_bf16 a[20:23], v[92:95], v[56:59], a[20:23]\n" \
"ds_read_b64_tr_b16 v[104:105], v8 offset:0\n" \
"v_mfma_f32_16x16x32_bf16 a[60:63], v[92:95], v[60:63], a[60:63]\n" \
"v_mfma_f32_16x16x32_bf16 a[100:103], v[92:95], v[64:67], a[100:103]\n" \
"ds_read_b64_tr_b16 v[106:107], v8 offset:768\n" \
"v_mfma_f32_16x16x32_bf16 a[140:143], v[92:95], v[68:71], a[140:143]\n" \
"v_mfma_f32_16x16x32_bf16 a[180:183], v[92:95], v[72:75], a[180:183]\n" \
"v_mfma_f32_16x16x32_bf16 v[144:147], v[92:95], v[76:79], v[144:147]\n" \
"v_mfma_f32_16x16x32_bf16 v[184:187], v[92:95], v[80:83], v[184:187]\n" \
"v_mfma_f32_16x16x32_bf16 v[224:227], v[92:95], v[84:87], v[224:227]\n" \
"s_waitcnt lgkmcnt(4)\n" \
"v_mfma_f32_16x16x32_bf16 a[24:27], v[96:99], v[56:59], a[24:27]\n" \
"ds_read_b64_tr_b16 v[108:109], v9 offset:0\n" \
"v_mfma_f32_16x16x32_bf16 a[64:67], v[96:99], v[60:63], a[64:67]\n" \
"v_mfma_f32_16x16x32_bf16 a[104:107], v[96:99], v[64:67], a[104:107]\n" \
"ds_read_b64_tr_b16 v[110:111], v9 offset:768\n" \
"v_mfma_f32_16x16x32_bf16 a[144:147], v[96:99], v[68:71], a[144:147]\n" \
"v_mfma_f32_16x16x32_bf16 a[184:187], v[96:99], v[72:75], a[184:187]\n" \
"v_mfma_f32_16x16x32_bf16 v[148:151], v[96:99], v[76:79], v[148:151]\n" \
"v_mfma_f32_16x16x32_bf16 v[188:191], v[96:99], v[80:83], v[188:191]\n" \
"v_mfma_f32_16x16x32_bf16 v[228:231], v[96:99], v[84:87], v[228:231]\n" \
"s_waitcnt lgkmcnt(0)\n" \
"s_waitcnt vmcnt(0)\n" \
"s_cmp_lg_u32 s49, 0\n" \
"s_cbranch_scc1 LQ9%=\n" \
"s_mov_b32 s88, 0\n" \
"LQ1%=:\n" \
"v_accvgpr_read_b32 v112, a220\n" \
"v_accvgpr_read_b32 v113, a221\n" \
"s_nop 0\n" \
"v_cmp_ne_u64 vcc, s[84:85], v[112:113]\n" \
"s_nop 4\n" \
"s_cbranch_vccz LQ2%=\n" \
"s_add_u32 s88, s88, 1\n" \
"s_cmp_gt_u32 s88, 0x40000\n" \
"s_cbranch_scc1 LQ2%=\n" \
"s_sleep 1\n" \
"global_load_dwordx2 a[220:221], v254, s[74:75] sc1\n" \
"s_waitcnt vmcnt(0)\n" \
"s_branch LQ1%=\n" \
"LQ2%=:\n" \
"s_cmp_eq_u32 s81, 0\n" \
"s_cbranch_scc1 LQ5%=\n" \
"LQ3%=:\n" \
"v_accvgpr_read_b32 v112, a222\n" \
"v_accvgpr_read_b32 v113, a223\n" \
"s_nop 0\n" \
"v_cmp_ne_u32 vcc, s84, v112\n" \
"v_cmp_ne_u32 s[88:89], s84, v113\n" \
"s_nop 1\n" \
"s_or_b64 vcc, vcc, s[88:89]\n" \
"s_mov_b64 s[90:91], vcc\n" \
"v_accvgpr_read_b32 v112, a224\n" \
"v_accvgpr_read_b32 v113, a225\n" \
"s_nop 0\n" \
"v_cmp_ne_u32 vcc, s84, v112\n" \
"v_cmp_ne_u32 s[88:89], s84, v113\n" \
"s_nop 1\n" \
"s_or_b64 vcc, vcc, s[88:89]\n" \
"s_or_b64 vcc, vcc, s[90:91]\n" \
"s_nop 4\n" \
"s_cbranch_vccz LQ4%=\n" \
"s_add_u32 s88, s88, 1\n" \
"s_cmp_gt_u32 s88, 0x40000\n" \
"s_cbranch_scc1 LQ5%=\n" \
"s_sleep 1\n" \
"v_mbcnt_lo_u32_b32 v255, -1, 0\n" \
"v_mbcnt_hi_u32_b32 v255, -1, v255\n" \
"v_lshlrev_b32 v255, 6, v255\n" \
"v_add_u32 v255, 4096, v255\n" \
"global_load_dword a222, v255, s[74:75] sc1\n" \
"v_add_u32 v255, 4096, v255\n" \
"global_load_dword a223, v255, s[74:75] sc1\n" \
"v_add_u32 v255, 4096, v255\n" \
"global_load_dword a224, v255, s[74:75] sc1\n" \
"v_add_u32 v255, 4096, v255\n" \
"global_load_dword a225, v255, s[74:75] sc1\n" \
"v_add_u32 v255, 4096, v255\n" \
"s_waitcnt vmcnt(0)\n" \
"s_branch LQ3%=\n" \
"LQ4%=:\n" \
"v_mov_b32 v114, s84\n" \
"v_mov_b32 v115, s85\n" \
"v_mov_b32 v112, 0\n" \
"s_mov_b64 exec, 1\n" \
"global_store_dwordx2 v112, v[114:115], s[74:75] sc1\n" \
"s_mov_b64 exec, -1\n" \
"LQ5%=:\n" \
"LQ9%=:\n" \
"s_barrier\n" \
"v_mfma_f32_16x16x32_bf16 a[28:31], v[100:103], v[56:59], a[28:31]\n" \
"v_add_u32 v1, 39936, v1\n" \
"ds_read_b64_tr_b16 v[92:93], v1 offset:0\n" \
"v_mfma_f32_16x16x32_bf16 a[68:71], v[100:103], v[60:63], a[68:71]\n" \
"ds_read_b64_tr_b16 v[94:95], v1 offset:1024\n" \
"v_mfma_f32_16x16x32_bf16 a[108:111], v[100:103], v[64:67], a[108:111]\n" \
"v_add_u32 v0, 39936, v0\n" \
"ds_read_b128 v[20:23], v0 offset:1024\n" \
"v_mfma_f32_16x16x32_bf16 a[148:151], v[100:103], v[68:71], a[148:151]\n" \
"v_add_u32 v4, 39936, v4\n" \
"ds_read_b128 v[24:27], v0 offset:2048\n" \
"v_mfma_f32_16x16x32_bf16 a[188:191], v[100:103], v[72:75], a[188:191]\n" \
"ds_read_b128 v[28:31], v0 offset:3072\n" \
"v_add_u32 v5, 39936, v5\n" \
"v_mfma_f32_16x16x32_bf16 v[152:155], v[100:103], v[76:79], v[152:155]\n" \
"ds_read_b128 v[32:35], v0 offset:4096\n" \
"v_mfma_f32_16x16x32_bf16 v[192:195], v[100:103], v[80:83], v[192:195]\n" \
"ds_read_b128 v[36:39], v0 offset:5120\n" \
"v_add_u32 v6, 39936, v6\n" \
"v_mfma_f32_16x16x32_bf16 v[232:235], v[100:103], v[84:87], v[232:235]\n" \
"ds_read_b128 v[40:43], v0 offset:6144\n" \
"v_mfma_f32_16x16x32_bf16 a[32:35], v[104:107], v[56:59], a[32:35]\n" \
"ds_read_b128 v[44:47], v0 offset:7168\n" \
"v_add_u32 v7, 39936, v7\n" \
"v_mfma_f32_16x16x32_bf16 a[72:75], v[104:107], v[60:63], a[72:75]\n" \
"ds_read_b128 v[48:51], v0 offset:8192\n" \
"v_mfma_f32_16x16x32_bf16 a[112:115], v[104:107], v[64:67], a[112:115]\n" \
"v_add_u32 v18, 39936, v18\n" \
"ds_read_b128 v[52:55], v18 offset:0\n" \
"v_mfma_f32_16x16x32_bf16 a[152:155], v[104:107], v[68:71], a[152:155]\n" \
"v_add_u32 v8, 39936, v8\n" \
"v_add_u32 v2, 39936, v2\n" \
"v_mfma_f32_16x16x32_bf16 a[192:195], v[104:107], v[72:75], a[192:195]\n" \
"ds_read_b64_tr_b16 v[96:97], v2 offset:0\n" \
"ds_read_b64_tr_b16 v[98:99], v2 offset:1024\n" \
"v_mfma_f32_16x16x32_bf16 v[156:159], v[104:107], v[76:79], v[156:159]\n" \
"v_add_u32 v9, 39936, v9\n" \
"v_add_u32 v3, 39936, v3\n" \
"v_mfma_f32_16x16x32_bf16 v[196:199], v[104:107], v[80:83], v[196:199]\n" \
"ds_read_b64_tr_b16 v[100:101], v3 offset:0\n" \
"ds_read_b64_tr_b16 v[102:103], v3 offset:1024\n" \
"v_mfma_f32_16x16x32_bf16 v[236:239], v[104:107], v[84:87], v[236:239]\n" \
"v_mfma_f32_16x16x32_bf16 v[244:247], v[104:107], v[88:91], v[244:247]\n" \
"v_mfma_f32_16x16x32_bf16 a[36:39], v[108:111], v[56:59], a[36:39]\n" \
"v_mfma_f32_16x16x32_bf16 a[76:79], v[108:111], v[60:63], a[76:79]\n" \
"v_mfma_f32_16x16x32_bf16 a[116:119], v[108:111], v[64:67], a[116:119]\n" \
"v_mfma_f32_16x16x32_bf16 a[156:159], v[108:111], v[68:71], a[156:159]\n" \
"v_mfma_f32_16x16x32_bf16 v[120:123], v[108:111], v[72:75], v[120:123]\n" \
"v_mfma_f32_16x16x32_bf16 v[160:163], v[108:111], v[76:79], v[160:163]\n" \
"v_mfma_f32_16x16x32_bf16 v[200:203], v[108:111], v[80:83], v[200:203]\n" \
"v_mfma_f32_16x16x32_bf16 v[240:243], v[108:111], v[84:87], v[240:243]\n" \
"s_waitcnt lgkmcnt(9)\n" \
"v_mfma_f32_16x16x32_bf16 a[0:3], v[92:95], v[20:23], a[0:3]\n" \
"ds_read_b64_tr_b16 v[104:105], v4 offset:0\n" \
"v_mbcnt_lo_u32_b32 v10, -1, 0\n" \
"v_mfma_f32_16x16x32_bf16 a[40:43], v[92:95], v[24:27], a[40:43]\n" \
"v_mfma_f32_16x16x32_bf16 a[80:83], v[92:95], v[28:31], a[80:83]\n" \
"ds_read_b64_tr_b16 v[106:107], v5 offset:0\n" \
"v_mbcnt_hi_u32_b32 v10, -1, v10\n" \
"v_mfma_f32_16x16x32_bf16 a[120:123], v[92:95], v[32:35], a[120:123]\n" \
"s_waitcnt lgkmcnt(7)\n" \
"v_mfma_f32_16x16x32_bf16 a[160:163], v[92:95], v[36:39], a[160:163]\n" \
"v_lshlrev_b32 v11, 4, v10\n" \
"v_mfma_f32_16x16x32_bf16 v[124:127], v[92:95], v[40:43], v[124:127]\n" \
"v_mfma_f32_16x16x32_bf16 v[164:167], v[92:95], v[44:47], v[164:167]\n" \
"s_lshl_b32 s88, s65, 10\n" \
"s_add_u32 s88, s88, s76\n" \
"v_add_u32 v11, s88, v11\n" \
"v_mfma_f32_16x16x32_bf16 v[204:207], v[92:95], v[48:51], v[204:207]\n" \
"s_waitcnt lgkmcnt(4)\n" \
"v_mfma_f32_16x16x32_bf16 a[4:7], v[96:99], v[20:23], a[4:7]\n" \
"ds_read_b64_tr_b16 v[108:109], v1 offset:8192\n" \
"v_and_b32 v12, 15, v10\n" \
"v_mfma_f32_16x16x32_bf16 a[44:47], v[96:99], v[24:27], a[44:47]\n" \
"v_mfma_f32_16x16x32_bf16 a[84:87], v[96:99], v[28:31], a[84:87]\n" \
"ds_read_b64_tr_b16 v[110:111], v1 offset:9216\n" \
"v_lshlrev_b32 v12, 2, v12\n" \
"v_mfma_f32_16x16x32_bf16 a[124:127], v[96:99], v[32:35], a[124:127]\n" \
"v_mfma_f32_16x16x32_bf16 a[164:167], v[96:99], v[36:39], a[164:167]\n" \
"v_add_u32 v12, s83, v12\n" \
"v_mfma_f32_16x16x32_bf16 v[128:131], v[96:99], v[40:43], v[128:131]\n" \
"v_mfma_f32_16x16x32_bf16 v[168:171], v[96:99], v[44:47], v[168:171]\n" \
"s_lshl_b32 s88, s66, 9\n" \
"v_add_u32 v13, s88, v12\n" \
"v_mfma_f32_16x16x32_bf16 v[208:211], v[96:99], v[48:51], v[208:211]\n" \
"s_waitcnt lgkmcnt(4)\n" \
"v_mfma_f32_16x16x32_bf16 a[8:11], v[100:103], v[20:23], a[8:11]\n" \
"ds_read_b64_tr_b16 v[92:93], v2 offset:8192\n" \
"v_add_u32 v12, s77, v12\n" \
"v_mfma_f32_16x16x32_bf16 a[48:51], v[100:103], v[24:27], a[48:51]\n" \
"v_mfma_f32_16x16x32_bf16 a[88:91], v[100:103], v[28:31], a[88:91]\n" \
"ds_read_b64_tr_b16 v[94:95], v2 offset:9216\n" \
"v_mfma_f32_16x16x32_bf16 a[128:131], v[100:103], v[32:35], a[128:131]\n" \
"v_mfma_f32_16x16x32_bf16 a[168:171], v[100:103], v[36:39], a[168:171]\n" \
"global_load_dwordx4 a[204:207], v11, s[74:75] offset:0 sc1\n" \
"v_mfma_f32_16x16x32_bf16 v[132:135], v[100:103], v[40:43], v[132:135]\n" \
"v_mfma_f32_16x16x32_bf16 v[172:175], v[100:103], v[44:47], v[172:175]\n" \
"v_mfma_f32_16x16x32_bf16 v[212:215], v[100:103], v[48:51], v[212:215]\n" \
"global_load_dwordx4 a[208:211], v11, s[74:75] offset:2048 sc1\n" \
"s_waitcnt lgkmcnt(4)\n" \
"v_mfma_f32_16x16x32_bf16 a[12:15], v[104:107], v[20:23], a[12:15]\n" \
"ds_read_b64_tr_b16 v[96:97], v3 offset:8192\n" \
"v_mfma_f32_16x16x32_bf16 a[52:55], v[104:107], v[24:27], a[52:55]\n" \
"v_add_u32 v11, 4096, v11\n" \
"v_mfma_f32_16x16x32_bf16 a[92:95], v[104:107], v[28:31], a[92:95]\n" \
"ds_read_b64_tr_b16 v[98:99], v3 offset:9216\n" \
"global_load_dwordx4 a[212:215], v11, s[74:75] offset:0 sc1\n" \
"v_mfma_f32_16x16x32_bf16 a[132:135], v[104:107], v[32:35], a[132:135]\n" \
"v_mfma_f32_16x16x32_bf16 a[172:175], v[104:107], v[36:39], a[172:175]\n" \
"v_mfma_f32_16x16x32_bf16 v[136:139], v[104:107], v[40:43], v[136:139]\n" \
"global_load_dwordx4 a[216:219], v11, s[74:75] offset:2048 sc1\n" \
"v_mfma_f32_16x16x32_bf16 v[176:179], v[104:107], v[44:47], v[176:179]\n" \
"v_mfma_f32_16x16x32_bf16 v[216:219], v[104:107], v[48:51], v[216:219]\n" \
"v_add_u32 v11, 4096, v11\n" \
"s_waitcnt lgkmcnt(4)\n" \
"v_mfma_f32_16x16x32_bf16 a[16:19], v[108:111], v[20:23], a[16:19]\n" \
"ds_read_b64_tr_b16 v[100:101], v6 offset:0\n" \
"global_load_dwordx4 a[220:223], v11, s[74:75] offset:0 sc1\n" \
"v_mfma_f32_16x16x32_bf16 a[56:59], v[108:111], v[24:27], a[56:59]\n" \
"v_mfma_f32_16x16x32_bf16 a[96:99], v[108:111], v[28:31], a[96:99]\n" \
"ds_read_b64_tr_b16 v[102:103], v7 offset:0\n" \
"v_mfma_f32_16x16x32_bf16 a[136:139], v[108:111], v[32:35], a[136:139]\n" \
"global_load_dwordx4 a[224:227], v11, s[74:75] offset:2048 sc1\n" \
"v_mfma_f32_16x16x32_bf16 a[176:179], v[108:111], v[36:39], a[176:179]\n" \
"v_mfma_f32_16x16x32_bf16 v[140:143], v[108:111], v[40:43], v[140:143]\n" \
"v_add_u32 v11, 4096, v11\n" \
"v_mfma_f32_16x16x32_bf16 v[180:183], v[108:111], v[44:47], v[180:183]\n" \
"global_load_dwordx4 a[228:231], v11, s[74:75] offset:0 sc1\n" \
"global_load_dword a236, v12, s[74:75] sc1\n" \
"v_mfma_f32_16x16x32_bf16 v[220:223], v[108:111], v[48:51], v[220:223]\n" \
"s_waitcnt lgkmcnt(4)\n" \
"v_mfma_f32_16x16x32_bf16 a[20:23], v[92:95], v[20:23], a[20:23]\n" \
"ds_read_b64_tr_b16 v[104:105], v8 offset:0\n" \
"global_load_dword a237, v13, s[74:75] offset:64 sc1\n" \
"v_mfma_f32_16x16x32_bf16 a[60:63], v[92:95], v[24:27], a[60:63]\n" \
"global_load_dwordx4 a[232:235], v11, s[74:75] offset:2048 sc1\n" \
"v_mfma_f32_16x16x32_bf16 a[100:103], v[92:95], v[28:31], a[100:103]\n" \
"ds_read_b64_tr_b16 v[106:107], v8 offset:768\n" \
"global_load_dword a238, v13, s[74:75] offset:128 sc1\n" \
"v_mfma_f32_16x16x32_bf16 a[140:143], v[92:95], v[32:35], a[140:143]\n" \
"v_mfma_f32_16x16x32_bf16 a[180:183], v[92:95], v[36:39], a[180:183]\n" \
"global_load_dword a239, v13, s[74:75] offset:192 sc1\n" \
"v_mfma_f32_16x16x32_bf16 v[144:147], v[92:95], v[40:43], v[144:147]\n" \
"v_mfma_f32_16x16x32_bf16 v[184:187], v[92:95], v[44:47], v[184:187]\n" \
"global_load_dword a240, v13, s[74:75] offset:256 sc1\n" \
"v_mfma_f32_16x16x32_bf16 v[224:227], v[92:95], v[48:51], v[224:227]\n" \
"s_waitcnt lgkmcnt(4)\n" \
"v_mfma_f32_16x16x32_bf16 a[24:27], v[96:99], v[20:23], a[24:27]\n" \
"ds_read_b64_tr_b16 v[108:109], v9 offset:0\n" \
"global_load_dword a241, v13, s[74:75] offset:320 sc1\n" \
"v_mfma_f32_16x16x32_bf16 a[64:67], v[96:99], v[24:27], a[64:67]\n" \
"v_mfma_f32_16x16x32_bf16 a[104:107], v[96:99], v[28:31], a[104:107]\n" \
"ds_read_b64_tr_b16 v[110:111], v9 offset:768\n" \
"global_load_dword a242, v13, s[74:75] offset:384 sc1\n" \
"v_mfma_f32_16x16x32_bf16 a[144:147], v[96:99], v[32:35], a[144:147]\n" \
"v_mfma_f32_16x16x32_bf16 a[184:187], v[96:99], v[36:39], a[184:187]\n" \
"global_load_dword a243, v13, s[74:75] offset:448 sc1\n" \
"v_mfma_f32_16x16x32_bf16 v[148:151], v[96:99], v[40:43], v[148:151]\n" \
"v_mfma_f32_16x16x32_bf16 v[188:191], v[96:99], v[44:47], v[188:191]\n" \
"global_load_dword a244, v13, s[74:75] offset:512 sc1\n" \
"v_mfma_f32_16x16x32_bf16 v[228:231], v[96:99], v[48:51], v[228:231]\n" \
"s_waitcnt lgkmcnt(0)\n" \
"s_barrier\n" \
"v_mfma_f32_16x16x32_bf16 a[28:31], v[100:103], v[20:23], a[28:31]\n" \
"v_add_u32 v1, 39936, v1\n" \
"ds_read_b64_tr_b16 v[92:93], v1 offset:0\n" \
"v_mfma_f32_16x16x32_bf16 a[68:71], v[100:103], v[24:27], a[68:71]\n" \
"ds_read_b64_tr_b16 v[94:95], v1 offset:1024\n" \
"v_mfma_f32_16x16x32_bf16 a[108:111], v[100:103], v[28:31], a[108:111]\n" \
"v_add_u32 v0, 39936, v0\n" \
"ds_read_b128 v[56:59], v0 offset:1024\n" \
"v_mfma_f32_16x16x32_bf16 a[148:151], v[100:103], v[32:35], a[148:151]\n" \
"v_add_u32 v4, 39936, v4\n" \
"global_load_dword v255, v19, s[60:61]\n" \
"v_mfma_f32_16x16x32_bf16 a[188:191], v[100:103], v[36:39], a[188:191]\n" \
"ds_read_b128 v[60:63], v0 offset:2048\n" \
"ds_read_b128 v[64:67], v0 offset:3072\n" \
"v_mfma_f32_16x16x32_bf16 v[152:155], v[100:103], v[40:43], v[152:155]\n" \
"v_add_u32 v5, 39936, v5\n" \
"v_mfma_f32_16x16x32_bf16 v[192:195], v[100:103], v[44:47], v[192:195]\n" \
"s_add_u32 s60, s60, s64\n" \
"s_addc_u32 s61, s61, 0\n" \
"v_mfma_f32_16x16x32_bf16 v[232:235], v[100:103], v[48:51], v[232:235]\n" \
"ds_read_b128 v[68:71], v0 offset:4096\n" \
"v_mfma_f32_16x16x32_bf16 a[32:35], v[104:107], v[20:23], a[32:35]\n" \
"ds_read_b128 v[72:75], v0 offset:5120\n" \
"v_add_u32 v6, 39936, v6\n" \
"v_mfma_f32_16x16x32_bf16 a[72:75], v[104:107], v[24:27], a[72:75]\n" \
"ds_read_b128 v[76:79], v0 offset:6144\n" \
"ds_read_b128 v[80:83], v0 offset:7168\n" \
"v_mfma_f32_16x16x32_bf16 a[112:115], v[104:107], v[28:31], a[112:115]\n" \
"v_add_u32 v7, 39936, v7\n" \
"ds_read_b128 v[84:87], v0 offset:8192\n" \
"v_mfma_f32_16x16x32_bf16 a[152:155], v[104:107], v[32:35], a[152:155]\n" \
"v_add_u32 v18, 39936, v18\n" \
"ds_read_b128 v[88:91], v18 offset:0\n" \
"v_mfma_f32_16x16x32_bf16 a[192:195], v[104:107], v[36:39], a[192:195]\n" \
"v_add_u32 v8, 39936, v8\n" \
"v_add_u32 v2, 39936, v2\n" \
"v_mfma_f32_16x16x32_bf16 v[156:159], v[104:107], v[40:43], v[156:159]\n" \
"ds_read_b64_tr_b16 v[96:97], v2 offset:0\n" \
"ds_read_b64_tr_b16 v[98:99], v2 offset:1024\n" \
"v_mfma_f32_16x16x32_bf16 v[196:199], v[104:107], v[44:47], v[196:199]\n" \
"v_add_u32 v9, 39936, v9\n" \
"v_add_u32 v3, 39936, v3\n" \
"v_mfma_f32_16x16x32_bf16 v[236:239], v[104:107], v[48:51], v[236:239]\n" \
"ds_read_b64_tr_b16 v[100:101], v3 offset:0\n" \
"ds_read_b64_tr_b16 v[102:103], v3 offset:1024\n" \
"v_mfma_f32_16x16x32_bf16 v[244:247], v[104:107], v[52:55], v[244:247]\n" \
"v_mfma_f32_16x16x32_bf16 a[36:39], v[108:111], v[20:23], a[36:39]\n" \
"v_mfma_f32_16x16x32_bf16 a[76:79], v[108:111], v[24:27], a[76:79]\n" \
"global_load_dword v255, v19, s[62:63]\n" \
"v_mfma_f32_16x16x32_bf16 a[116:119], v[108:111], v[28:31], a[116:119]\n" \
"v_mfma_f32_16x16x32_bf16 a[156:159], v[108:111], v[32:35], a[156:159]\n" \
"v_mfma_f32_16x16x32_bf16 v[120:123], v[108:111], v[36:39], v[120:123]\n" \
"v_mfma_f32_16x16x32_bf16 v[160:163], v[108:111], v[40:43], v[160:163]\n" \
"v_mfma_f32_16x16x32_bf16 v[200:203], v[108:111], v[44:47], v[200:203]\n" \
"v_mfma_f32_16x16x32_bf16 v[240:243], v[108:111], v[48:51], v[240:243]\n" \
"s_waitcnt lgkmcnt(9)\n" \
"v_mfma_f32_16x16x32_bf16 a[0:3], v[92:95], v[56:59], a[0:3]\n" \
"ds_read_b64_tr_b16 v[104:105], v4 offset:0\n" \
"v_mfma_f32_16x16x32_bf16 a[40:43], v[92:95], v[60:63], a[40:43]\n" \
"v_mfma_f32_16x16x32_bf16 a[80:83], v[92:95], v[64:67], a[80:83]\n" \
"ds_read_b64_tr_b16 v[106:107], v5 offset:0\n" \
"v_mfma_f32_16x16x32_bf16 a[120:123], v[92:95], v[68:71], a[120:123]\n" \
"s_waitcnt lgkmcnt(7)\n" \
"v_mfma_f32_16x16x32_bf16 a[160:163], v[92:95], v[72:75], a[160:163]\n" \
"v_mfma_f32_16x16x32_bf16 v[124:127], v[92:95], v[76:79], v[124:127]\n" \
"v_mfma_f32_16x16x32_bf16 v[164:167], v[92:95], v[80:83], v[164:167]\n" \
"v_mfma_f32_16x16x32_bf16 v[204:207], v[92:95], v[84:87], v[204:207]\n" \
"s_waitcnt lgkmcnt(4)\n" \
"v_mfma_f32_16x16x32_bf16 a[4:7], v[96:99], v[56:59], a[4:7]\n" \
"ds_read_b64_tr_b16 v[108:109], v1 offset:8192\n" \
"v_mfma_f32_16x16x32_bf16 a[44:47], v[96:99], v[60:63], a[44:47]\n" \
"v_mfma_f32_16x16x32_bf16 a[84:87], v[96:99], v[64:67], a[84:87]\n" \
"ds_read_b64_tr_b16 v[110:111], v1 offset:9216\n" \
"global_load_dword v255, v19, s[60:61]\n" \
"v_mfma_f32_16x16x32_bf16 a[124:127], v[96:99], v[68:71], a[124:127]\n" \
"v_mfma_f32_16x16x32_bf16 a[164:167], v[96:99], v[72:75], a[164:167]\n" \
"v_mfma_f32_16x16x32_bf16 v[128:131], v[96:99], v[76:79], v[128:131]\n" \
"v_mfma_f32_16x16x32_bf16 v[168:171], v[96:99], v[80:83], v[168:171]\n" \
"v_mfma_f32_16x16x32_bf16 v[208:211], v[96:99], v[84:87], v[208:211]\n" \
"s_waitcnt lgkmcnt(4)\n" \
"v_mfma_f32_16x16x32_bf16 a[8:11], v[100:103], v[56:59], a[8:11]\n" \
"ds_read_b64_tr_b16 v[92:93], v2 offset:8192\n" \
"v_mfma_f32_16x16x32_bf16 a[48:51], v[100:103], v[60:63], a[48:51]\n" \
"v_mfma_f32_16x16x32_bf16 a[88:91], v[100:103], v[64:67], a[88:91]\n" \
"ds_read_b64_tr_b16 v[94:95], v2 offset:9216\n" \
"v_mfma_f32_16x16x32_bf16 a[128:131], v[100:103], v[68:71], a[128:131]\n" \
"v_mfma_f32_16x16x32_bf16 a[168:171], v[100:103], v[72:75], a[168:171]\n" \
"v_mfma_f32_16x16x32_bf16 v[132:135], v[100:103], v[76:79], v[132:135]\n" \
"v_mfma_f32_16x16x32_bf16 v[172:175], v[100:103], v[80:83], v[172:175]\n" \
"v_mfma_f32_16x16x32_bf16 v[212:215], v[100:103], v[84:87], v[212:215]\n" \
"s_waitcnt lgkmcnt(4)\n" \
"v_mfma_f32_16x16x32_bf16 a[12:15], v[104:107], v[56:59], a[12:15]\n" \
"ds_read_b64_tr_b16 v[96:97], v3 offset:8192\n" \
"v_mfma_f32_16x16x32_bf16 a[52:55], v[104:107], v[60:63], a[52:55]\n" \
"v_mfma_f32_16x16x32_bf16 a[92:95], v[104:107], v[64:67], a[92:95]\n" \
"ds_read_b64_tr_b16 v[98:99], v3 offset:9216\n" \
"v_mfma_f32_16x16x32_bf16 a[132:135], v[104:107], v[68:71], a[132:135]\n" \
"v_mfma_f32_16x16x32_bf16 a[172:175], v[104:107], v[72:75], a[172:175]\n" \
"v_mfma_f32_16x16x32_bf16 v[136:139], v[104:107], v[76:79], v[136:139]\n" \
"v_mfma_f32_16x16x32_bf16 v[176:179], v[104:107], v[80:83], v[176:179]\n" \
"v_mfma_f32_16x16x32_bf16 v[216:219], v[104:107], v[84:87], v[216:219]\n" \
"s_waitcnt lgkmcnt(4)\n" \
"v_mfma_f32_16x16x32_bf16 a[16:19], v[108:111], v[56:59], a[16:19]\n" \
"ds_read_b64_tr_b16 v[100:101], v6 offset:0\n" \
"v_mfma_f32_16x16x32_bf16 a[56:59], v[108:111], v[60:63], a[56:59]\n" \
"v_mfma_f32_16x16x32_bf16 a[96:99], v[108:111], v[64:67], a[96:99]\n" \
"ds_read_b64_tr_b16 v[102:103], v7 offset:0\n" \
"v_mfma_f32_16x16x32_bf16 a[136:139], v[108:111], v[68:71], a[136:139]\n" \
"v_mfma_f32_16x16x32_bf16 a[176:179], v[108:111], v[72:75], a[176:179]\n" \
"v_mfma_f32_16x16x32_bf16 v[140:143], v[108:111], v[76:79], v[140:143]\n" \
"v_mfma_f32_16x16x32_bf16 v[180:183], v[108:111], v[80:83], v[180:183]\n" \
"v_mfma_f32_16x16x32_bf16 v[220:223], v[108:111], v[84:87], v[220:223]\n" \
"s_waitcnt lgkmcnt(4)\n" \
"v_mfma_f32_16x16x32_bf16 a[20:23], v[92:95], v[56:59], a[20:23]\n" \
"ds_read_b64_tr_b16 v[104:105], v8 offset:0\n" \
"v_mfma_f32_16x16x32_bf16 a[60:63], v[92:95], v[60:63], a[60:63]\n" \
"v_mfma_f32_16x16x32_bf16 a[100:103], v[92:95], v[64:67], a[100:103]\n" \
"ds_read_b64_tr_b16 v[106:107], v8 offset:768\n" \
"v_mfma_f32_16x16x32_bf16 a[140:143], v[92:95], v[68:71], a[140:143]\n" \
"v_mfma_f32_16x16x32_bf16 a[180:183], v[92:95], v[72:75], a[180:183]\n" \
"v_mfma_f32_16x16x32_bf16 v[144:147], v[92:95], v[76:79], v[144:147]\n" \
"v_mfma_f32_16x16x32_bf16 v[184:187], v[92:95], v[80:83], v[184:187]\n" \
"v_mfma_f32_16x16x32_bf16 v[224:227], v[92:95], v[84:87], v[224:227]\n" \
"s_waitcnt lgkmcnt(4)\n" \
"v_mfma_f32_16x16x32_bf16 a[24:27], v[96:99], v[56:59], a[24:27]\n" \
"ds_read_b64_tr_b16 v[108:109], v9 offset:0\n" \
"v_mfma_f32_16x16x32_bf16 a[64:67], v[96:99], v[60:63], a[64:67]\n" \
"v_mfma_f32_16x16x32_bf16 a[104:107], v[96:99], v[64:67], a[104:107]\n" \
"ds_read_b64_tr_b16 v[110:111], v9 offset:768\n" \
"v_mfma_f32_16x16x32_bf16 a[144:147], v[96:99], v[68:71], a[144:147]\n" \
"v_mfma_f32_16x16x32_bf16 a[184:187], v[96:99], v[72:75], a[184:187]\n" \
"v_mfma_f32_16x16x32_bf16 v[148:151], v[96:99], v[76:79], v[148:151]\n" \
"v_mfma_f32_16x16x32_bf16 v[188:191], v[96:99], v[80:83], v[188:191]\n" \
"v_mfma_f32_16x16x32_bf16 v[228:231], v[96:99], v[84:87], v[228:231]\n" \
"s_waitcnt lgkmcnt(4)\n" \
"v_mfma_f32_16x16x32_bf16 a[28:31], v[100:103], v[56:59], a[28:31]\n" \
"v_mfma_f32_16x16x32_bf16 a[68:71], v[100:103], v[60:63], a[68:71]\n" \
"v_mfma_f32_16x16x32_bf16 a[108:111], v[100:103], v[64:67], a[108:111]\n" \
"v_mfma_f32_16x16x32_bf16 a[148:151], v[100:103], v[68:71], a[148:151]\n" \
"v_mfma_f32_16x16x32_bf16 a[188:191], v[100:103], v[72:75], a[188:191]\n" \
"v_mfma_f32_16x16x32_bf16 v[152:155], v[100:103], v[76:79], v[152:155]\n" \
"v_mfma_f32_16x16x32_bf16 v[192:195], v[100:103], v[80:83], v[192:195]\n" \
"v_mfma_f32_16x16x32_bf16 v[232:235], v[100:103], v[84:87], v[232:235]\n" \
"s_waitcnt lgkmcnt(2)\n" \
"v_mfma_f32_16x16x32_bf16 a[32:35], v[104:107], v[56:59], a[32:35]\n" \
"v_mfma_f32_16x16x32_bf16 a[72:75], v[104:107], v[60:63], a[72:75]\n" \
"v_mfma_f32_16x16x32_bf16 a[112:115], v[104:107], v[64:67], a[112:115]\n" \
"v_mfma_f32_16x16x32_bf16 a[152:155], v[104:107], v[68:71], a[152:155]\n" \
"v_mfma_f32_16x16x32_bf16 a[192:195], v[104:107], v[72:75], a[192:195]\n" \
"v_mfma_f32_16x16x32_bf16 v[156:159], v[104:107], v[76:79], v[156:159]\n" \
"v_mfma_f32_16x16x32_bf16 v[196:199], v[104:107], v[80:83], v[196:199]\n" \
"v_mfma_f32_16x16x32_bf16 v[236:239], v[104:107], v[84:87], v[236:239]\n" \
"v_mfma_f32_16x16x32_bf16 v[244:247], v[104:107], v[88:91], v[244:247]\n" \
"s_waitcnt lgkmcnt(0)\n" \
"v_mfma_f32_16x16x32_bf16 a[36:39], v[108:111], v[56:59], a[36:39]\n" \
"v_mfma_f32_16x16x32_bf16 a[76:79], v[108:111], v[60:63], a[76:79]\n" \
"v_mfma_f32_16x16x32_bf16 a[116:119], v[108:111], v[64:67], a[116:119]\n" \
"v_mfma_f32_16x16x32_bf16 a[156:159], v[108:111], v[68:71], a[156:159]\n" \
"v_mfma_f32_16x16x32_bf16 v[120:123], v[108:111], v[72:75], v[120:123]\n" \
"v_mfma_f32_16x16x32_bf16 v[160:163], v[108:111], v[76:79], v[160:163]\n" \
"v_mfma_f32_16x16x32_bf16 v[200:203], v[108:111], v[80:83], v[200:203]\n" \
"v_mfma_f32_16x16x32_bf16 v[240:243], v[108:111], v[84:87], v[240:243]\n" \
"s_nop 7\n" \
"s_waitcnt vmcnt(3) lgkmcnt(0)\n" \
"s_barrier\n" \
K1_STAMP2 \
"s_mov_b32 s94, 0xbfb8aa3b\n" \
"v_mbcnt_lo_u32_b32 v8, -1, 0\n" \
"v_mbcnt_hi_u32_b32 v8, -1, v8\n" \
"v_accvgpr_read_b32 v60, a204\n" \
"v_accvgpr_read_b32 v61, a205\n" \
"v_accvgpr_read_b32 v62, a206\n" \
"v_accvgpr_read_b32 v63, a207\n" \
"v_accvgpr_read_b32 v64, a208\n" \
"v_accvgpr_read_b32 v65, a209\n" \
"v_accvgpr_read_b32 v66, a210\n" \
"v_accvgpr_read_b32 v67, a211\n" \
"v_accvgpr_read_b32 v68, a212\n" \
"v_accvgpr_read_b32 v69, a213\n" \
"v_accvgpr_read_b32 v70, a214\n" \
"v_accvgpr_read_b32 v71, a215\n" \
"v_accvgpr_read_b32 v72, a216\n" \
"v_accvgpr_read_b32 v73, a217\n" \
"v_accvgpr_read_b32 v74, a218\n" \
"v_accvgpr_read_b32 v75, a219\n" \
"v_accvgpr_read_b32 v76, a220\n" \
"v_accvgpr_read_b32 v77, a221\n" \
"v_accvgpr_read_b32 v78, a222\n" \
"v_accvgpr_read_b32 v79, a223\n" \
"v_accvgpr_read_b32 v80, a224\n" \
"v_accvgpr_read_b32 v81, a225\n" \
"v_accvgpr_read_b32 v82, a226\n" \
"v_accvgpr_read_b32 v83, a227\n" \
"v_accvgpr_read_b32 v84, a228\n" \
"v_accvgpr_read_b32 v85, a229\n" \
"v_accvgpr_read_b32 v86, a230\n" \
"v_accvgpr_read_b32 v87, a231\n" \
"v_accvgpr_read_b32 v88, a232\n" \
"v_accvgpr_read_b32 v89, a233\n" \
"v_accvgpr_read_b32 v90, a234\n" \
"v_accvgpr_read_b32 v91, a235\n" \
"v_accvgpr_read_b32 v100, a236\n" \
"v_accvgpr_read_b32 v101, a237\n" \
"v_accvgpr_read_b32 v102, a238\n" \
"v_accvgpr_read_b32 v103, a239\n" \
"v_accvgpr_read_b32 v104, a240\n" \
"v_accvgpr_read_b32 v105, a241\n" \
"v_accvgpr_read_b32 v106, a242\n" \
"v_accvgpr_read_b32 v107, a243\n" \
"v_accvgpr_read_b32 v108, a244\n" \
"v_pk_add_f32 v[60:61], v[60:61], v[64:65]\n" \
"v_pk_add_f32 v[62:63], v[62:63], v[66:67]\n" \
"v_pk_add_f32 v[60:61], v[60:61], v[68:69]\n" \
"v_pk_add_f32 v[62:63], v[62:63], v[70:71]\n" \
"v_pk_add_f32 v[60:61], v[60:61], v[72:73]\n" \
"v_pk_add_f32 v[62:63], v[62:63], v[74:75]\n" \
"v_pk_add_f32 v[60:61], v[60:61], v[76:77]\n" \
"v_pk_add_f32 v[62:63], v[62:63], v[78:79]\n" \
"v_pk_add_f32 v[60:61], v[60:61], v[80:81]\n" \
"v_pk_add_f32 v[62:63], v[62:63], v[82:83]\n" \
"v_pk_add_f32 v[60:61], v[60:61], v[84:85]\n" \
"v_pk_add_f32 v[62:63], v[62:63], v[86:87]\n" \
"v_pk_add_f32 v[60:61], v[60:61], v[88:89]\n" \
"v_pk_add_f32 v[62:63], v[62:63], v[90:91]\n" \
"v_lshrrev_b32 v9, 5, v8\n" \
"v_mul_u32_u24 v9, 320, v9\n" \
"v_bfe_u32 v10, v8, 4, 1\n" \
"v_lshl_add_u32 v9, v10, 8, v9\n" \
"v_and_b32 v10, 15, v8\n" \
"v_lshl_add_u32 v9, v10, 2, v9\n" \
"s_lshr_b32 s88, s49, 12\n" \
"s_mul_i32 s88, s88, 640\n" \
"s_add_u32 s88, s88, 2048\n" \
"v_add_u32 v9, s88, v9\n" \
"ds_write_b32 v9, v60\n" \
"s_mov_b32 exec_lo, 0x0000ffff\n" \
"s_mov_b32 exec_hi, 0x0000ffff\n" \
"ds_write_b32 v9, v61 offset:64\n" \
"ds_write_b32 v9, v62 offset:128\n" \
"ds_write_b32 v9, v63 offset:192\n" \
"s_mov_b64 exec, -1\n" \
"v_lshrrev_b32 v10, 4, v8\n" \
"v_lshlrev_b32 v10, 4, v10\n" \
"v_add_u32 v10, s88, v10\n" \
"s_lshl_b32 s89, s66, 6\n" \
"v_add_u32 v13, s89, v10\n" \
"v_subrev_u32 v9, s89, v10\n" \
"v_mov_b32 v60, v100\n" \
"v_mov_b32 v61, v101\n" \
"v_mov_b32 v62, v102\n" \
"v_mov_b32 v63, v103\n" \
"v_mov_b32 v64, v104\n" \
"v_mov_b32 v65, v105\n" \
"v_mov_b32 v66, v106\n" \
"v_mov_b32 v67, v107\n" \
"v_mov_b32 v68, v108\n" \
"ds_read_b128 v[20:23], v10 offset:0\n" \
"ds_read_b128 v[24:27], v10 offset:64\n" \
"ds_read_b128 v[28:31], v10 offset:128\n" \
"ds_read_b128 v[32:35], v10 offset:192\n" \
"ds_read_b128 v[36:39], v10 offset:256\n" \
"ds_read_b128 v[40:43], v10 offset:320\n" \
"ds_read_b128 v[44:47], v10 offset:384\n" \
"ds_read_b128 v[48:51], v10 offset:448\n" \
"ds_read_b128 v[52:55], v9 offset:576\n" \
"ds_read_b128 v[56:59], v13 offset:512\n" \
"s_waitcnt lgkmcnt(0)\n" \
"v_lshlrev_b32 v70, 16, v60\n" \
"v_and_b32 v74, 0xffff0000, v60\n" \
"v_rcp_f32 v72, v74\n" \
"v_mul_f32 v74, v74, v74\n" \
"v_mul_f32 v76, s94, v72\n" \
"s_nop 0\n" \
"v_pk_fma_f32 v[244:245], v[52:53], v[70:71], v[244:245] op_sel_hi:[1,0,1] neg_lo:[0,1,0] neg_hi:[0,1,0]\n" \
"v_pk_fma_f32 v[246:247], v[54:55], v[70:71], v[246:247] op_sel_hi:[1,0,1] neg_lo:[0,1,0] neg_hi:[0,1,0]\n" \
"v_pk_mul_f32 v[244:245], v[244:245], v[72:73] op_sel_hi:[1,0]\n" \
"v_pk_mul_f32 v[246:247], v[246:247], v[72:73] op_sel_hi:[1,0]\n" \
"v_cvt_pk_bf16_f32 v86, v244, v245\n" \
"v_cvt_pk_bf16_f32 v87, v246, v247\n" \
"ds_write_b64 v250, v[86:87] offset:0\n" \
"v_lshlrev_b32 v70, 16, v61\n" \
"v_lshlrev_b32 v78, 16, v62\n" \
"v_and_b32 v74, 0xffff0000, v61\n" \
"v_and_b32 v82, 0xffff0000, v62\n" \
"v_rcp_f32 v72, v74\n" \
"v_rcp_f32 v80, v82\n" \
"v_mul_f32 v74, v74, v74\n" \
"v_mul_f32 v82, v82, v82\n" \
"v_mul_f32 v76, s94, v72\n" \
"v_mul_f32 v84, s94, v80\n" \
"s_nop 0\n" \
"v_accvgpr_read_b32 v88, a32\n" \
"v_accvgpr_read_b32 v94, a36\n" \
"v_accvgpr_read_b32 v100, a72\n" \
"v_accvgpr_read_b32 v106, a76\n" \
"v_accvgpr_read_b32 v89, a33\n" \
"v_accvgpr_read_b32 v95, a37\n" \
"v_accvgpr_read_b32 v101, a73\n" \
"v_accvgpr_read_b32 v107, a77\n" \
"v_accvgpr_read_b32 v90, a34\n" \
"v_accvgpr_read_b32 v96, a38\n" \
"v_accvgpr_read_b32 v102, a74\n" \
"v_accvgpr_read_b32 v108, a78\n" \
"v_accvgpr_read_b32 v91, a35\n" \
"v_accvgpr_read_b32 v97, a39\n" \
"v_accvgpr_read_b32 v103, a75\n" \
"v_accvgpr_read_b32 v109, a79\n" \
"v_pk_fma_f32 v[88:89], v[52:53], v[70:71], v[88:89] op_sel_hi:[1,0,1] neg_lo:[0,1,0] neg_hi:[0,1,0]\n" \
"v_pk_fma_f32 v[94:95], v[56:57], v[70:71], v[94:95] op_sel_hi:[1,0,1] neg_lo:[0,1,0] neg_hi:[0,1,0]\n" \
"v_pk_fma_f32 v[100:101], v[52:53], v[78:79], v[100:101] op_sel_hi:[1,0,1] neg_lo:[0,1,0] neg_hi:[0,1,0]\n" \
"v_pk_fma_f32 v[106:107], v[56:57], v[78:79], v[106:107] op_sel_hi:[1,0,1] neg_lo:[0,1,0] neg_hi:[0,1,0]\n" \
"v_pk_fma_f32 v[90:91], v[54:55], v[70:71], v[90:91] op_sel_hi:[1,0,1] neg_lo:[0,1,0] neg_hi:[0,1,0]\n" \
"v_pk_fma_f32 v[96:97], v[58:59], v[70:71], v[96:97] op_sel_hi:[1,0,1] neg_lo:[0,1,0] neg_hi:[0,1,0]\n" \
"v_pk_fma_f32 v[102:103], v[54:55], v[78:79], v[102:103] op_sel_hi:[1,0,1] neg_lo:[0,1,0] neg_hi:[0,1,0]\n" \
"v_pk_fma_f32 v[108:109], v[58:59], v[78:79], v[108:109] op_sel_hi:[1,0,1] neg_lo:[0,1,0] neg_hi:[0,1,0]\n" \
"v_pk_mul_f32 v[88:89], v[88:89], v[72:73] op_sel_hi:[1,0]\n" \
"v_pk_mul_f32 v[94:95], v[94:95], v[72:73] op_sel_hi:[1,0]\n" \
"v_pk_mul_f32 v[100:101], v[100:101], v[80:81] op_sel_hi:[1,0]\n" \
"v_pk_mul_f32 v[106:107], v[106:107], v[80:81] op_sel_hi:[1,0]\n" \
"v_pk_mul_f32 v[90:91], v[90:91], v[72:73] op_sel_hi:[1,0]\n" \
"v_pk_mul_f32 v[96:97], v[96:97], v[72:73] op_sel_hi:[1,0]\n" \
"v_pk_mul_f32 v[102:103], v[102:103], v[80:81] op_sel_hi:[1,0]\n" \
"v_pk_mul_f32 v[108:109], v[108:109], v[80:81] op_sel_hi:[1,0]\n" \
"v_cvt_pk_bf16_f32 v92, v88, v89\n" \
"v_cvt_pk_bf16_f32 v98, v94, v95\n" \
"v_cvt_pk_bf16_f32 v104, v100, v101\n" \
"v_cvt_pk_bf16_f32 v110, v106, v107\n" \
"v_cvt_pk_bf16_f32 v93, v90, v91\n" \
"v_cvt_pk_bf16_f32 v99, v96, v97\n" \
"v_cvt_pk_bf16_f32 v105, v102, v103\n" \
"v_cvt_pk_bf16_f32 v111, v108, v109\n" \
"ds_write_b64 v248, v[92:93] offset:512\n" \
"ds_write_b64 v249, v[98:99] offset:512\n" \
"ds_write_b64 v248, v[104:105] offset:1024\n" \
"ds_write_b64 v249, v[110:111] offset:1024\n" \
"v_lshlrev_b32 v70, 16, v63\n" \
"v_lshlrev_b32 v78, 16, v64\n" \
"v_and_b32 v74, 0xffff0000, v63\n" \
"v_and_b32 v82, 0xffff0000, v64\n" \
"v_rcp_f32 v72, v74\n" \
"v_rcp_f32 v80, v82\n" \
"v_mul_f32 v74, v74, v74\n" \
"v_mul_f32 v82, v82, v82\n" \
"v_mul_f32 v76, s94, v72\n" \
"v_mul_f32 v84, s94, v80\n" \
"s_nop 0\n" \
"v_accvgpr_read_b32 v112, a112\n" \
"v_accvgpr_read_b32 v86, a116\n" \
"v_accvgpr_read_b32 v92, a152\n" \
"v_accvgpr_read_b32 v98, a156\n" \
"v_accvgpr_read_b32 v113, a113\n" \
"v_accvgpr_read_b32 v87, a117\n" \
"v_accvgpr_read_b32 v93, a153\n" \
"v_accvgpr_read_b32 v99, a157\n" \
"v_accvgpr_read_b32 v114, a114\n" \
"v_accvgpr_read_b32 v88, a118\n" \
"v_accvgpr_read_b32 v94, a154\n" \
"v_accvgpr_read_b32 v100, a158\n" \
"v_accvgpr_read_b32 v115, a115\n" \
"v_accvgpr_read_b32 v89, a119\n" \
"v_accvgpr_read_b32 v95, a155\n" \
"v_accvgpr_read_b32 v101, a159\n" \
"v_pk_fma_f32 v[112:113], v[52:53], v[70:71], v[112:113] op_sel_hi:[1,0,1] neg_lo:[0,1,0] neg_hi:[0,1,0]\n" \
"v_pk_fma_f32 v[86:87], v[56:57], v[70:71], v[86:87] op_sel_hi:[1,0,1] neg_lo:[0,1,0] neg_hi:[0,1,0]\n" \
"v_pk_fma_f32 v[92:93], v[52:53], v[78:79], v[92:93] op_sel_hi:[1,0,1] neg_lo:[0,1,0] neg_hi:[0,1,0]\n" \
"v_pk_fma_f32 v[98:99], v[56:57], v[78:79], v[98:99] op_sel_hi:[1,0,1] neg_lo:[0,1,0] neg_hi:[0,1,0]\n" \
"v_pk_fma_f32 v[114:115], v[54:55], v[70:71], v[114:115] op_sel_hi:[1,0,1] neg_lo:[0,1,0] neg_hi:[0,1,0]\n" \
"v_pk_fma_f32 v[88:89], v[58:59], v[70:71], v[88:89] op_sel_hi:[1,0,1] neg_lo:[0,1,0] neg_hi:[0,1,0]\n" \
"v_pk_fma_f32 v[94:95], v[54:55], v[78:79], v[94:95] op_sel_hi:[1,0,1] neg_lo:[0,1,0] neg_hi:[0,1,0]\n" \
"v_pk_fma_f32 v[100:101], v[58:59], v[78:79], v[100:101] op_sel_hi:[1,0,1] neg_lo:[0,1,0] neg_hi:[0,1,0]\n" \
"v_pk_mul_f32 v[112:113], v[112:113], v[72:73] op_sel_hi:[1,0]\n" \
"v_pk_mul_f32 v[86:87], v[86:87], v[72:73] op_sel_hi:[1,0]\n" \
"v_pk_mul_f32 v[92:93], v[92:93], v[80:81] op_sel_hi:[1,0]\n" \
"v_pk_mul_f32 v[98:99], v[98:99], v[80:81] op_sel_hi:[1,0]\n" \
"v_pk_mul_f32 v[114:115], v[114:115], v[72:73] op_sel_hi:[1,0]\n" \
"v_pk_mul_f32 v[88:89], v[88:89], v[72:73] op_sel_hi:[1,0]\n" \
"v_pk_mul_f32 v[94:95], v[94:95], v[80:81] op_sel_hi:[1,0]\n" \
"v_pk_mul_f32 v[100:101], v[100:101], v[80:81] op_sel_hi:[1,0]\n" \
"v_cvt_pk_bf16_f32 v116, v112, v113\n" \
"v_cvt_pk_bf16_f32 v90, v86, v87\n" \
"v_cvt_pk_bf16_f32 v96, v92, v93\n" \
"v_cvt_pk_bf16_f32 v102, v98, v99\n" \
"v_cvt_pk_bf16_f32 v117, v114, v115\n" \
"v_cvt_pk_bf16_f32 v91, v88, v89\n" \
"v_cvt_pk_bf16_f32 v97, v94, v95\n" \
"v_cvt_pk_bf16_f32 v103, v100, v101\n" \
"ds_write_b64 v248, v[116:117] offset:1536\n" \
"ds_write_b64 v249, v[90:91] offset:1536\n" \
"ds_write_b64 v248, v[96:97] offset:2048\n" \
"ds_write_b64 v249, v[102:103] offset:2048\n" \
"v_lshlrev_b32 v70, 16, v65\n" \
"v_lshlrev_b32 v78, 16, v66\n" \
"v_and_b32 v74, 0xffff0000, v65\n" \
"v_and_b32 v82, 0xffff0000, v66\n" \
"v_rcp_f32 v72, v74\n" \
"v_rcp_f32 v80, v82\n" \
"v_mul_f32 v74, v74, v74\n" \
"v_mul_f32 v82, v82, v82\n" \
"v_mul_f32 v76, s94, v72\n" \
"v_mul_f32 v84, s94, v80\n" \
"s_nop 0\n" \
"v_accvgpr_read_b32 v104, a192\n" \
"v_pk_fma_f32 v[120:121], v[56:57], v[70:71], v[120:121] op_sel_hi:[1,0,1] neg_lo:[0,1,0] neg_hi:[0,1,0]\n" \
"v_pk_fma_f32 v[156:157], v[52:53], v[78:79], v[156:157] op_sel_hi:[1,0,1] neg_lo:[0,1,0] neg_hi:[0,1,0]\n" \
"v_pk_fma_f32 v[160:161], v[56:57], v[78:79], v[160:161] op_sel_hi:[1,0,1] neg_lo:[0,1,0] neg_hi:[0,1,0]\n" \
"v_accvgpr_read_b32 v105, a193\n" \
"v_pk_fma_f32 v[122:123], v[58:59], v[70:71], v[122:123] op_sel_hi:[1,0,1] neg_lo:[0,1,0] neg_hi:[0,1,0]\n" \
"v_pk_fma_f32 v[158:159], v[54:55], v[78:79], v[158:159] op_sel_hi:[1,0,1] neg_lo:[0,1,0] neg_hi:[0,1,0]\n" \
"v_pk_fma_f32 v[162:163], v[58:59], v[78:79], v[162:163] op_sel_hi:[1,0,1] neg_lo:[0,1,0] neg_hi:[0,1,0]\n" \
"v_accvgpr_read_b32 v106, a194\n" \
"v_pk_mul_f32 v[120:121], v[120:121], v[72:73] op_sel_hi:[1,0]\n" \
"v_pk_mul_f32 v[156:157], v[156:157], v[80:81] op_sel_hi:[1,0]\n" \
"v_pk_mul_f32 v[160:161], v[160:161], v[80:81] op_sel_hi:[1,0]\n" \
"v_accvgpr_read_b32 v107, a195\n" \
"v_pk_mul_f32 v[122:123], v[122:123], v[72:73] op_sel_hi:[1,0]\n" \
"v_pk_mul_f32 v[158:159], v[158:159], v[80:81] op_sel_hi:[1,0]\n" \
"v_pk_mul_f32 v[162:163], v[162:163], v[80:81] op_sel_hi:[1,0]\n" \
"v_pk_fma_f32 v[104:105], v[52:53], v[70:71], v[104:105] op_sel_hi:[1,0,1] neg_lo:[0,1,0] neg_hi:[0,1,0]\n" \
"v_cvt_pk_bf16_f32 v110, v120, v121\n" \
"v_cvt_pk_bf16_f32 v112, v156, v157\n" \
"v_cvt_pk_bf16_f32 v114, v160, v161\n" \
"v_pk_fma_f32 v[106:107], v[54:55], v[70:71], v[106:107] op_sel_hi:[1,0,1] neg_lo:[0,1,0] neg_hi:[0,1,0]\n" \
"v_cvt_pk_bf16_f32 v111, v122, v123\n" \
"v_cvt_pk_bf16_f32 v113, v158, v159\n" \
"v_cvt_pk_bf16_f32 v115, v162, v163\n" \
"v_pk_mul_f32 v[104:105], v[104:105], v[72:73] op_sel_hi:[1,0]\n" \
"ds_write_b64 v249, v[110:111] offset:2560\n" \
"ds_write_b64 v248, v[112:113] offset:3072\n" \
"ds_write_b64 v249, v[114:115] offset:3072\n" \
"v_pk_mul_f32 v[106:107], v[106:107], v[72:73] op_sel_hi:[1,0]\n" \
"v_cvt_pk_bf16_f32 v108, v104, v105\n" \
"v_cvt_pk_bf16_f32 v109, v106, v107\n" \
"ds_write_b64 v248, v[108:109] offset:2560\n" \
"v_lshlrev_b32 v70, 16, v67\n" \
"v_lshlrev_b32 v78, 16, v68\n" \
"v_and_b32 v74, 0xffff0000, v67\n" \
"v_and_b32 v82, 0xffff0000, v68\n" \
"v_rcp_f32 v72, v74\n" \
"v_rcp_f32 v80, v82\n" \
"v_mul_f32 v74, v74, v74\n" \
"v_mul_f32 v82, v82, v82\n" \
"v_mul_f32 v76, s94, v72\n" \
"v_mul_f32 v84, s94, v80\n" \
"s_nop 0\n" \
"v_pk_fma_f32 v[196:197], v[52:53], v[70:71], v[196:197] op_sel_hi:[1,0,1] neg_lo:[0,1,0] neg_hi:[0,1,0]\n" \
"v_pk_fma_f32 v[200:201], v[56:57], v[70:71], v[200:201] op_sel_hi:[1,0,1] neg_lo:[0,1,0] neg_hi:[0,1,0]\n" \
"v_pk_fma_f32 v[236:237], v[52:53], v[78:79], v[236:237] op_sel_hi:[1,0,1] neg_lo:[0,1,0] neg_hi:[0,1,0]\n" \
"v_pk_fma_f32 v[240:241], v[56:57], v[78:79], v[240:241] op_sel_hi:[1,0,1] neg_lo:[0,1,0] neg_hi:[0,1,0]\n" \
"v_pk_fma_f32 v[198:199], v[54:55], v[70:71], v[198:199] op_sel_hi:[1,0,1] neg_lo:[0,1,0] neg_hi:[0,1,0]\n" \
"v_pk_fma_f32 v[202:203], v[58:59], v[70:71], v[202:203] op_sel_hi:[1,0,1] neg_lo:[0,1,0] neg_hi:[0,1,0]\n" \
"v_pk_fma_f32 v[238:239], v[54:55], v[78:79], v[238:239] op_sel_hi:[1,0,1] neg_lo:[0,1,0] neg_hi:[0,1,0]\n" \
"v_pk_fma_f32 v[242:243], v[58:59], v[78:79], v[242:243] op_sel_hi:[1,0,1] neg_lo:[0,1,0] neg_hi:[0,1,0]\n" \
"v_pk_mul_f32 v[196:197], v[196:197], v[72:73] op_sel_hi:[1,0]\n" \
"v_pk_mul_f32 v[200:201], v[200:201], v[72:73] op_sel_hi:[1,0]\n" \
"v_pk_mul_f32 v[236:237], v[236:237], v[80:81] op_sel_hi:[1,0]\n" \
"v_pk_mul_f32 v[240:241], v[240:241], v[80:81] op_sel_hi:[1,0]\n" \
"v_pk_mul_f32 v[198:199], v[198:199], v[72:73] op_sel_hi:[1,0]\n" \
"v_pk_mul_f32 v[202:203], v[202:203], v[72:73] op_sel_hi:[1,0]\n" \
"v_pk_mul_f32 v[238:239], v[238:239], v[80:81] op_sel_hi:[1,0]\n" \
"v_pk_mul_f32 v[242:243], v[242:243], v[80:81] op_sel_hi:[1,0]\n" \
"v_cvt_pk_bf16_f32 v116, v196, v197\n" \
"v_cvt_pk_bf16_f32 v118, v200, v201\n" \
"v_cvt_pk_bf16_f32 v86, v236, v237\n" \
"v_cvt_pk_bf16_f32 v88, v240, v241\n" \
"v_cvt_pk_bf16_f32 v117, v198, v199\n" \
"v_cvt_pk_bf16_f32 v119, v202, v203\n" \
"v_cvt_pk_bf16_f32 v87, v238, v239\n" \
"v_cvt_pk_bf16_f32 v89, v242, v243\n" \
"ds_write_b64 v248, v[116:117] offset:3584\n" \
"ds_write_b64 v249, v[118:119] offset:3584\n" \
"ds_write_b64 v248, v[86:87] offset:4096\n" \
"ds_write_b64 v249, v[88:89] offset:4096\n" \
"s_cmp_lg_u32 s65, 0\n" \
"s_cbranch_scc1 LE1%=\n" \
"v_lshlrev_b32 v70, 16, v61\n" \
"v_and_b32 v74, 0xffff0000, v61\n" \
"v_rcp_f32 v72, v74\n" \
"v_mul_f32 v74, v74, v74\n" \
"v_mul_f32 v76, s94, v72\n" \
"s_nop 0\n" \
"v_accvgpr_read_b32 v90, a0\n" \
"v_accvgpr_read_b32 v102, a4\n" \
"v_accvgpr_read_b32 v91, a1\n" \
"v_accvgpr_read_b32 v103, a5\n" \
"v_accvgpr_read_b32 v92, a2\n" \
"v_accvgpr_read_b32 v104, a6\n" \
"v_accvgpr_read_b32 v93, a3\n" \
"v_accvgpr_read_b32 v105, a7\n" \
"v_accvgpr_read_b32 v94, a16\n" \
"v_accvgpr_read_b32 v106, a20\n" \
"v_accvgpr_read_b32 v95, a17\n" \
"v_accvgpr_read_b32 v107, a21\n" \
"v_accvgpr_read_b32 v96, a18\n" \
"v_accvgpr_read_b32 v108, a22\n" \
"v_accvgpr_read_b32 v97, a19\n" \
"v_accvgpr_read_b32 v109, a23\n" \
"v_pk_fma_f32 v[90:91], v[20:21], v[70:71], v[90:91] op_sel_hi:[1,0,1] neg_lo:[0,1,0] neg_hi:[0,1,0]\n" \
"v_pk_fma_f32 v[102:103], v[24:25], v[70:71], v[102:103] op_sel_hi:[1,0,1] neg_lo:[0,1,0] neg_hi:[0,1,0]\n" \
"v_pk_fma_f32 v[92:93], v[22:23], v[70:71], v[92:93] op_sel_hi:[1,0,1] neg_lo:[0,1,0] neg_hi:[0,1,0]\n" \
"v_pk_fma_f32 v[104:105], v[26:27], v[70:71], v[104:105] op_sel_hi:[1,0,1] neg_lo:[0,1,0] neg_hi:[0,1,0]\n" \
"v_pk_fma_f32 v[94:95], v[36:37], v[70:71], v[94:95] op_sel_hi:[1,0,1] neg_lo:[0,1,0] neg_hi:[0,1,0]\n" \
"v_pk_fma_f32 v[106:107], v[40:41], v[70:71], v[106:107] op_sel_hi:[1,0,1] neg_lo:[0,1,0] neg_hi:[0,1,0]\n" \
"v_pk_fma_f32 v[96:97], v[38:39], v[70:71], v[96:97] op_sel_hi:[1,0,1] neg_lo:[0,1,0] neg_hi:[0,1,0]\n" \
"v_pk_fma_f32 v[108:109], v[42:43], v[70:71], v[108:109] op_sel_hi:[1,0,1] neg_lo:[0,1,0] neg_hi:[0,1,0]\n" \
"v_pk_mul_f32 v[98:99], v[90:91], v[76:77] op_sel_hi:[1,0]\n" \
"v_pk_mul_f32 v[110:111], v[102:103], v[76:77] op_sel_hi:[1,0]\n" \
"v_pk_mul_f32 v[100:101], v[92:93], v[76:77] op_sel_hi:[1,0]\n" \
"v_pk_mul_f32 v[112:113], v[104:105], v[76:77] op_sel_hi:[1,0]\n" \
"v_exp_f32 v98, v98\n" \
"v_exp_f32 v110, v110\n" \
"v_exp_f32 v99, v99\n" \
"v_exp_f32 v111, v111\n" \
"v_exp_f32 v100, v100\n" \
"v_exp_f32 v112, v112\n" \
"v_exp_f32 v101, v101\n" \
"v_exp_f32 v113, v113\n" \
"v_pk_mul_f32 v[94:95], v[90:91], v[94:95]\n" \
"v_pk_mul_f32 v[106:107], v[102:103], v[106:107]\n" \
"v_pk_mul_f32 v[96:97], v[92:93], v[96:97]\n" \
"v_pk_mul_f32 v[108:109], v[104:105], v[108:109]\n" \
"v_pk_fma_f32 v[98:99], v[98:99], v[74:75], v[74:75] op_sel_hi:[1,0,0]\n" \
"v_pk_fma_f32 v[110:111], v[110:111], v[74:75], v[74:75] op_sel_hi:[1,0,0]\n" \
"v_pk_fma_f32 v[100:101], v[100:101], v[74:75], v[74:75] op_sel_hi:[1,0,0]\n" \
"v_pk_fma_f32 v[112:113], v[112:113], v[74:75], v[74:75] op_sel_hi:[1,0,0]\n" \
"v_rcp_f32 v98, v98\n" \
"v_rcp_f32 v110, v110\n" \
"v_rcp_f32 v99, v99\n" \
"v_rcp_f32 v111, v111\n" \
"v_rcp_f32 v100, v100\n" \
"v_rcp_f32 v112, v112\n" \
"v_rcp_f32 v101, v101\n" \
"v_rcp_f32 v113, v113\n" \
"v_pk_mul_f32 v[94:95], v[94:95], v[98:99]\n" \
"v_pk_mul_f32 v[106:107], v[106:107], v[110:111]\n" \
"v_pk_mul_f32 v[96:97], v[96:97], v[100:101]\n" \
"v_pk_mul_f32 v[108:109], v[108:109], v[112:113]\n" \
"v_cvt_pk_bf16_f32 v114, v94, v95\n" \
"v_cvt_pk_bf16_f32 v116, v106, v107\n" \
"v_cvt_pk_bf16_f32 v115, v96, v97\n" \
"v_cvt_pk_bf16_f32 v117, v108, v109\n" \
"s_nop 1\n" \
"v_permlane16_swap_b32 v114, v116\n" \
"v_permlane16_swap_b32 v115, v117\n" \
"global_store_dwordx4 v252, v[114:117], s[56:57] offset:0 sc1\n" \
"v_accvgpr_read_b32 v86, a8\n" \
"v_accvgpr_read_b32 v98, a12\n" \
"v_accvgpr_read_b32 v87, a9\n" \
"v_accvgpr_read_b32 v99, a13\n" \
"v_accvgpr_read_b32 v88, a10\n" \
"v_accvgpr_read_b32 v100, a14\n" \
"v_accvgpr_read_b32 v89, a11\n" \
"v_accvgpr_read_b32 v101, a15\n" \
"v_accvgpr_read_b32 v90, a24\n" \
"v_accvgpr_read_b32 v102, a28\n" \
"v_accvgpr_read_b32 v91, a25\n" \
"v_accvgpr_read_b32 v103, a29\n" \
"v_accvgpr_read_b32 v92, a26\n" \
"v_accvgpr_read_b32 v104, a30\n" \
"v_accvgpr_read_b32 v93, a27\n" \
"v_accvgpr_read_b32 v105, a31\n" \
"v_pk_fma_f32 v[86:87], v[28:29], v[70:71], v[86:87] op_sel_hi:[1,0,1] neg_lo:[0,1,0] neg_hi:[0,1,0]\n" \
"v_pk_fma_f32 v[98:99], v[32:33], v[70:71], v[98:99] op_sel_hi:[1,0,1] neg_lo:[0,1,0] neg_hi:[0,1,0]\n" \
"v_pk_fma_f32 v[88:89], v[30:31], v[70:71], v[88:89] op_sel_hi:[1,0,1] neg_lo:[0,1,0] neg_hi:[0,1,0]\n" \
"v_pk_fma_f32 v[100:101], v[34:35], v[70:71], v[100:101] op_sel_hi:[1,0,1] neg_lo:[0,1,0] neg_hi:[0,1,0]\n" \
"v_pk_fma_f32 v[90:91], v[44:45], v[70:71], v[90:91] op_sel_hi:[1,0,1] neg_lo:[0,1,0] neg_hi:[0,1,0]\n" \
"v_pk_fma_f32 v[102:103], v[48:49], v[70:71], v[102:103] op_sel_hi:[1,0,1] neg_lo:[0,1,0] neg_hi:[0,1,0]\n" \
"v_pk_fma_f32 v[92:93], v[46:47], v[70:71], v[92:93] op_sel_hi:[1,0,1] neg_lo:[0,1,0] neg_hi:[0,1,0]\n" \
"v_pk_fma_f32 v[104:105], v[50:51], v[70:71], v[104:105] op_sel_hi:[1,0,1] neg_lo:[0,1,0] neg_hi:[0,1,0]\n" \
"v_pk_mul_f32 v[94:95], v[86:87], v[76:77] op_sel_hi:[1,0]\n" \
"v_pk_mul_f32 v[106:107], v[98:99], v[76:77] op_sel_hi:[1,0]\n" \
"v_pk_mul_f32 v[96:97], v[88:89], v[76:77] op_sel_hi:[1,0]\n" \
"v_pk_mul_f32 v[108:109], v[100:101], v[76:77] op_sel_hi:[1,0]\n" \
"v_exp_f32 v94, v94\n" \
"v_exp_f32 v106, v106\n" \
"v_exp_f32 v95, v95\n" \
"v_exp_f32 v107, v107\n" \
"v_exp_f32 v96, v96\n" \
"v_exp_f32 v108, v108\n" \
"v_exp_f32 v97, v97\n" \
"v_exp_f32 v109, v109\n" \
"v_pk_mul_f32 v[90:91], v[86:87], v[90:91]\n" \
"v_pk_mul_f32 v[102:103], v[98:99], v[102:103]\n" \
"v_pk_mul_f32 v[92:93], v[88:89], v[92:93]\n" \
"v_pk_mul_f32 v[104:105], v[100:101], v[104:105]\n" \
"v_pk_fma_f32 v[94:95], v[94:95], v[74:75], v[74:75] op_sel_hi:[1,0,0]\n" \
"v_pk_fma_f32 v[106:107], v[106:107], v[74:75], v[74:75] op_sel_hi:[1,0,0]\n" \
"v_pk_fma_f32 v[96:97], v[96:97], v[74:75], v[74:75] op_sel_hi:[1,0,0]\n" \
"v_pk_fma_f32 v[108:109], v[108:109], v[74:75], v[74:75] op_sel_hi:[1,0,0]\n" \
"v_rcp_f32 v94, v94\n" \
"v_rcp_f32 v106, v106\n" \
"v_rcp_f32 v95, v95\n" \
"v_rcp_f32 v107, v107\n" \
"v_rcp_f32 v96, v96\n" \
"v_rcp_f32 v108, v108\n" \
"v_rcp_f32 v97, v97\n" \
"v_rcp_f32 v109, v109\n" \
"v_pk_mul_f32 v[90:91], v[90:91], v[94:95]\n" \
"v_pk_mul_f32 v[102:103], v[102:103], v[106:107]\n" \
"v_pk_mul_f32 v[92:93], v[92:93], v[96:97]\n" \
"v_pk_mul_f32 v[104:105], v[104:105], v[108:109]\n" \
"v_cvt_pk_bf16_f32 v110, v90, v91\n" \
"v_cvt_pk_bf16_f32 v112, v102, v103\n" \
"v_cvt_pk_bf16_f32 v111, v92, v93\n" \
"v_cvt_pk_bf16_f32 v113, v104, v105\n" \
"s_nop 1\n" \
"v_permlane16_swap_b32 v110, v112\n" \
"v_permlane16_swap_b32 v111, v113\n" \
"global_store_dwordx4 v252, v[110:113], s[56:57] offset:1024 sc1\n" \
"s_add_u32 s56, s56, 114688\n" \
"s_addc_u32 s57, s57, 0\n" \
"s_waitcnt lgkmcnt(0)\n" \
"s_barrier\n" \
"s_waitcnt vmcnt(2)\n" \
"v_lshlrev_b32 v78, 16, v62\n" \
"v_and_b32 v82, 0xffff0000, v62\n" \
"v_rcp_f32 v80, v82\n" \
"v_mul_f32 v82, v82, v82\n" \
"v_mul_f32 v84, s94, v80\n" \
"s_nop 0\n" \
"v_accvgpr_read_b32 v114, a40\n" \
"v_accvgpr_read_b32 v94, a44\n" \
"v_accvgpr_read_b32 v115, a41\n" \
"v_accvgpr_read_b32 v95, a45\n" \
"v_accvgpr_read_b32 v116, a42\n" \
"v_accvgpr_read_b32 v96, a46\n" \
"v_accvgpr_read_b32 v117, a43\n" \
"v_accvgpr_read_b32 v97, a47\n" \
"v_accvgpr_read_b32 v86, a56\n" \
"v_accvgpr_read_b32 v98, a60\n" \
"v_accvgpr_read_b32 v87, a57\n" \
"v_accvgpr_read_b32 v99, a61\n" \
"v_accvgpr_read_b32 v88, a58\n" \
"v_accvgpr_read_b32 v100, a62\n" \
"v_accvgpr_read_b32 v89, a59\n" \
"v_accvgpr_read_b32 v101, a63\n" \
"v_pk_fma_f32 v[114:115], v[20:21], v[78:79], v[114:115] op_sel_hi:[1,0,1] neg_lo:[0,1,0] neg_hi:[0,1,0]\n" \
"v_pk_fma_f32 v[94:95], v[24:25], v[78:79], v[94:95] op_sel_hi:[1,0,1] neg_lo:[0,1,0] neg_hi:[0,1,0]\n" \
"v_pk_fma_f32 v[116:117], v[22:23], v[78:79], v[116:117] op_sel_hi:[1,0,1] neg_lo:[0,1,0] neg_hi:[0,1,0]\n" \
"v_pk_fma_f32 v[96:97], v[26:27], v[78:79], v[96:97] op_sel_hi:[1,0,1] neg_lo:[0,1,0] neg_hi:[0,1,0]\n" \
"v_pk_fma_f32 v[86:87], v[36:37], v[78:79], v[86:87] op_sel_hi:[1,0,1] neg_lo:[0,1,0] neg_hi:[0,1,0]\n" \
"v_pk_fma_f32 v[98:99], v[40:41], v[78:79], v[98:99] op_sel_hi:[1,0,1] neg_lo:[0,1,0] neg_hi:[0,1,0]\n" \
"v_pk_fma_f32 v[88:89], v[38:39], v[78:79], v[88:89] op_sel_hi:[1,0,1] neg_lo:[0,1,0] neg_hi:[0,1,0]\n" \
"v_pk_fma_f32 v[100:101], v[42:43], v[78:79], v[100:101] op_sel_hi:[1,0,1] neg_lo:[0,1,0] neg_hi:[0,1,0]\n" \
"v_pk_mul_f32 v[90:91], v[114:115], v[84:85] op_sel_hi:[1,0]\n" \
"v_pk_mul_f32 v[102:103], v[94:95], v[84:85] op_sel_hi:[1,0]\n" \
"v_pk_mul_f32 v[92:93], v[116:117], v[84:85] op_sel_hi:[1,0]\n" \
"v_pk_mul_f32 v[104:105], v[96:97], v[84:85] op_sel_hi:[1,0]\n" \
"v_exp_f32 v90, v90\n" \
"v_exp_f32 v102, v102\n" \
"v_exp_f32 v91, v91\n" \
"v_exp_f32 v103, v103\n" \
"v_exp_f32 v92, v92\n" \
"v_exp_f32 v104, v104\n" \
"v_exp_f32 v93, v93\n" \
"v_exp_f32 v105, v105\n" \
"v_pk_mul_f32 v[86:87], v[114:115], v[86:87]\n" \
"v_pk_mul_f32 v[98:99], v[94:95], v[98:99]\n" \
"v_pk_mul_f32 v[88:89], v[116:117], v[88:89]\n" \
"v_pk_mul_f32 v[100:101], v[96:97], v[100:101]\n" \
"v_pk_fma_f32 v[90:91], v[90:91], v[82:83], v[82:83] op_sel_hi:[1,0,0]\n" \
"v_pk_fma_f32 v[102:103], v[102:103], v[82:83], v[82:83] op_sel_hi:[1,0,0]\n" \
"v_pk_fma_f32 v[92:93], v[92:93], v[82:83], v[82:83] op_sel_hi:[1,0,0]\n" \
"v_pk_fma_f32 v[104:105], v[104:105], v[82:83], v[82:83] op_sel_hi:[1,0,0]\n" \
"v_rcp_f32 v90, v90\n" \
"v_rcp_f32 v102, v102\n" \
"v_rcp_f32 v91, v91\n" \
"v_rcp_f32 v103, v103\n" \
"v_rcp_f32 v92, v92\n" \
"v_rcp_f32 v104, v104\n" \
"v_rcp_f32 v93, v93\n" \
"v_rcp_f32 v105, v105\n" \
"v_pk_mul_f32 v[86:87], v[86:87], v[90:91]\n" \
"v_pk_mul_f32 v[98:99], v[98:99], v[102:103]\n" \
"v_pk_mul_f32 v[88:89], v[88:89], v[92:93]\n" \
"v_pk_mul_f32 v[100:101], v[100:101], v[104:105]\n" \
"v_cvt_pk_bf16_f32 v106, v86, v87\n" \
"v_cvt_pk_bf16_f32 v108, v98, v99\n" \
"v_cvt_pk_bf16_f32 v107, v88, v89\n" \
"v_cvt_pk_bf16_f32 v109, v100, v101\n" \
"s_nop 1\n" \
"v_permlane16_swap_b32 v106, v108\n" \
"v_permlane16_swap_b32 v107, v109\n" \
"global_store_dwordx4 v252, v[106:109], s[56:57] offset:0 sc1\n" \
"v_accvgpr_read_b32 v110, a48\n" \
"v_accvgpr_read_b32 v90, a52\n" \
"v_accvgpr_read_b32 v111, a49\n" \
"v_accvgpr_read_b32 v91, a53\n" \
"v_accvgpr_read_b32 v112, a50\n" \
"v_accvgpr_read_b32 v92, a54\n" \
"v_accvgpr_read_b32 v113, a51\n" \
"v_accvgpr_read_b32 v93, a55\n" \
"v_accvgpr_read_b32 v114, a64\n" \
"v_accvgpr_read_b32 v94, a68\n" \
"v_accvgpr_read_b32 v115, a65\n" \
"v_accvgpr_read_b32 v95, a69\n" \
"v_accvgpr_read_b32 v116, a66\n" \
"v_accvgpr_read_b32 v96, a70\n" \
"v_accvgpr_read_b32 v117, a67\n" \
"v_accvgpr_read_b32 v97, a71\n" \
"v_pk_fma_f32 v[110:111], v[28:29], v[78:79], v[110:111] op_sel_hi:[1,0,1] neg_lo:[0,1,0] neg_hi:[0,1,0]\n" \
"v_pk_fma_f32 v[90:91], v[32:33], v[78:79], v[90:91] op_sel_hi:[1,0,1] neg_lo:[0,1,0] neg_hi:[0,1,0]\n" \
"v_pk_fma_f32 v[112:113], v[30:31], v[78:79], v[112:113] op_sel_hi:[1,0,1] neg_lo:[0,1,0] neg_hi:[0,1,0]\n" \
"v_pk_fma_f32 v[92:93], v[34:35], v[78:79], v[92:93] op_sel_hi:[1,0,1] neg_lo:[0,1,0] neg_hi:[0,1,0]\n" \
"v_pk_fma_f32 v[114:115], v[44:45], v[78:79], v[114:115] op_sel_hi:[1,0,1] neg_lo:[0,1,0] neg_hi:[0,1,0]\n" \
"v_pk_fma_f32 v[94:95], v[48:49], v[78:79], v[94:95] op_sel_hi:[1,0,1] neg_lo:[0,1,0] neg_hi:[0,1,0]\n" \
"v_pk_fma_f32 v[116:117], v[46:47], v[78:79], v[116:117] op_sel_hi:[1,0,1] neg_lo:[0,1,0] neg_hi:[0,1,0]\n" \
"v_pk_fma_f32 v[96:97], v[50:51], v[78:79], v[96:97] op_sel_hi:[1,0,1] neg_lo:[0,1,0] neg_hi:[0,1,0]\n" \
"v_pk_mul_f32 v[86:87], v[110:111], v[84:85] op_sel_hi:[1,0]\n" \
"v_pk_mul_f32 v[98:99], v[90:91], v[84:85] op_sel_hi:[1,0]\n" \
"v_pk_mul_f32 v[88:89], v[112:113], v[84:85] op_sel_hi:[1,0]\n" \
"v_pk_mul_f32 v[100:101], v[92:93], v[84:85] op_sel_hi:[1,0]\n" \
"v_exp_f32 v86, v86\n" \
"v_exp_f32 v98, v98\n" \
"v_exp_f32 v87, v87\n" \
"v_exp_f32 v99, v99\n" \
"v_exp_f32 v88, v88\n" \
"v_exp_f32 v100, v100\n" \
"v_exp_f32 v89, v89\n" \
"v_exp_f32 v101, v101\n" \
"v_pk_mul_f32 v[114:115], v[110:111], v[114:115]\n" \
"v_pk_mul_f32 v[94:95], v[90:91], v[94:95]\n" \
"v_pk_mul_f32 v[116:117], v[112:113], v[116:117]\n" \
"v_pk_mul_f32 v[96:97], v[92:93], v[96:97]\n" \
"v_pk_fma_f32 v[86:87], v[86:87], v[82:83], v[82:83] op_sel_hi:[1,0,0]\n" \
"v_pk_fma_f32 v[98:99], v[98:99], v[82:83], v[82:83] op_sel_hi:[1,0,0]\n" \
"v_pk_fma_f32 v[88:89], v[88:89], v[82:83], v[82:83] op_sel_hi:[1,0,0]\n" \
"v_pk_fma_f32 v[100:101], v[100:101], v[82:83], v[82:83] op_sel_hi:[1,0,0]\n" \
"v_rcp_f32 v86, v86\n" \
"v_rcp_f32 v98, v98\n" \
"v_rcp_f32 v87, v87\n" \
"v_rcp_f32 v99, v99\n" \
"v_rcp_f32 v88, v88\n" \
"v_rcp_f32 v100, v100\n" \
"v_rcp_f32 v89, v89\n" \
"v_rcp_f32 v101, v101\n" \
"v_pk_mul_f32 v[114:115], v[114:115], v[86:87]\n" \
"v_pk_mul_f32 v[94:95], v[94:95], v[98:99]\n" \
"v_pk_mul_f32 v[116:117], v[116:117], v[88:89]\n" \
"v_pk_mul_f32 v[96:97], v[96:97], v[100:101]\n" \
"v_cvt_pk_bf16_f32 v102, v114, v115\n" \
"v_cvt_pk_bf16_f32 v104, v94, v95\n" \
"v_cvt_pk_bf16_f32 v103, v116, v117\n" \
"v_cvt_pk_bf16_f32 v105, v96, v97\n" \
"s_nop 1\n" \
"v_permlane16_swap_b32 v102, v104\n" \
"v_permlane16_swap_b32 v103, v105\n" \
"global_store_dwordx4 v252, v[102:105], s[56:57] offset:1024 sc1\n" \
"s_add_u32 s56, s56, 114688\n" \
"s_addc_u32 s57, s57, 0\n" \
"v_lshlrev_b32 v70, 16, v63\n" \
"v_and_b32 v74, 0xffff0000, v63\n" \
"v_rcp_f32 v72, v74\n" \
"v_mul_f32 v74, v74, v74\n" \
"v_mul_f32 v76, s94, v72\n" \
"s_nop 0\n" \
"v_accvgpr_read_b32 v106, a80\n" \
"v_accvgpr_read_b32 v86, a84\n" \
"v_accvgpr_read_b32 v107, a81\n" \
"v_accvgpr_read_b32 v87, a85\n" \
"v_accvgpr_read_b32 v108, a82\n" \
"v_accvgpr_read_b32 v88, a86\n" \
"v_accvgpr_read_b32 v109, a83\n" \
"v_accvgpr_read_b32 v89, a87\n" \
"v_accvgpr_read_b32 v110, a96\n" \
"v_accvgpr_read_b32 v90, a100\n" \
"v_accvgpr_read_b32 v111, a97\n" \
"v_accvgpr_read_b32 v91, a101\n" \
"v_accvgpr_read_b32 v112, a98\n" \
"v_accvgpr_read_b32 v92, a102\n" \
"v_accvgpr_read_b32 v113, a99\n" \
"v_accvgpr_read_b32 v93, a103\n" \
"v_pk_fma_f32 v[106:107], v[20:21], v[70:71], v[106:107] op_sel_hi:[1,0,1] neg_lo:[0,1,0] neg_hi:[0,1,0]\n" \
"v_pk_fma_f32 v[86:87], v[24:25], v[70:71], v[86:87] op_sel_hi:[1,0,1] neg_lo:[0,1,0] neg_hi:[0,1,0]\n" \
"v_pk_fma_f32 v[108:109], v[22:23], v[70:71], v[108:109] op_sel_hi:[1,0,1] neg_lo:[0,1,0] neg_hi:[0,1,0]\n" \
"v_pk_fma_f32 v[88:89], v[26:27], v[70:71], v[88:89] op_sel_hi:[1,0,1] neg_lo:[0,1,0] neg_hi:[0,1,0]\n" \
"v_pk_fma_f32 v[110:111], v[36:37], v[70:71], v[110:111] op_sel_hi:[1,0,1] neg_lo:[0,1,0] neg_hi:[0,1,0]\n" \
"v_pk_fma_f32 v[90:91], v[40:41], v[70:71], v[90:91] op_sel_hi:[1,0,1] neg_lo:[0,1,0] neg_hi:[0,1,0]\n" \
"v_pk_fma_f32 v[112:113], v[38:39], v[70:71], v[112:113] op_sel_hi:[1,0,1] neg_lo:[0,1,0] neg_hi:[0,1,0]\n" \
"v_pk_fma_f32 v[92:93], v[42:43], v[70:71], v[92:93] op_sel_hi:[1,0,1] neg_lo:[0,1,0] neg_hi:[0,1,0]\n" \
"v_pk_mul_f32 v[114:115], v[106:107], v[76:77] op_sel_hi:[1,0]\n" \
"v_pk_mul_f32 v[94:95], v[86:87], v[76:77] op_sel_hi:[1,0]\n" \
"v_pk_mul_f32 v[116:117], v[108:109], v[76:77] op_sel_hi:[1,0]\n" \
"v_pk_mul_f32 v[96:97], v[88:89], v[76:77] op_sel_hi:[1,0]\n" \
"v_exp_f32 v114, v114\n" \
"v_exp_f32 v94, v94\n" \
"v_exp_f32 v115, v115\n" \
"v_exp_f32 v95, v95\n" \
"v_exp_f32 v116, v116\n" \
"v_exp_f32 v96, v96\n" \
"v_exp_f32 v117, v117\n" \
"v_exp_f32 v97, v97\n" \
"v_pk_mul_f32 v[110:111], v[106:107], v[110:111]\n" \
"v_pk_mul_f32 v[90:91], v[86:87], v[90:91]\n" \
"v_pk_mul_f32 v[112:113], v[108:109], v[112:113]\n" \
"v_pk_mul_f32 v[92:93], v[88:89], v[92:93]\n" \
"v_pk_fma_f32 v[114:115], v[114:115], v[74:75], v[74:75] op_sel_hi:[1,0,0]\n" \
"v_pk_fma_f32 v[94:95], v[94:95], v[74:75], v[74:75] op_sel_hi:[1,0,0]\n" \
"v_pk_fma_f32 v[116:117], v[116:117], v[74:75], v[74:75] op_sel_hi:[1,0,0]\n" \
"v_pk_fma_f32 v[96:97], v[96:97], v[74:75], v[74:75] op_sel_hi:[1,0,0]\n" \
"v_rcp_f32 v114, v114\n" \
"v_rcp_f32 v94, v94\n" \
"v_rcp_f32 v115, v115\n" \
"v_rcp_f32 v95, v95\n" \
"v_rcp_f32 v116, v116\n" \
"v_rcp_f32 v96, v96\n" \
"v_rcp_f32 v117, v117\n" \
"v_rcp_f32 v97, v97\n" \
"v_pk_mul_f32 v[110:111], v[110:111], v[114:115]\n" \
"v_pk_mul_f32 v[90:91], v[90:91], v[94:95]\n" \
"v_pk_mul_f32 v[112:113], v[112:113], v[116:117]\n" \
"v_pk_mul_f32 v[92:93], v[92:93], v[96:97]\n" \
"v_cvt_pk_bf16_f32 v98, v110, v111\n" \
"v_cvt_pk_bf16_f32 v100, v90, v91\n" \
"v_cvt_pk_bf16_f32 v99, v112, v113\n" \
"v_cvt_pk_bf16_f32 v101, v92, v93\n" \
"s_nop 1\n" \
"v_permlane16_swap_b32 v98, v100\n" \
"v_permlane16_swap_b32 v99, v101\n" \
"global_store_dwordx4 v252, v[98:101], s[56:57] offset:0 sc1\n" \
"v_accvgpr_read_b32 v102, a88\n" \
"v_accvgpr_read_b32 v114, a92\n" \
"v_accvgpr_read_b32 v103, a89\n" \
"v_accvgpr_read_b32 v115, a93\n" \
"v_accvgpr_read_b32 v104, a90\n" \
"v_accvgpr_read_b32 v116, a94\n" \
"v_accvgpr_read_b32 v105, a91\n" \
"v_accvgpr_read_b32 v117, a95\n" \
"v_accvgpr_read_b32 v106, a104\n" \
"v_accvgpr_read_b32 v86, a108\n" \
"v_accvgpr_read_b32 v107, a105\n" \
"v_accvgpr_read_b32 v87, a109\n" \
"v_accvgpr_read_b32 v108, a106\n" \
"v_accvgpr_read_b32 v88, a110\n" \
"v_accvgpr_read_b32 v109, a107\n" \
"v_accvgpr_read_b32 v89, a111\n" \
"v_pk_fma_f32 v[102:103], v[28:29], v[70:71], v[102:103] op_sel_hi:[1,0,1] neg_lo:[0,1,0] neg_hi:[0,1,0]\n" \
"v_pk_fma_f32 v[114:115], v[32:33], v[70:71], v[114:115] op_sel_hi:[1,0,1] neg_lo:[0,1,0] neg_hi:[0,1,0]\n" \
"v_pk_fma_f32 v[104:105], v[30:31], v[70:71], v[104:105] op_sel_hi:[1,0,1] neg_lo:[0,1,0] neg_hi:[0,1,0]\n" \
"v_pk_fma_f32 v[116:117], v[34:35], v[70:71], v[116:117] op_sel_hi:[1,0,1] neg_lo:[0,1,0] neg_hi:[0,1,0]\n" \
"v_pk_fma_f32 v[106:107], v[44:45], v[70:71], v[106:107] op_sel_hi:[1,0,1] neg_lo:[0,1,0] neg_hi:[0,1,0]\n" \
"v_pk_fma_f32 v[86:87], v[48:49], v[70:71], v[86:87] op_sel_hi:[1,0,1] neg_lo:[0,1,0] neg_hi:[0,1,0]\n" \
"v_pk_fma_f32 v[108:109], v[46:47], v[70:71], v[108:109] op_sel_hi:[1,0,1] neg_lo:[0,1,0] neg_hi:[0,1,0]\n" \
"v_pk_fma_f32 v[88:89], v[50:51], v[70:71], v[88:89] op_sel_hi:[1,0,1] neg_lo:[0,1,0] neg_hi:[0,1,0]\n" \
"v_pk_mul_f32 v[110:111], v[102:103], v[76:77] op_sel_hi:[1,0]\n" \
"v_pk_mul_f32 v[90:91], v[114:115], v[76:77] op_sel_hi:[1,0]\n" \
"v_pk_mul_f32 v[112:113], v[104:105], v[76:77] op_sel_hi:[1,0]\n" \
"v_pk_mul_f32 v[92:93], v[116:117], v[76:77] op_sel_hi:[1,0]\n" \
"v_exp_f32 v110, v110\n" \
"v_exp_f32 v90, v90\n" \
"v_exp_f32 v111, v111\n" \
"v_exp_f32 v91, v91\n" \
"v_exp_f32 v112, v112\n" \
"v_exp_f32 v92, v92\n" \
"v_exp_f32 v113, v113\n" \
"v_exp_f32 v93, v93\n" \
"v_pk_mul_f32 v[106:107], v[102:103], v[106:107]\n" \
"v_pk_mul_f32 v[86:87], v[114:115], v[86:87]\n" \
"v_pk_mul_f32 v[108:109], v[104:105], v[108:109]\n" \
"v_pk_mul_f32 v[88:89], v[116:117], v[88:89]\n" \
"v_pk_fma_f32 v[110:111], v[110:111], v[74:75], v[74:75] op_sel_hi:[1,0,0]\n" \
"v_pk_fma_f32 v[90:91], v[90:91], v[74:75], v[74:75] op_sel_hi:[1,0,0]\n" \
"v_pk_fma_f32 v[112:113], v[112:113], v[74:75], v[74:75] op_sel_hi:[1,0,0]\n" \
"v_pk_fma_f32 v[92:93], v[92:93], v[74:75], v[74:75] op_sel_hi:[1,0,0]\n" \
"v_rcp_f32 v110, v110\n" \
"v_rcp_f32 v90, v90\n" \
"v_rcp_f32 v111, v111\n" \
"v_rcp_f32 v91, v91\n" \
"v_rcp_f32 v112, v112\n" \
"v_rcp_f32 v92, v92\n" \
"v_rcp_f32 v113, v113\n" \
"v_rcp_f32 v93, v93\n" \
"v_pk_mul_f32 v[106:107], v[106:107], v[110:111]\n" \
"v_pk_mul_f32 v[86:87], v[86:87], v[90:91]\n" \
"v_pk_mul_f32 v[108:109], v[108:109], v[112:113]\n" \
"v_pk_mul_f32 v[88:89], v[88:89], v[92:93]\n" \
"v_cvt_pk_bf16_f32 v94, v106, v107\n" \
"v_cvt_pk_bf16_f32 v96, v86, v87\n" \
"v_cvt_pk_bf16_f32 v95, v108, v109\n" \
"v_cvt_pk_bf16_f32 v97, v88, v89\n" \
"s_nop 1\n" \
"v_permlane16_swap_b32 v94, v96\n" \
"v_permlane16_swap_b32 v95, v97\n" \
"global_store_dwordx4 v252, v[94:97], s[56:57] offset:1024 sc1\n" \
"s_add_u32 s56, s56, 114688\n" \
"s_addc_u32 s57, s57, 0\n" \
"v_lshlrev_b32 v78, 16, v64\n" \
"v_and_b32 v82, 0xffff0000, v64\n" \
"v_rcp_f32 v80, v82\n" \
"v_mul_f32 v82, v82, v82\n" \
"v_mul_f32 v84, s94, v80\n" \
"s_nop 0\n" \
"v_accvgpr_read_b32 v98, a120\n" \
"v_accvgpr_read_b32 v110, a124\n" \
"v_accvgpr_read_b32 v99, a121\n" \
"v_accvgpr_read_b32 v111, a125\n" \
"v_accvgpr_read_b32 v100, a122\n" \
"v_accvgpr_read_b32 v112, a126\n" \
"v_accvgpr_read_b32 v101, a123\n" \
"v_accvgpr_read_b32 v113, a127\n" \
"v_accvgpr_read_b32 v102, a136\n" \
"v_accvgpr_read_b32 v114, a140\n" \
"v_accvgpr_read_b32 v103, a137\n" \
"v_accvgpr_read_b32 v115, a141\n" \
"v_accvgpr_read_b32 v104, a138\n" \
"v_accvgpr_read_b32 v116, a142\n" \
"v_accvgpr_read_b32 v105, a139\n" \
"v_accvgpr_read_b32 v117, a143\n" \
"v_pk_fma_f32 v[98:99], v[20:21], v[78:79], v[98:99] op_sel_hi:[1,0,1] neg_lo:[0,1,0] neg_hi:[0,1,0]\n" \
"v_pk_fma_f32 v[110:111], v[24:25], v[78:79], v[110:111] op_sel_hi:[1,0,1] neg_lo:[0,1,0] neg_hi:[0,1,0]\n" \
"v_pk_fma_f32 v[100:101], v[22:23], v[78:79], v[100:101] op_sel_hi:[1,0,1] neg_lo:[0,1,0] neg_hi:[0,1,0]\n" \
"v_pk_fma_f32 v[112:113], v[26:27], v[78:79], v[112:113] op_sel_hi:[1,0,1] neg_lo:[0,1,0] neg_hi:[0,1,0]\n" \
"v_pk_fma_f32 v[102:103], v[36:37], v[78:79], v[102:103] op_sel_hi:[1,0,1] neg_lo:[0,1,0] neg_hi:[0,1,0]\n" \
"v_pk_fma_f32 v[114:115], v[40:41], v[78:79], v[114:115] op_sel_hi:[1,0,1] neg_lo:[0,1,0] neg_hi:[0,1,0]\n" \
"v_pk_fma_f32 v[104:105], v[38:39], v[78:79], v[104:105] op_sel_hi:[1,0,1] neg_lo:[0,1,0] neg_hi:[0,1,0]\n" \
"v_pk_fma_f32 v[116:117], v[42:43], v[78:79], v[116:117] op_sel_hi:[1,0,1] neg_lo:[0,1,0] neg_hi:[0,1,0]\n" \
"v_pk_mul_f32 v[106:107], v[98:99], v[84:85] op_sel_hi:[1,0]\n" \
"v_pk_mul_f32 v[86:87], v[110:111], v[84:85] op_sel_hi:[1,0]\n" \
"v_pk_mul_f32 v[108:109], v[100:101], v[84:85] op_sel_hi:[1,0]\n" \
"v_pk_mul_f32 v[88:89], v[112:113], v[84:85] op_sel_hi:[1,0]\n" \
"v_exp_f32 v106, v106\n" \
"v_exp_f32 v86, v86\n" \
"v_exp_f32 v107, v107\n" \
"v_exp_f32 v87, v87\n" \
"v_exp_f32 v108, v108\n" \
"v_exp_f32 v88, v88\n" \
"v_exp_f32 v109, v109\n" \
"v_exp_f32 v89, v89\n" \
"v_pk_mul_f32 v[102:103], v[98:99], v[102:103]\n" \
"v_pk_mul_f32 v[114:115], v[110:111], v[114:115]\n" \
"v_pk_mul_f32 v[104:105], v[100:101], v[104:105]\n" \
"v_pk_mul_f32 v[116:117], v[112:113], v[116:117]\n" \
"v_pk_fma_f32 v[106:107], v[106:107], v[82:83], v[82:83] op_sel_hi:[1,0,0]\n" \
"v_pk_fma_f32 v[86:87], v[86:87], v[82:83], v[82:83] op_sel_hi:[1,0,0]\n" \
"v_pk_fma_f32 v[108:109], v[108:109], v[82:83], v[82:83] op_sel_hi:[1,0,0]\n" \
"v_pk_fma_f32 v[88:89], v[88:89], v[82:83], v[82:83] op_sel_hi:[1,0,0]\n" \
"v_rcp_f32 v106, v106\n" \
"v_rcp_f32 v86, v86\n" \
"v_rcp_f32 v107, v107\n" \
"v_rcp_f32 v87, v87\n" \
"v_rcp_f32 v108, v108\n" \
"v_rcp_f32 v88, v88\n" \
"v_rcp_f32 v109, v109\n" \
"v_rcp_f32 v89, v89\n" \
"v_pk_mul_f32 v[102:103], v[102:103], v[106:107]\n" \
"v_pk_mul_f32 v[114:115], v[114:115], v[86:87]\n" \
"v_pk_mul_f32 v[104:105], v[104:105], v[108:109]\n" \
"v_pk_mul_f32 v[116:117], v[116:117], v[88:89]\n" \
"v_cvt_pk_bf16_f32 v90, v102, v103\n" \
"v_cvt_pk_bf16_f32 v92, v114, v115\n" \
"v_cvt_pk_bf16_f32 v91, v104, v105\n" \
"v_cvt_pk_bf16_f32 v93, v116, v117\n" \
"s_nop 1\n" \
"v_permlane16_swap_b32 v90, v92\n" \
"v_permlane16_swap_b32 v91, v93\n" \
"global_store_dwordx4 v252, v[90:93], s[56:57] offset:0 sc1\n" \
"v_accvgpr_read_b32 v94, a128\n" \
"v_accvgpr_read_b32 v106, a132\n" \
"v_accvgpr_read_b32 v95, a129\n" \
"v_accvgpr_read_b32 v107, a133\n" \
"v_accvgpr_read_b32 v96, a130\n" \
"v_accvgpr_read_b32 v108, a134\n" \
"v_accvgpr_read_b32 v97, a131\n" \
"v_accvgpr_read_b32 v109, a135\n" \
"v_accvgpr_read_b32 v98, a144\n" \
"v_accvgpr_read_b32 v110, a148\n" \
"v_accvgpr_read_b32 v99, a145\n" \
"v_accvgpr_read_b32 v111, a149\n" \
"v_accvgpr_read_b32 v100, a146\n" \
"v_accvgpr_read_b32 v112, a150\n" \
"v_accvgpr_read_b32 v101, a147\n" \
"v_accvgpr_read_b32 v113, a151\n" \
"v_pk_fma_f32 v[94:95], v[28:29], v[78:79], v[94:95] op_sel_hi:[1,0,1] neg_lo:[0,1,0] neg_hi:[0,1,0]\n" \
"v_pk_fma_f32 v[106:107], v[32:33], v[78:79], v[106:107] op_sel_hi:[1,0,1] neg_lo:[0,1,0] neg_hi:[0,1,0]\n" \
"v_pk_fma_f32 v[96:97], v[30:31], v[78:79], v[96:97] op_sel_hi:[1,0,1] neg_lo:[0,1,0] neg_hi:[0,1,0]\n" \
"v_pk_fma_f32 v[108:109], v[34:35], v[78:79], v[108:109] op_sel_hi:[1,0,1] neg_lo:[0,1,0] neg_hi:[0,1,0]\n" \
"v_pk_fma_f32 v[98:99], v[44:45], v[78:79], v[98:99] op_sel_hi:[1,0,1] neg_lo:[0,1,0] neg_hi:[0,1,0]\n" \
"v_pk_fma_f32 v[110:111], v[48:49], v[78:79], v[110:111] op_sel_hi:[1,0,1] neg_lo:[0,1,0] neg_hi:[0,1,0]\n" \
"v_pk_fma_f32 v[100:101], v[46:47], v[78:79], v[100:101] op_sel_hi:[1,0,1] neg_lo:[0,1,0] neg_hi:[0,1,0]\n" \
"v_pk_fma_f32 v[112:113], v[50:51], v[78:79], v[112:113] op_sel_hi:[1,0,1] neg_lo:[0,1,0] neg_hi:[0,1,0]\n" \
"v_pk_mul_f32 v[102:103], v[94:95], v[84:85] op_sel_hi:[1,0]\n" \
"v_pk_mul_f32 v[114:115], v[106:107], v[84:85] op_sel_hi:[1,0]\n" \
"v_pk_mul_f32 v[104:105], v[96:97], v[84:85] op_sel_hi:[1,0]\n" \
"v_pk_mul_f32 v[116:117], v[108:109], v[84:85] op_sel_hi:[1,0]\n" \
"v_exp_f32 v102, v102\n" \
"v_exp_f32 v114, v114\n" \
"v_exp_f32 v103, v103\n" \
"v_exp_f32 v115, v115\n" \
"v_exp_f32 v104, v104\n" \
"v_exp_f32 v116, v116\n" \
"v_exp_f32 v105, v105\n" \
"v_exp_f32 v117, v117\n" \
"v_pk_mul_f32 v[98:99], v[94:95], v[98:99]\n" \
"v_pk_mul_f32 v[110:111], v[106:107], v[110:111]\n" \
"v_pk_mul_f32 v[100:101], v[96:97], v[100:101]\n" \
"v_pk_mul_f32 v[112:113], v[108:109], v[112:113]\n" \
"v_pk_fma_f32 v[102:103], v[102:103], v[82:83], v[82:83] op_sel_hi:[1,0,0]\n" \
"v_pk_fma_f32 v[114:115], v[114:115], v[82:83], v[82:83] op_sel_hi:[1,0,0]\n" \
"v_pk_fma_f32 v[104:105], v[104:105], v[82:83], v[82:83] op_sel_hi:[1,0,0]\n" \
"v_pk_fma_f32 v[116:117], v[116:117], v[82:83], v[82:83] op_sel_hi:[1,0,0]\n" \
"v_rcp_f32 v102, v102\n" \
"v_rcp_f32 v114, v114\n" \
"v_rcp_f32 v103, v103\n" \
"v_rcp_f32 v115, v115\n" \
"v_rcp_f32 v104, v104\n" \
"v_rcp_f32 v116, v116\n" \
"v_rcp_f32 v105, v105\n" \
"v_rcp_f32 v117, v117\n" \
"v_pk_mul_f32 v[98:99], v[98:99], v[102:103]\n" \
"v_pk_mul_f32 v[110:111], v[110:111], v[114:115]\n" \
"v_pk_mul_f32 v[100:101], v[100:101], v[104:105]\n" \
"v_pk_mul_f32 v[112:113], v[112:113], v[116:117]\n" \
"v_cvt_pk_bf16_f32 v86, v98, v99\n" \
"v_cvt_pk_bf16_f32 v88, v110, v111\n" \
"v_cvt_pk_bf16_f32 v87, v100, v101\n" \
"v_cvt_pk_bf16_f32 v89, v112, v113\n" \
"s_nop 1\n" \
"v_permlane16_swap_b32 v86, v88\n" \
"v_permlane16_swap_b32 v87, v89\n" \
"global_store_dwordx4 v252, v[86:89], s[56:57] offset:1024 sc1\n" \
"s_add_u32 s56, s56, 114688\n" \
"s_addc_u32 s57, s57, 0\n" \
"v_lshlrev_b32 v70, 16, v65\n" \
"v_and_b32 v74, 0xffff0000, v65\n" \
"v_rcp_f32 v72, v74\n" \
"v_mul_f32 v74, v74, v74\n" \
"v_mul_f32 v76, s94, v72\n" \
"s_nop 0\n" \
"v_accvgpr_read_b32 v90, a160\n" \
"v_accvgpr_read_b32 v102, a164\n" \
"v_accvgpr_read_b32 v91, a161\n" \
"v_accvgpr_read_b32 v103, a165\n" \
"v_accvgpr_read_b32 v92, a162\n" \
"v_accvgpr_read_b32 v104, a166\n" \
"v_accvgpr_read_b32 v93, a163\n" \
"v_accvgpr_read_b32 v105, a167\n" \
"v_accvgpr_read_b32 v94, a176\n" \
"v_accvgpr_read_b32 v106, a180\n" \
"v_accvgpr_read_b32 v95, a177\n" \
"v_accvgpr_read_b32 v107, a181\n" \
"v_accvgpr_read_b32 v96, a178\n" \
"v_accvgpr_read_b32 v108, a182\n" \
"v_accvgpr_read_b32 v97, a179\n" \
"v_accvgpr_read_b32 v109, a183\n" \
"v_pk_fma_f32 v[90:91], v[20:21], v[70:71], v[90:91] op_sel_hi:[1,0,1] neg_lo:[0,1,0] neg_hi:[0,1,0]\n" \
"v_pk_fma_f32 v[102:103], v[24:25], v[70:71], v[102:103] op_sel_hi:[1,0,1] neg_lo:[0,1,0] neg_hi:[0,1,0]\n" \
"v_pk_fma_f32 v[92:93], v[22:23], v[70:71], v[92:93] op_sel_hi:[1,0,1] neg_lo:[0,1,0] neg_hi:[0,1,0]\n" \
"v_pk_fma_f32 v[104:105], v[26:27], v[70:71], v[104:105] op_sel_hi:[1,0,1] neg_lo:[0,1,0] neg_hi:[0,1,0]\n" \
"v_pk_fma_f32 v[94:95], v[36:37], v[70:71], v[94:95] op_sel_hi:[1,0,1] neg_lo:[0,1,0] neg_hi:[0,1,0]\n" \
"v_pk_fma_f32 v[106:107], v[40:41], v[70:71], v[106:107] op_sel_hi:[1,0,1] neg_lo:[0,1,0] neg_hi:[0,1,0]\n" \
"v_pk_fma_f32 v[96:97], v[38:39], v[70:71], v[96:97] op_sel_hi:[1,0,1] neg_lo:[0,1,0] neg_hi:[0,1,0]\n" \
"v_pk_fma_f32 v[108:109], v[42:43], v[70:71], v[108:109] op_sel_hi:[1,0,1] neg_lo:[0,1,0] neg_hi:[0,1,0]\n" \
"v_pk_mul_f32 v[98:99], v[90:91], v[76:77] op_sel_hi:[1,0]\n" \
"v_pk_mul_f32 v[110:111], v[102:103], v[76:77] op_sel_hi:[1,0]\n" \
"v_pk_mul_f32 v[100:101], v[92:93], v[76:77] op_sel_hi:[1,0]\n" \
"v_pk_mul_f32 v[112:113], v[104:105], v[76:77] op_sel_hi:[1,0]\n" \
"v_exp_f32 v98, v98\n" \
"v_exp_f32 v110, v110\n" \
"v_exp_f32 v99, v99\n" \
"v_exp_f32 v111, v111\n" \
"v_exp_f32 v100, v100\n" \
"v_exp_f32 v112, v112\n" \
"v_exp_f32 v101, v101\n" \
"v_exp_f32 v113, v113\n" \
"v_pk_mul_f32 v[94:95], v[90:91], v[94:95]\n" \
"v_pk_mul_f32 v[106:107], v[102:103], v[106:107]\n" \
"v_pk_mul_f32 v[96:97], v[92:93], v[96:97]\n" \
"v_pk_mul_f32 v[108:109], v[104:105], v[108:109]\n" \
"v_pk_fma_f32 v[98:99], v[98:99], v[74:75], v[74:75] op_sel_hi:[1,0,0]\n" \
"v_pk_fma_f32 v[110:111], v[110:111], v[74:75], v[74:75] op_sel_hi:[1,0,0]\n" \
"v_pk_fma_f32 v[100:101], v[100:101], v[74:75], v[74:75] op_sel_hi:[1,0,0]\n" \
"v_pk_fma_f32 v[112:113], v[112:113], v[74:75], v[74:75] op_sel_hi:[1,0,0]\n" \
"v_rcp_f32 v98, v98\n" \
"v_rcp_f32 v110, v110\n" \
"v_rcp_f32 v99, v99\n" \
"v_rcp_f32 v111, v111\n" \
"v_rcp_f32 v100, v100\n" \
"v_rcp_f32 v112, v112\n" \
"v_rcp_f32 v101, v101\n" \
"v_rcp_f32 v113, v113\n" \
"v_pk_mul_f32 v[94:95], v[94:95], v[98:99]\n" \
"v_pk_mul_f32 v[106:107], v[106:107], v[110:111]\n" \
"v_pk_mul_f32 v[96:97], v[96:97], v[100:101]\n" \
"v_pk_mul_f32 v[108:109], v[108:109], v[112:113]\n" \
"v_cvt_pk_bf16_f32 v114, v94, v95\n" \
"v_cvt_pk_bf16_f32 v116, v106, v107\n" \
"v_cvt_pk_bf16_f32 v115, v96, v97\n" \
"v_cvt_pk_bf16_f32 v117, v108, v109\n" \
"s_nop 1\n" \
"v_permlane16_swap_b32 v114, v116\n" \
"v_permlane16_swap_b32 v115, v117\n" \
"global_store_dwordx4 v252, v[114:117], s[56:57] offset:0 sc1\n" \
"v_accvgpr_read_b32 v86, a168\n" \
"v_accvgpr_read_b32 v98, a172\n" \
"v_accvgpr_read_b32 v87, a169\n" \
"v_accvgpr_read_b32 v99, a173\n" \
"v_accvgpr_read_b32 v88, a170\n" \
"v_accvgpr_read_b32 v100, a174\n" \
"v_accvgpr_read_b32 v89, a171\n" \
"v_accvgpr_read_b32 v101, a175\n" \
"v_accvgpr_read_b32 v90, a184\n" \
"v_accvgpr_read_b32 v102, a188\n" \
"v_accvgpr_read_b32 v91, a185\n" \
"v_accvgpr_read_b32 v103, a189\n" \
"v_accvgpr_read_b32 v92, a186\n" \
"v_accvgpr_read_b32 v104, a190\n" \
"v_accvgpr_read_b32 v93, a187\n" \
"v_accvgpr_read_b32 v105, a191\n" \
"v_pk_fma_f32 v[86:87], v[28:29], v[70:71], v[86:87] op_sel_hi:[1,0,1] neg_lo:[0,1,0] neg_hi:[0,1,0]\n" \
"v_pk_fma_f32 v[98:99], v[32:33], v[70:71], v[98:99] op_sel_hi:[1,0,1] neg_lo:[0,1,0] neg_hi:[0,1,0]\n" \
"v_pk_fma_f32 v[88:89], v[30:31], v[70:71], v[88:89] op_sel_hi:[1,0,1] neg_lo:[0,1,0] neg_hi:[0,1,0]\n" \
"v_pk_fma_f32 v[100:101], v[34:35], v[70:71], v[100:101] op_sel_hi:[1,0,1] neg_lo:[0,1,0] neg_hi:[0,1,0]\n" \
"v_pk_fma_f32 v[90:91], v[44:45], v[70:71], v[90:91] op_sel_hi:[1,0,1] neg_lo:[0,1,0] neg_hi:[0,1,0]\n" \
"v_pk_fma_f32 v[102:103], v[48:49], v[70:71], v[102:103] op_sel_hi:[1,0,1] neg_lo:[0,1,0] neg_hi:[0,1,0]\n" \
"v_pk_fma_f32 v[92:93], v[46:47], v[70:71], v[92:93] op_sel_hi:[1,0,1] neg_lo:[0,1,0] neg_hi:[0,1,0]\n" \
"v_pk_fma_f32 v[104:105], v[50:51], v[70:71], v[104:105] op_sel_hi:[1,0,1] neg_lo:[0,1,0] neg_hi:[0,1,0]\n" \
"v_pk_mul_f32 v[94:95], v[86:87], v[76:77] op_sel_hi:[1,0]\n" \
"v_pk_mul_f32 v[106:107], v[98:99], v[76:77] op_sel_hi:[1,0]\n" \
"v_pk_mul_f32 v[96:97], v[88:89], v[76:77] op_sel_hi:[1,0]\n" \
"v_pk_mul_f32 v[108:109], v[100:101], v[76:77] op_sel_hi:[1,0]\n" \
"v_exp_f32 v94, v94\n" \
"v_exp_f32 v106, v106\n" \
"v_exp_f32 v95, v95\n" \
"v_exp_f32 v107, v107\n" \
"v_exp_f32 v96, v96\n" \
"v_exp_f32 v108, v108\n" \
"v_exp_f32 v97, v97\n" \
"v_exp_f32 v109, v109\n" \
"v_pk_mul_f32 v[90:91], v[86:87], v[90:91]\n" \
"v_pk_mul_f32 v[102:103], v[98:99], v[102:103]\n" \
"v_pk_mul_f32 v[92:93], v[88:89], v[92:93]\n" \
"v_pk_mul_f32 v[104:105], v[100:101], v[104:105]\n" \
"v_pk_fma_f32 v[94:95], v[94:95], v[74:75], v[74:75] op_sel_hi:[1,0,0]\n" \
"v_pk_fma_f32 v[106:107], v[106:107], v[74:75], v[74:75] op_sel_hi:[1,0,0]\n" \
"v_pk_fma_f32 v[96:97], v[96:97], v[74:75], v[74:75] op_sel_hi:[1,0,0]\n" \
"v_pk_fma_f32 v[108:109], v[108:109], v[74:75], v[74:75] op_sel_hi:[1,0,0]\n" \
"v_rcp_f32 v94, v94\n" \
"v_rcp_f32 v106, v106\n" \
"v_rcp_f32 v95, v95\n" \
"v_rcp_f32 v107, v107\n" \
"v_rcp_f32 v96, v96\n" \
"v_rcp_f32 v108, v108\n" \
"v_rcp_f32 v97, v97\n" \
"v_rcp_f32 v109, v109\n" \
"v_pk_mul_f32 v[90:91], v[90:91], v[94:95]\n" \
"v_pk_mul_f32 v[102:103], v[102:103], v[106:107]\n" \
"v_pk_mul_f32 v[92:93], v[92:93], v[96:97]\n" \
"v_pk_mul_f32 v[104:105], v[104:105], v[108:109]\n" \
"v_cvt_pk_bf16_f32 v110, v90, v91\n" \
"v_cvt_pk_bf16_f32 v112, v102, v103\n" \
"v_cvt_pk_bf16_f32 v111, v92, v93\n" \
"v_cvt_pk_bf16_f32 v113, v104, v105\n" \
"s_nop 1\n" \
"v_permlane16_swap_b32 v110, v112\n" \
"v_permlane16_swap_b32 v111, v113\n" \
"global_store_dwordx4 v252, v[110:113], s[56:57] offset:1024 sc1\n" \
"s_add_u32 s56, s56, 114688\n" \
"s_addc_u32 s57, s57, 0\n" \
"v_lshlrev_b32 v78, 16, v66\n" \
"v_and_b32 v82, 0xffff0000, v66\n" \
"v_rcp_f32 v80, v82\n" \
"v_mul_f32 v82, v82, v82\n" \
"v_mul_f32 v84, s94, v80\n" \
"s_nop 0\n" \
"v_pk_fma_f32 v[124:125], v[20:21], v[78:79], v[124:125] op_sel_hi:[1,0,1] neg_lo:[0,1,0] neg_hi:[0,1,0]\n" \
"v_pk_fma_f32 v[128:129], v[24:25], v[78:79], v[128:129] op_sel_hi:[1,0,1] neg_lo:[0,1,0] neg_hi:[0,1,0]\n" \
"v_pk_fma_f32 v[126:127], v[22:23], v[78:79], v[126:127] op_sel_hi:[1,0,1] neg_lo:[0,1,0] neg_hi:[0,1,0]\n" \
"v_pk_fma_f32 v[130:131], v[26:27], v[78:79], v[130:131] op_sel_hi:[1,0,1] neg_lo:[0,1,0] neg_hi:[0,1,0]\n" \
"v_pk_fma_f32 v[140:141], v[36:37], v[78:79], v[140:141] op_sel_hi:[1,0,1] neg_lo:[0,1,0] neg_hi:[0,1,0]\n" \
"v_pk_fma_f32 v[144:145], v[40:41], v[78:79], v[144:145] op_sel_hi:[1,0,1] neg_lo:[0,1,0] neg_hi:[0,1,0]\n" \
"v_pk_fma_f32 v[142:143], v[38:39], v[78:79], v[142:143] op_sel_hi:[1,0,1] neg_lo:[0,1,0] neg_hi:[0,1,0]\n" \
"v_pk_fma_f32 v[146:147], v[42:43], v[78:79], v[146:147] op_sel_hi:[1,0,1] neg_lo:[0,1,0] neg_hi:[0,1,0]\n" \
"v_pk_mul_f32 v[114:115], v[124:125], v[84:85] op_sel_hi:[1,0]\n" \
"v_pk_mul_f32 v[86:87], v[128:129], v[84:85] op_sel_hi:[1,0]\n" \
"v_pk_mul_f32 v[116:117], v[126:127], v[84:85] op_sel_hi:[1,0]\n" \
"v_pk_mul_f32 v[88:89], v[130:131], v[84:85] op_sel_hi:[1,0]\n" \
"v_exp_f32 v114, v114\n" \
"v_exp_f32 v86, v86\n" \
"v_exp_f32 v115, v115\n" \
"v_exp_f32 v87, v87\n" \
"v_exp_f32 v116, v116\n" \
"v_exp_f32 v88, v88\n" \
"v_exp_f32 v117, v117\n" \
"v_exp_f32 v89, v89\n" \
"v_pk_mul_f32 v[140:141], v[124:125], v[140:141]\n" \
"v_pk_mul_f32 v[144:145], v[128:129], v[144:145]\n" \
"v_pk_mul_f32 v[142:143], v[126:127], v[142:143]\n" \
"v_pk_mul_f32 v[146:147], v[130:131], v[146:147]\n" \
"v_pk_fma_f32 v[114:115], v[114:115], v[82:83], v[82:83] op_sel_hi:[1,0,0]\n" \
"v_pk_fma_f32 v[86:87], v[86:87], v[82:83], v[82:83] op_sel_hi:[1,0,0]\n" \
"v_pk_fma_f32 v[116:117], v[116:117], v[82:83], v[82:83] op_sel_hi:[1,0,0]\n" \
"v_pk_fma_f32 v[88:89], v[88:89], v[82:83], v[82:83] op_sel_hi:[1,0,0]\n" \
"v_rcp_f32 v114, v114\n" \
"v_rcp_f32 v86, v86\n" \
"v_rcp_f32 v115, v115\n" \
"v_rcp_f32 v87, v87\n" \
"v_rcp_f32 v116, v116\n" \
"v_rcp_f32 v88, v88\n" \
"v_rcp_f32 v117, v117\n" \
"v_rcp_f32 v89, v89\n" \
"v_pk_mul_f32 v[140:141], v[140:141], v[114:115]\n" \
"v_pk_mul_f32 v[144:145], v[144:145], v[86:87]\n" \
"v_pk_mul_f32 v[142:143], v[142:143], v[116:117]\n" \
"v_pk_mul_f32 v[146:147], v[146:147], v[88:89]\n" \
"v_cvt_pk_bf16_f32 v90, v140, v141\n" \
"v_cvt_pk_bf16_f32 v92, v144, v145\n" \
"v_cvt_pk_bf16_f32 v91, v142, v143\n" \
"v_cvt_pk_bf16_f32 v93, v146, v147\n" \
"s_nop 1\n" \
"v_permlane16_swap_b32 v90, v92\n" \
"v_permlane16_swap_b32 v91, v93\n" \
"global_store_dwordx4 v252, v[90:93], s[56:57] offset:0 sc1\n" \
"v_pk_fma_f32 v[132:133], v[28:29], v[78:79], v[132:133] op_sel_hi:[1,0,1] neg_lo:[0,1,0] neg_hi:[0,1,0]\n" \
"v_pk_fma_f32 v[136:137], v[32:33], v[78:79], v[136:137] op_sel_hi:[1,0,1] neg_lo:[0,1,0] neg_hi:[0,1,0]\n" \
"v_pk_fma_f32 v[134:135], v[30:31], v[78:79], v[134:135] op_sel_hi:[1,0,1] neg_lo:[0,1,0] neg_hi:[0,1,0]\n" \
"v_pk_fma_f32 v[138:139], v[34:35], v[78:79], v[138:139] op_sel_hi:[1,0,1] neg_lo:[0,1,0] neg_hi:[0,1,0]\n" \
"v_pk_fma_f32 v[148:149], v[44:45], v[78:79], v[148:149] op_sel_hi:[1,0,1] neg_lo:[0,1,0] neg_hi:[0,1,0]\n" \
"v_pk_fma_f32 v[152:153], v[48:49], v[78:79], v[152:153] op_sel_hi:[1,0,1] neg_lo:[0,1,0] neg_hi:[0,1,0]\n" \
"v_pk_fma_f32 v[150:151], v[46:47], v[78:79], v[150:151] op_sel_hi:[1,0,1] neg_lo:[0,1,0] neg_hi:[0,1,0]\n" \
"v_pk_fma_f32 v[154:155], v[50:51], v[78:79], v[154:155] op_sel_hi:[1,0,1] neg_lo:[0,1,0] neg_hi:[0,1,0]\n" \
"v_pk_mul_f32 v[94:95], v[132:133], v[84:85] op_sel_hi:[1,0]\n" \
"v_pk_mul_f32 v[98:99], v[136:137], v[84:85] op_sel_hi:[1,0]\n" \
"v_pk_mul_f32 v[96:97], v[134:135], v[84:85] op_sel_hi:[1,0]\n" \
"v_pk_mul_f32 v[100:101], v[138:139], v[84:85] op_sel_hi:[1,0]\n" \
"v_exp_f32 v94, v94\n" \
"v_exp_f32 v98, v98\n" \
"v_exp_f32 v95, v95\n" \
"v_exp_f32 v99, v99\n" \
"v_exp_f32 v96, v96\n" \
"v_exp_f32 v100, v100\n" \
"v_exp_f32 v97, v97\n" \
"v_exp_f32 v101, v101\n" \
"v_pk_mul_f32 v[148:149], v[132:133], v[148:149]\n" \
"v_pk_mul_f32 v[152:153], v[136:137], v[152:153]\n" \
"v_pk_mul_f32 v[150:151], v[134:135], v[150:151]\n" \
"v_pk_mul_f32 v[154:155], v[138:139], v[154:155]\n" \
"v_pk_fma_f32 v[94:95], v[94:95], v[82:83], v[82:83] op_sel_hi:[1,0,0]\n" \
"v_pk_fma_f32 v[98:99], v[98:99], v[82:83], v[82:83] op_sel_hi:[1,0,0]\n" \
"v_pk_fma_f32 v[96:97], v[96:97], v[82:83], v[82:83] op_sel_hi:[1,0,0]\n" \
"v_pk_fma_f32 v[100:101], v[100:101], v[82:83], v[82:83] op_sel_hi:[1,0,0]\n" \
"v_rcp_f32 v94, v94\n" \
"v_rcp_f32 v98, v98\n" \
"v_rcp_f32 v95, v95\n" \
"v_rcp_f32 v99, v99\n" \
"v_rcp_f32 v96, v96\n" \
"v_rcp_f32 v100, v100\n" \
"v_rcp_f32 v97, v97\n" \
"v_rcp_f32 v101, v101\n" \
"v_pk_mul_f32 v[148:149], v[148:149], v[94:95]\n" \
"v_pk_mul_f32 v[152:153], v[152:153], v[98:99]\n" \
"v_pk_mul_f32 v[150:151], v[150:151], v[96:97]\n" \
"v_pk_mul_f32 v[154:155], v[154:155], v[100:101]\n" \
"v_cvt_pk_bf16_f32 v102, v148, v149\n" \
"v_cvt_pk_bf16_f32 v104, v152, v153\n" \
"v_cvt_pk_bf16_f32 v103, v150, v151\n" \
"v_cvt_pk_bf16_f32 v105, v154, v155\n" \
"s_nop 1\n" \
"v_permlane16_swap_b32 v102, v104\n" \
"v_permlane16_swap_b32 v103, v105\n" \
"global_store_dwordx4 v252, v[102:105], s[56:57] offset:1024 sc1\n" \
"s_add_u32 s56, s56, 114688\n" \
"s_addc_u32 s57, s57, 0\n" \
"v_lshlrev_b32 v70, 16, v67\n" \
"v_and_b32 v74, 0xffff0000, v67\n" \
"v_rcp_f32 v72, v74\n" \
"v_mul_f32 v74, v74, v74\n" \
"v_mul_f32 v76, s94, v72\n" \
"s_nop 0\n" \
"v_pk_fma_f32 v[164:165], v[20:21], v[70:71], v[164:165] op_sel_hi:[1,0,1] neg_lo:[0,1,0] neg_hi:[0,1,0]\n" \
"v_pk_fma_f32 v[168:169], v[24:25], v[70:71], v[168:169] op_sel_hi:[1,0,1] neg_lo:[0,1,0] neg_hi:[0,1,0]\n" \
"v_pk_fma_f32 v[166:167], v[22:23], v[70:71], v[166:167] op_sel_hi:[1,0,1] neg_lo:[0,1,0] neg_hi:[0,1,0]\n" \
"v_pk_fma_f32 v[170:171], v[26:27], v[70:71], v[170:171] op_sel_hi:[1,0,1] neg_lo:[0,1,0] neg_hi:[0,1,0]\n" \
"v_pk_fma_f32 v[180:181], v[36:37], v[70:71], v[180:181] op_sel_hi:[1,0,1] neg_lo:[0,1,0] neg_hi:[0,1,0]\n" \
"v_pk_fma_f32 v[184:185], v[40:41], v[70:71], v[184:185] op_sel_hi:[1,0,1] neg_lo:[0,1,0] neg_hi:[0,1,0]\n" \
"v_pk_fma_f32 v[182:183], v[38:39], v[70:71], v[182:183] op_sel_hi:[1,0,1] neg_lo:[0,1,0] neg_hi:[0,1,0]\n" \
"v_pk_fma_f32 v[186:187], v[42:43], v[70:71], v[186:187] op_sel_hi:[1,0,1] neg_lo:[0,1,0] neg_hi:[0,1,0]\n" \
"v_pk_mul_f32 v[106:107], v[164:165], v[76:77] op_sel_hi:[1,0]\n" \
"v_pk_mul_f32 v[110:111], v[168:169], v[76:77] op_sel_hi:[1,0]\n" \
"v_pk_mul_f32 v[108:109], v[166:167], v[76:77] op_sel_hi:[1,0]\n" \
"v_pk_mul_f32 v[112:113], v[170:171], v[76:77] op_sel_hi:[1,0]\n" \
"v_exp_f32 v106, v106\n" \
"v_exp_f32 v110, v110\n" \
"v_exp_f32 v107, v107\n" \
"v_exp_f32 v111, v111\n" \
"v_exp_f32 v108, v108\n" \
"v_exp_f32 v112, v112\n" \
"v_exp_f32 v109, v109\n" \
"v_exp_f32 v113, v113\n" \
"v_pk_mul_f32 v[180:181], v[164:165], v[180:181]\n" \
"v_pk_mul_f32 v[184:185], v[168:169], v[184:185]\n" \
"v_pk_mul_f32 v[182:183], v[166:167], v[182:183]\n" \
"v_pk_mul_f32 v[186:187], v[170:171], v[186:187]\n" \
"v_pk_fma_f32 v[106:107], v[106:107], v[74:75], v[74:75] op_sel_hi:[1,0,0]\n" \
"v_pk_fma_f32 v[110:111], v[110:111], v[74:75], v[74:75] op_sel_hi:[1,0,0]\n" \
"v_pk_fma_f32 v[108:109], v[108:109], v[74:75], v[74:75] op_sel_hi:[1,0,0]\n" \
"v_pk_fma_f32 v[112:113], v[112:113], v[74:75], v[74:75] op_sel_hi:[1,0,0]\n" \
"v_rcp_f32 v106, v106\n" \
"v_rcp_f32 v110, v110\n" \
"v_rcp_f32 v107, v107\n" \
"v_rcp_f32 v111, v111\n" \
"v_rcp_f32 v108, v108\n" \
"v_rcp_f32 v112, v112\n" \
"v_rcp_f32 v109, v109\n" \
"v_rcp_f32 v113, v113\n" \
"v_pk_mul_f32 v[180:181], v[180:181], v[106:107]\n" \
"v_pk_mul_f32 v[184:185], v[184:185], v[110:111]\n" \
"v_pk_mul_f32 v[182:183], v[182:183], v[108:109]\n" \
"v_pk_mul_f32 v[186:187], v[186:187], v[112:113]\n" \
"v_cvt_pk_bf16_f32 v114, v180, v181\n" \
"v_cvt_pk_bf16_f32 v116, v184, v185\n" \
"v_cvt_pk_bf16_f32 v115, v182, v183\n" \
"v_cvt_pk_bf16_f32 v117, v186, v187\n" \
"s_nop 1\n" \
"v_permlane16_swap_b32 v114, v116\n" \
"v_permlane16_swap_b32 v115, v117\n" \
"global_store_dwordx4 v252, v[114:117], s[56:57] offset:0 sc1\n" \
"v_pk_fma_f32 v[172:173], v[28:29], v[70:71], v[172:173] op_sel_hi:[1,0,1] neg_lo:[0,1,0] neg_hi:[0,1,0]\n" \
"v_pk_fma_f32 v[176:177], v[32:33], v[70:71], v[176:177] op_sel_hi:[1,0,1] neg_lo:[0,1,0] neg_hi:[0,1,0]\n" \
"v_pk_fma_f32 v[174:175], v[30:31], v[70:71], v[174:175] op_sel_hi:[1,0,1] neg_lo:[0,1,0] neg_hi:[0,1,0]\n" \
"v_pk_fma_f32 v[178:179], v[34:35], v[70:71], v[178:179] op_sel_hi:[1,0,1] neg_lo:[0,1,0] neg_hi:[0,1,0]\n" \
"v_pk_fma_f32 v[188:189], v[44:45], v[70:71], v[188:189] op_sel_hi:[1,0,1] neg_lo:[0,1,0] neg_hi:[0,1,0]\n" \
"v_pk_fma_f32 v[192:193], v[48:49], v[70:71], v[192:193] op_sel_hi:[1,0,1] neg_lo:[0,1,0] neg_hi:[0,1,0]\n" \
"v_pk_fma_f32 v[190:191], v[46:47], v[70:71], v[190:191] op_sel_hi:[1,0,1] neg_lo:[0,1,0] neg_hi:[0,1,0]\n" \
"v_pk_fma_f32 v[194:195], v[50:51], v[70:71], v[194:195] op_sel_hi:[1,0,1] neg_lo:[0,1,0] neg_hi:[0,1,0]\n" \
"v_pk_mul_f32 v[86:87], v[172:173], v[76:77] op_sel_hi:[1,0]\n" \
"v_pk_mul_f32 v[90:91], v[176:177], v[76:77] op_sel_hi:[1,0]\n" \
"v_pk_mul_f32 v[88:89], v[174:175], v[76:77] op_sel_hi:[1,0]\n" \
"v_pk_mul_f32 v[92:93], v[178:179], v[76:77] op_sel_hi:[1,0]\n" \
"v_exp_f32 v86, v86\n" \
"v_exp_f32 v90, v90\n" \
"v_exp_f32 v87, v87\n" \
"v_exp_f32 v91, v91\n" \
"v_exp_f32 v88, v88\n" \
"v_exp_f32 v92, v92\n" \
"v_exp_f32 v89, v89\n" \
"v_exp_f32 v93, v93\n" \
"v_pk_mul_f32 v[188:189], v[172:173], v[188:189]\n" \
"v_pk_mul_f32 v[192:193], v[176:177], v[192:193]\n" \
"v_pk_mul_f32 v[190:191], v[174:175], v[190:191]\n" \
"v_pk_mul_f32 v[194:195], v[178:179], v[194:195]\n" \
"v_pk_fma_f32 v[86:87], v[86:87], v[74:75], v[74:75] op_sel_hi:[1,0,0]\n" \
"v_pk_fma_f32 v[90:91], v[90:91], v[74:75], v[74:75] op_sel_hi:[1,0,0]\n" \
"v_pk_fma_f32 v[88:89], v[88:89], v[74:75], v[74:75] op_sel_hi:[1,0,0]\n" \
"v_pk_fma_f32 v[92:93], v[92:93], v[74:75], v[74:75] op_sel_hi:[1,0,0]\n" \
"v_rcp_f32 v86, v86\n" \
"v_rcp_f32 v90, v90\n" \
"v_rcp_f32 v87, v87\n" \
"v_rcp_f32 v91, v91\n" \
"v_rcp_f32 v88, v88\n" \
"v_rcp_f32 v92, v92\n" \
"v_rcp_f32 v89, v89\n" \
"v_rcp_f32 v93, v93\n" \
"v_pk_mul_f32 v[188:189], v[188:189], v[86:87]\n" \
"v_pk_mul_f32 v[192:193], v[192:193], v[90:91]\n" \
"v_pk_mul_f32 v[190:191], v[190:191], v[88:89]\n" \
"v_pk_mul_f32 v[194:195], v[194:195], v[92:93]\n" \
"v_cvt_pk_bf16_f32 v94, v188, v189\n" \
"v_cvt_pk_bf16_f32 v96, v192, v193\n" \
"v_cvt_pk_bf16_f32 v95, v190, v191\n" \
"v_cvt_pk_bf16_f32 v97, v194, v195\n" \
"s_nop 1\n" \
"v_permlane16_swap_b32 v94, v96\n" \
"v_permlane16_swap_b32 v95, v97\n" \
"global_store_dwordx4 v252, v[94:97], s[56:57] offset:1024 sc1\n" \
"s_add_u32 s56, s56, 114688\n" \
"s_addc_u32 s57, s57, 0\n" \
"v_lshlrev_b32 v78, 16, v68\n" \
"v_and_b32 v82, 0xffff0000, v68\n" \
"v_rcp_f32 v80, v82\n" \
"v_mul_f32 v82, v82, v82\n" \
"v_mul_f32 v84, s94, v80\n" \
"s_nop 0\n" \
"v_pk_fma_f32 v[204:205], v[20:21], v[78:79], v[204:205] op_sel_hi:[1,0,1] neg_lo:[0,1,0] neg_hi:[0,1,0]\n" \
"v_pk_fma_f32 v[208:209], v[24:25], v[78:79], v[208:209] op_sel_hi:[1,0,1] neg_lo:[0,1,0] neg_hi:[0,1,0]\n" \
"v_pk_fma_f32 v[206:207], v[22:23], v[78:79], v[206:207] op_sel_hi:[1,0,1] neg_lo:[0,1,0] neg_hi:[0,1,0]\n" \
"v_pk_fma_f32 v[210:211], v[26:27], v[78:79], v[210:211] op_sel_hi:[1,0,1] neg_lo:[0,1,0] neg_hi:[0,1,0]\n" \
"v_pk_fma_f32 v[220:221], v[36:37], v[78:79], v[220:221] op_sel_hi:[1,0,1] neg_lo:[0,1,0] neg_hi:[0,1,0]\n" \
"v_pk_fma_f32 v[224:225], v[40:41], v[78:79], v[224:225] op_sel_hi:[1,0,1] neg_lo:[0,1,0] neg_hi:[0,1,0]\n" \
"v_pk_fma_f32 v[222:223], v[38:39], v[78:79], v[222:223] op_sel_hi:[1,0,1] neg_lo:[0,1,0] neg_hi:[0,1,0]\n" \
"v_pk_fma_f32 v[226:227], v[42:43], v[78:79], v[226:227] op_sel_hi:[1,0,1] neg_lo:[0,1,0] neg_hi:[0,1,0]\n" \
"v_pk_mul_f32 v[98:99], v[204:205], v[84:85] op_sel_hi:[1,0]\n" \
"v_pk_mul_f32 v[102:103], v[208:209], v[84:85] op_sel_hi:[1,0]\n" \
"v_pk_mul_f32 v[100:101], v[206:207], v[84:85] op_sel_hi:[1,0]\n" \
"v_pk_mul_f32 v[104:105], v[210:211], v[84:85] op_sel_hi:[1,0]\n" \
"v_exp_f32 v98, v98\n" \
"v_exp_f32 v102, v102\n" \
"v_exp_f32 v99, v99\n" \
"v_exp_f32 v103, v103\n" \
"v_exp_f32 v100, v100\n" \
"v_exp_f32 v104, v104\n" \
"v_exp_f32 v101, v101\n" \
"v_exp_f32 v105, v105\n" \
"v_pk_mul_f32 v[220:221], v[204:205], v[220:221]\n" \
"v_pk_mul_f32 v[224:225], v[208:209], v[224:225]\n" \
"v_pk_mul_f32 v[222:223], v[206:207], v[222:223]\n" \
"v_pk_mul_f32 v[226:227], v[210:211], v[226:227]\n" \
"v_pk_fma_f32 v[98:99], v[98:99], v[82:83], v[82:83] op_sel_hi:[1,0,0]\n" \
"v_pk_fma_f32 v[102:103], v[102:103], v[82:83], v[82:83] op_sel_hi:[1,0,0]\n" \
"v_pk_fma_f32 v[100:101], v[100:101], v[82:83], v[82:83] op_sel_hi:[1,0,0]\n" \
"v_pk_fma_f32 v[104:105], v[104:105], v[82:83], v[82:83] op_sel_hi:[1,0,0]\n" \
"v_rcp_f32 v98, v98\n" \
"v_rcp_f32 v102, v102\n" \
"v_rcp_f32 v99, v99\n" \
"v_rcp_f32 v103, v103\n" \
"v_rcp_f32 v100, v100\n" \
"v_rcp_f32 v104, v104\n" \
"v_rcp_f32 v101, v101\n" \
"v_rcp_f32 v105, v105\n" \
"v_pk_mul_f32 v[220:221], v[220:221], v[98:99]\n" \
"v_pk_mul_f32 v[224:225], v[224:225], v[102:103]\n" \
"v_pk_mul_f32 v[222:223], v[222:223], v[100:101]\n" \
"v_pk_mul_f32 v[226:227], v[226:227], v[104:105]\n" \
"v_cvt_pk_bf16_f32 v106, v220, v221\n" \
"v_cvt_pk_bf16_f32 v108, v224, v225\n" \
"v_cvt_pk_bf16_f32 v107, v222, v223\n" \
"v_cvt_pk_bf16_f32 v109, v226, v227\n" \
"s_nop 1\n" \
"v_permlane16_swap_b32 v106, v108\n" \
"v_permlane16_swap_b32 v107, v109\n" \
"global_store_dwordx4 v252, v[106:109], s[56:57] offset:0 sc1\n" \
"v_pk_fma_f32 v[212:213], v[28:29], v[78:79], v[212:213] op_sel_hi:[1,0,1] neg_lo:[0,1,0] neg_hi:[0,1,0]\n" \
"v_pk_fma_f32 v[216:217], v[32:33], v[78:79], v[216:217] op_sel_hi:[1,0,1] neg_lo:[0,1,0] neg_hi:[0,1,0]\n" \
"v_pk_fma_f32 v[214:215], v[30:31], v[78:79], v[214:215] op_sel_hi:[1,0,1] neg_lo:[0,1,0] neg_hi:[0,1,0]\n" \
"v_pk_fma_f32 v[218:219], v[34:35], v[78:79], v[218:219] op_sel_hi:[1,0,1] neg_lo:[0,1,0] neg_hi:[0,1,0]\n" \
"v_pk_fma_f32 v[228:229], v[44:45], v[78:79], v[228:229] op_sel_hi:[1,0,1] neg_lo:[0,1,0] neg_hi:[0,1,0]\n" \
"v_pk_fma_f32 v[232:233], v[48:49], v[78:79], v[232:233] op_sel_hi:[1,0,1] neg_lo:[0,1,0] neg_hi:[0,1,0]\n" \
"v_pk_fma_f32 v[230:231], v[46:47], v[78:79], v[230:231] op_sel_hi:[1,0,1] neg_lo:[0,1,0] neg_hi:[0,1,0]\n" \
"v_pk_fma_f32 v[234:235], v[50:51], v[78:79], v[234:235] op_sel_hi:[1,0,1] neg_lo:[0,1,0] neg_hi:[0,1,0]\n" \
"v_pk_mul_f32 v[110:111], v[212:213], v[84:85] op_sel_hi:[1,0]\n" \
"v_pk_mul_f32 v[114:115], v[216:217], v[84:85] op_sel_hi:[1,0]\n" \
"v_pk_mul_f32 v[112:113], v[214:215], v[84:85] op_sel_hi:[1,0]\n" \
"v_pk_mul_f32 v[116:117], v[218:219], v[84:85] op_sel_hi:[1,0]\n" \
"v_exp_f32 v110, v110\n" \
"v_exp_f32 v114, v114\n" \
"v_exp_f32 v111, v111\n" \
"v_exp_f32 v115, v115\n" \
"v_exp_f32 v112, v112\n" \
"v_exp_f32 v116, v116\n" \
"v_exp_f32 v113, v113\n" \
"v_exp_f32 v117, v117\n" \
"v_pk_mul_f32 v[228:229], v[212:213], v[228:229]\n" \
"v_pk_mul_f32 v[232:233], v[216:217], v[232:233]\n" \
"v_pk_mul_f32 v[230:231], v[214:215], v[230:231]\n" \
"v_pk_mul_f32 v[234:235], v[218:219], v[234:235]\n" \
"v_pk_fma_f32 v[110:111], v[110:111], v[82:83], v[82:83] op_sel_hi:[1,0,0]\n" \
"v_pk_fma_f32 v[114:115], v[114:115], v[82:83], v[82:83] op_sel_hi:[1,0,0]\n" \
"v_pk_fma_f32 v[112:113], v[112:113], v[82:83], v[82:83] op_sel_hi:[1,0,0]\n" \
"v_pk_fma_f32 v[116:117], v[116:117], v[82:83], v[82:83] op_sel_hi:[1,0,0]\n" \
"v_rcp_f32 v110, v110\n" \
"v_rcp_f32 v114, v114\n" \
"v_rcp_f32 v111, v111\n" \
"v_rcp_f32 v115, v115\n" \
"v_rcp_f32 v112, v112\n" \
"v_rcp_f32 v116, v116\n" \
"v_rcp_f32 v113, v113\n" \
"v_rcp_f32 v117, v117\n" \
"v_pk_mul_f32 v[228:229], v[228:229], v[110:111]\n" \
"v_pk_mul_f32 v[232:233], v[232:233], v[114:115]\n" \
"v_pk_mul_f32 v[230:231], v[230:231], v[112:113]\n" \
"v_pk_mul_f32 v[234:235], v[234:235], v[116:117]\n" \
"v_cvt_pk_bf16_f32 v86, v228, v229\n" \
"v_cvt_pk_bf16_f32 v88, v232, v233\n" \
"v_cvt_pk_bf16_f32 v87, v230, v231\n" \
"v_cvt_pk_bf16_f32 v89, v234, v235\n" \
"s_nop 1\n" \
"v_permlane16_swap_b32 v86, v88\n" \
"v_permlane16_swap_b32 v87, v89\n" \
"global_store_dwordx4 v252, v[86:89], s[56:57] offset:1024 sc1\n" \
"s_add_u32 s56, s56, 114688\n" \
"s_addc_u32 s57, s57, 0\n" \
K1_STAMP3 \
"v_mbcnt_lo_u32_b32 v8, -1, 0\n" \
"v_mbcnt_hi_u32_b32 v8, -1, v8\n" \
"v_and_b32 v9, 15, v8\n" \
"v_lshrrev_b32 v10, 5, v8\n" \
"v_bfe_u32 v11, v8, 4, 1\n" \
"v_lshlrev_b32 v14, 9, v10\n" \
"v_lshl_add_u32 v14, v9, 5, v14\n" \
"v_lshl_add_u32 v14, v11, 4, v14\n" \
"v_lshl_add_u32 v15, v10, 13, v14\n" \
"v_add_u32 v15, s68, v15\n" \
"v_add_u32 v14, s67, v14\n" \
"v_lshrrev_b32 v16, 2, v8\n" \
"v_lshlrev_b32 v16, 5, v16\n" \
"v_and_b32 v12, 3, v8\n" \
"v_lshl_add_u32 v16, v12, 3, v16\n" \
"v_add_u32 v16, s69, v16\n" \
"v_lshlrev_b32 v17, 9, v11\n" \
"v_lshl_add_u32 v17, v9, 5, v17\n" \
"v_lshl_add_u32 v17, v10, 4, v17\n" \
"v_add_u32 v17, s70, v17\n" \
"v_lshrrev_b32 v12, 4, v8\n" \
"v_lshlrev_b32 v12, 2, v12\n" \
"v_sub_u32 v12, v9, v12\n" \
"v_and_b32 v10, 1, v9\n" \
"v_cmp_eq_u32 s[40:41], 0, v10\n" \
"v_cmp_ne_u32 s[42:43], 0, v10\n" \
"s_mov_b64 s[88:89], s[58:59]\n" \
"s_mov_b32 s96, 0x3e8293ee\n" \
"v_add_u32 v11, 16, v12\n" \
"v_cmp_ge_u32 s[56:57], 14, v11\n" \
"s_nop 0\n" \
"s_and_b64 s[56:57], s[56:57], s[40:41]\n" \
"v_add_u32 v11, 15, v12\n" \
"v_cmp_ge_u32 s[58:59], 14, v11\n" \
"s_nop 0\n" \
"s_and_b64 s[58:59], s[58:59], s[42:43]\n" \
"v_add_u32 v11, 14, v12\n" \
"v_cmp_ge_u32 s[60:61], 14, v11\n" \
"s_nop 0\n" \
"s_and_b64 s[60:61], s[60:61], s[40:41]\n" \
"v_add_u32 v11, 13, v12\n" \
"v_cmp_ge_u32 s[62:63], 14, v11\n" \
"s_nop 0\n" \
"s_and_b64 s[62:63], s[62:63], s[42:43]\n" \
"v_add_u32 v11, 0, v12\n" \
"v_cmp_ge_u32 s[64:65], 14, v11\n" \
"s_nop 0\n" \
"s_and_b64 s[64:65], s[64:65], s[40:41]\n" \
"v_add_u32 v11, -1, v12\n" \
"v_cmp_ge_u32 s[66:67], 14, v11\n" \
"s_nop 0\n" \
"s_and_b64 s[66:67], s[66:67], s[42:43]\n" \
"v_add_u32 v11, -2, v12\n" \
"v_cmp_ge_u32 s[68:69], 14, v11\n" \
"s_nop 0\n" \
"s_and_b64 s[68:69], s[68:69], s[40:41]\n" \
"v_add_u32 v11, -3, v12\n" \
"v_cmp_ge_u32 s[70:71], 14, v11\n" \
"s_nop 0\n" \
"s_and_b64 s[70:71], s[70:71], s[42:43]\n" \
"s_mov_b32 s81, s80\n" \
"s_and_b64 s[40:41], s[56:57], s[80:81]\n" \
"s_and_b64 s[42:43], s[58:59], s[80:81]\n" \
"s_and_b64 s[44:45], s[60:61], s[80:81]\n" \
"s_and_b64 s[46:47], s[62:63], s[80:81]\n" \
"s_nop 1\n" \
"v_mov_b32 v20, 0x3f803f80\n" \
"v_mov_b32 v21, 0x3f803f80\n" \
"v_mov_b32 v22, 0x3f803f80\n" \
"v_mov_b32 v23, 0x3f803f80\n" \
"v_mov_b32 v19, 0xff800000\n" \
"ds_read_b128 v[24:27], v14 offset:0\n" \
"ds_read_b128 v[28:31], v15 offset:0\n" \
"ds_read_b128 v[32:35], v15 offset:512\n" \
"ds_read_b128 v[68:71], v14 offset:1024\n" \
"ds_read_b128 v[72:75], v15 offset:512\n" \
"ds_read_b128 v[76:79], v15 offset:1024\n" \
"ds_read_b128 v[112:115], v14 offset:2048\n" \
"ds_read_b128 v[116:119], v15 offset:1024\n" \
"ds_read_b128 v[120:123], v15 offset:1536\n" \
"ds_read_b64_tr_b16 v[36:37], v16 offset:0\n" \
"ds_read_b64_tr_b16 v[38:39], v16 offset:512\n" \
"ds_read_b64_tr_b16 v[40:41], v16 offset:8704\n" \
"ds_read_b64_tr_b16 v[42:43], v16 offset:9216\n" \
"ds_read_b64_tr_b16 v[80:81], v16 offset:512\n" \
"ds_read_b64_tr_b16 v[82:83], v16 offset:1024\n" \
"ds_read_b64_tr_b16 v[84:85], v16 offset:9216\n" \
"ds_read_b64_tr_b16 v[86:87], v16 offset:9728\n" \
"ds_read_b64_tr_b16 v[124:125], v16 offset:1024\n" \
"ds_read_b64_tr_b16 v[126:127], v16 offset:1536\n" \
"ds_read_b64_tr_b16 v[128:129], v16 offset:9728\n" \
"ds_read_b64_tr_b16 v[130:131], v16 offset:10240\n" \
"s_waitcnt lgkmcnt(12)\n" \
"v_mfma_f32_16x16x32_bf16 v[44:47], v[28:31], v[24:27], 0\n" \
"v_mfma_f32_16x16x32_bf16 v[88:91], v[72:75], v[68:71], 0\n" \
"v_mfma_f32_16x16x32_bf16 v[132:135], v[116:119], v[112:115], 0\n" \
"v_mfma_f32_16x16x32_bf16 v[48:51], v[32:35], v[24:27], 0\n" \
"v_mfma_f32_16x16x32_bf16 v[92:95], v[76:79], v[68:71], 0\n" \
"v_mfma_f32_16x16x32_bf16 v[136:139], v[120:123], v[112:115], 0\n" \
"s_waitcnt lgkmcnt(0)\n" \
"s_nop 1\n" \
"v_cndmask_b32 v44, v19, v44, s[40:41]\n" \
"v_cndmask_b32 v88, v19, v88, s[56:57]\n" \
"v_cndmask_b32 v132, v19, v132, s[56:57]\n" \
"v_cndmask_b32 v45, v19, v45, s[42:43]\n" \
"v_cndmask_b32 v89, v19, v89, s[58:59]\n" \
"v_cndmask_b32 v133, v19, v133, s[58:59]\n" \
"v_cndmask_b32 v46, v19, v46, s[44:45]\n" \
"v_cndmask_b32 v90, v19, v90, s[60:61]\n" \
"v_cndmask_b32 v134, v19, v134, s[60:61]\n" \
"v_cndmask_b32 v47, v19, v47, s[46:47]\n" \
"v_cndmask_b32 v91, v19, v91, s[62:63]\n" \
"v_cndmask_b32 v135, v19, v135, s[62:63]\n" \
"v_cndmask_b32 v48, v19, v48, s[64:65]\n" \
"v_cndmask_b32 v92, v19, v92, s[64:65]\n" \
"v_cndmask_b32 v136, v19, v136, s[64:65]\n" \
"v_cndmask_b32 v49, v19, v49, s[66:67]\n" \
"v_cndmask_b32 v93, v19, v93, s[66:67]\n" \
"v_cndmask_b32 v137, v19, v137, s[66:67]\n" \
"v_cndmask_b32 v50, v19, v50, s[68:69]\n" \
"v_cndmask_b32 v94, v19, v94, s[68:69]\n" \
"v_cndmask_b32 v138, v19, v138, s[68:69]\n" \
"v_cndmask_b32 v51, v19, v51, s[70:71]\n" \
"v_cndmask_b32 v95, v19, v95, s[70:71]\n" \
"v_cndmask_b32 v139, v19, v139, s[70:71]\n" \
"v_max3_f32 v52, v44, v45, v46\n" \
"v_max3_f32 v96, v88, v89, v90\n" \
"v_max3_f32 v140, v132, v133, v134\n" \
"v_max3_f32 v53, v47, v48, v49\n" \
"v_max3_f32 v97, v91, v92, v93\n" \
"v_max3_f32 v141, v135, v136, v137\n" \
"v_max3_f32 v52, v52, v50, v51\n" \
"v_max3_f32 v96, v96, v94, v95\n" \
"v_max3_f32 v140, v140, v138, v139\n" \
"v_max_f32 v52, v52, v53\n" \
"v_max_f32 v96, v96, v97\n" \
"v_max_f32 v140, v140, v141\n" \
"v_mov_b32 v53, v52\n" \
"v_mov_b32 v97, v96\n" \
"v_mov_b32 v141, v140\n" \
"v_permlane16_swap_b32 v52, v53\n" \
"v_permlane16_swap_b32 v96, v97\n" \
"v_permlane16_swap_b32 v140, v141\n" \
"v_max_f32 v52, v52, v53\n" \
"v_max_f32 v96, v96, v97\n" \
"v_max_f32 v140, v140, v141\n" \
"v_mov_b32 v53, v52\n" \
"v_mov_b32 v97, v96\n" \
"v_mov_b32 v141, v140\n" \
"v_permlane32_swap_b32 v52, v53\n" \
"v_permlane32_swap_b32 v96, v97\n" \
"v_permlane32_swap_b32 v140, v141\n" \
"v_max_f32 v52, v52, v53\n" \
"v_max_f32 v96, v96, v97\n" \
"v_max_f32 v140, v140, v141\n" \
"v_mul_f32 v52, s96, v52\n" \
"v_mul_f32 v96, s96, v96\n" \
"v_mul_f32 v140, s96, v140\n" \
"v_fma_f32 v44, v44, s96, -v52\n" \
"v_fma_f32 v88, v88, s96, -v96\n" \
"v_fma_f32 v132, v132, s96, -v140\n" \
"v_fma_f32 v45, v45, s96, -v52\n" \
"v_fma_f32 v89, v89, s96, -v96\n" \
"v_fma_f32 v133, v133, s96, -v140\n" \
"v_fma_f32 v46, v46, s96, -v52\n" \
"v_fma_f32 v90, v90, s96, -v96\n" \
"v_fma_f32 v134, v134, s96, -v140\n" \
"v_fma_f32 v47, v47, s96, -v52\n" \
"v_fma_f32 v91, v91, s96, -v96\n" \
"v_fma_f32 v135, v135, s96, -v140\n" \
"v_fma_f32 v48, v48, s96, -v52\n" \
"v_fma_f32 v92, v92, s96, -v96\n" \
"v_fma_f32 v136, v136, s96, -v140\n" \
"v_fma_f32 v49, v49, s96, -v52\n" \
"v_fma_f32 v93, v93, s96, -v96\n" \
"v_fma_f32 v137, v137, s96, -v140\n" \
"v_fma_f32 v50, v50, s96, -v52\n" \
"v_fma_f32 v94, v94, s96, -v96\n" \
"v_fma_f32 v138, v138, s96, -v140\n" \
"v_fma_f32 v51, v51, s96, -v52\n" \
"v_fma_f32 v95, v95, s96, -v96\n" \
"v_fma_f32 v139, v139, s96, -v140\n" \
"v_exp_f32 v44, v44\n" \
"v_exp_f32 v88, v88\n" \
"v_exp_f32 v132, v132\n" \
"v_exp_f32 v45, v45\n" \
"v_exp_f32 v89, v89\n" \
"v_exp_f32 v133, v133\n" \
"v_exp_f32 v46, v46\n" \
"v_exp_f32 v90, v90\n" \
"v_exp_f32 v134, v134\n" \
"v_exp_f32 v47, v47\n" \
"v_exp_f32 v91, v91\n" \
"v_exp_f32 v135, v135\n" \
"v_exp_f32 v48, v48\n" \
"v_exp_f32 v92, v92\n" \
"v_exp_f32 v136, v136\n" \
"v_exp_f32 v49, v49\n" \
"v_exp_f32 v93, v93\n" \
"v_exp_f32 v137, v137\n" \
"v_exp_f32 v50, v50\n" \
"v_exp_f32 v94, v94\n" \
"v_exp_f32 v138, v138\n" \
"v_exp_f32 v51, v51\n" \
"v_exp_f32 v95, v95\n" \
"v_exp_f32 v139, v139\n" \
"v_cvt_pk_bf16_f32 v56, v44, v45\n" \
"v_cvt_pk_bf16_f32 v100, v88, v89\n" \
"v_cvt_pk_bf16_f32 v144, v132, v133\n" \
"v_cvt_pk_bf16_f32 v57, v46, v47\n" \
"v_cvt_pk_bf16_f32 v101, v90, v91\n" \
"v_cvt_pk_bf16_f32 v145, v134, v135\n" \
"v_cvt_pk_bf16_f32 v58, v48, v49\n" \
"v_cvt_pk_bf16_f32 v102, v92, v93\n" \
"v_cvt_pk_bf16_f32 v146, v136, v137\n" \
"v_cvt_pk_bf16_f32 v59, v50, v51\n" \
"v_cvt_pk_bf16_f32 v103, v94, v95\n" \
"v_cvt_pk_bf16_f32 v147, v138, v139\n" \
"v_mfma_f32_16x16x32_bf16 v[60:63], v[20:23], v[56:59], 0\n" \
"v_mfma_f32_16x16x32_bf16 v[104:107], v[20:23], v[100:103], 0\n" \
"v_mfma_f32_16x16x32_bf16 v[148:151], v[20:23], v[144:147], 0\n" \
"v_mfma_f32_16x16x32_bf16 v[28:31], v[36:39], v[56:59], 0\n" \
"v_mfma_f32_16x16x32_bf16 v[72:75], v[80:83], v[100:103], 0\n" \
"v_mfma_f32_16x16x32_bf16 v[116:119], v[124:127], v[144:147], 0\n" \
"v_mfma_f32_16x16x32_bf16 v[32:35], v[40:43], v[56:59], 0\n" \
"v_mfma_f32_16x16x32_bf16 v[76:79], v[84:87], v[100:103], 0\n" \
"v_mfma_f32_16x16x32_bf16 v[120:123], v[128:131], v[144:147], 0\n" \
"v_rcp_f32 v54, v60\n" \
"v_rcp_f32 v98, v104\n" \
"v_rcp_f32 v142, v148\n" \
"v_pk_mul_f32 v[28:29], v[28:29], v[54:55] op_sel_hi:[1,0]\n" \
"v_pk_mul_f32 v[72:73], v[72:73], v[98:99] op_sel_hi:[1,0]\n" \
"v_pk_mul_f32 v[116:117], v[116:117], v[142:143] op_sel_hi:[1,0]\n" \
"v_pk_mul_f32 v[30:31], v[30:31], v[54:55] op_sel_hi:[1,0]\n" \
"v_pk_mul_f32 v[74:75], v[74:75], v[98:99] op_sel_hi:[1,0]\n" \
"v_pk_mul_f32 v[118:119], v[118:119], v[142:143] op_sel_hi:[1,0]\n" \
"v_pk_mul_f32 v[32:33], v[32:33], v[54:55] op_sel_hi:[1,0]\n" \
"v_pk_mul_f32 v[76:77], v[76:77], v[98:99] op_sel_hi:[1,0]\n" \
"v_pk_mul_f32 v[120:121], v[120:121], v[142:143] op_sel_hi:[1,0]\n" \
"v_pk_mul_f32 v[34:35], v[34:35], v[54:55] op_sel_hi:[1,0]\n" \
"v_pk_mul_f32 v[78:79], v[78:79], v[98:99] op_sel_hi:[1,0]\n" \
"v_pk_mul_f32 v[122:123], v[122:123], v[142:143] op_sel_hi:[1,0]\n" \
"v_cvt_pk_bf16_f32 v24, v28, v29\n" \
"v_cvt_pk_bf16_f32 v68, v72, v73\n" \
"v_cvt_pk_bf16_f32 v112, v116, v117\n" \
"v_cvt_pk_bf16_f32 v26, v32, v33\n" \
"v_cvt_pk_bf16_f32 v70, v76, v77\n" \
"v_cvt_pk_bf16_f32 v114, v120, v121\n" \
"v_cvt_pk_bf16_f32 v25, v30, v31\n" \
"v_cvt_pk_bf16_f32 v69, v74, v75\n" \
"v_cvt_pk_bf16_f32 v113, v118, v119\n" \
"v_cvt_pk_bf16_f32 v27, v34, v35\n" \
"v_cvt_pk_bf16_f32 v71, v78, v79\n" \
"v_cvt_pk_bf16_f32 v115, v122, v123\n" \
"v_permlane16_swap_b32 v24, v26\n" \
"v_permlane16_swap_b32 v68, v70\n" \
"v_permlane16_swap_b32 v112, v114\n" \
"v_permlane16_swap_b32 v25, v27\n" \
"v_permlane16_swap_b32 v69, v71\n" \
"v_permlane16_swap_b32 v113, v115\n" \
"v_add_u32 v64, 0, v17\n" \
"v_add_u32 v108, 32768, v17\n" \
"v_add_u32 v152, 65536, v17\n" \
"global_store_dwordx4 v64, v[24:27], s[88:89] sc1\n" \
"global_store_dwordx4 v108, v[68:71], s[88:89] sc1\n" \
"global_store_dwordx4 v152, v[112:115], s[88:89] sc1\n" \
"s_branch LE2%=\n" \
"LE1%=:\n" \
"v_lshlrev_b32 v70, 16, v61\n" \
"v_lshlrev_b32 v78, 16, v62\n" \
"v_and_b32 v74, 0xffff0000, v61\n" \
"v_and_b32 v82, 0xffff0000, v62\n" \
"v_rcp_f32 v72, v74\n" \
"v_rcp_f32 v80, v82\n" \
"v_mul_f32 v74, v74, v74\n" \
"v_mul_f32 v82, v82, v82\n" \
"v_mul_f32 v76, s94, v72\n" \
"v_mul_f32 v84, s94, v80\n" \
"s_nop 0\n" \
"v_accvgpr_read_b32 v90, a12\n" \
"v_accvgpr_read_b32 v96, a28\n" \
"v_accvgpr_read_b32 v102, a52\n" \
"v_accvgpr_read_b32 v108, a68\n" \
"v_accvgpr_read_b32 v91, a13\n" \
"v_accvgpr_read_b32 v97, a29\n" \
"v_accvgpr_read_b32 v103, a53\n" \
"v_accvgpr_read_b32 v109, a69\n" \
"v_accvgpr_read_b32 v92, a14\n" \
"v_accvgpr_read_b32 v98, a30\n" \
"v_accvgpr_read_b32 v104, a54\n" \
"v_accvgpr_read_b32 v110, a70\n" \
"v_accvgpr_read_b32 v93, a15\n" \
"v_accvgpr_read_b32 v99, a31\n" \
"v_accvgpr_read_b32 v105, a55\n" \
"v_accvgpr_read_b32 v111, a71\n" \
"v_pk_fma_f32 v[90:91], v[32:33], v[70:71], v[90:91] op_sel_hi:[1,0,1] neg_lo:[0,1,0] neg_hi:[0,1,0]\n" \
"v_pk_fma_f32 v[96:97], v[48:49], v[70:71], v[96:97] op_sel_hi:[1,0,1] neg_lo:[0,1,0] neg_hi:[0,1,0]\n" \
"v_pk_fma_f32 v[102:103], v[32:33], v[78:79], v[102:103] op_sel_hi:[1,0,1] neg_lo:[0,1,0] neg_hi:[0,1,0]\n" \
"v_pk_fma_f32 v[108:109], v[48:49], v[78:79], v[108:109] op_sel_hi:[1,0,1] neg_lo:[0,1,0] neg_hi:[0,1,0]\n" \
"v_pk_fma_f32 v[92:93], v[34:35], v[70:71], v[92:93] op_sel_hi:[1,0,1] neg_lo:[0,1,0] neg_hi:[0,1,0]\n" \
"v_pk_fma_f32 v[98:99], v[50:51], v[70:71], v[98:99] op_sel_hi:[1,0,1] neg_lo:[0,1,0] neg_hi:[0,1,0]\n" \
"v_pk_fma_f32 v[104:105], v[34:35], v[78:79], v[104:105] op_sel_hi:[1,0,1] neg_lo:[0,1,0] neg_hi:[0,1,0]\n" \
"v_pk_fma_f32 v[110:111], v[50:51], v[78:79], v[110:111] op_sel_hi:[1,0,1] neg_lo:[0,1,0] neg_hi:[0,1,0]\n" \
"v_pk_mul_f32 v[90:91], v[90:91], v[72:73] op_sel_hi:[1,0]\n" \
"v_pk_mul_f32 v[96:97], v[96:97], v[72:73] op_sel_hi:[1,0]\n" \
"v_pk_mul_f32 v[102:103], v[102:103], v[80:81] op_sel_hi:[1,0]\n" \
"v_pk_mul_f32 v[108:109], v[108:109], v[80:81] op_sel_hi:[1,0]\n" \
"v_pk_mul_f32 v[92:93], v[92:93], v[72:73] op_sel_hi:[1,0]\n" \
"v_pk_mul_f32 v[98:99], v[98:99], v[72:73] op_sel_hi:[1,0]\n" \
"v_pk_mul_f32 v[104:105], v[104:105], v[80:81] op_sel_hi:[1,0]\n" \
"v_pk_mul_f32 v[110:111], v[110:111], v[80:81] op_sel_hi:[1,0]\n" \
"v_cvt_pk_bf16_f32 v94, v90, v91\n" \
"v_cvt_pk_bf16_f32 v100, v96, v97\n" \
"v_cvt_pk_bf16_f32 v106, v102, v103\n" \
"v_cvt_pk_bf16_f32 v112, v108, v109\n" \
"v_cvt_pk_bf16_f32 v95, v92, v93\n" \
"v_cvt_pk_bf16_f32 v101, v98, v99\n" \
"v_cvt_pk_bf16_f32 v107, v104, v105\n" \
"v_cvt_pk_bf16_f32 v113, v110, v111\n" \
"ds_write_b64 v251, v[94:95] offset:0\n" \
"ds_write_b64 v251, v[100:101] offset:512\n" \
"ds_write_b64 v251, v[106:107] offset:1024\n" \
"ds_write_b64 v251, v[112:113] offset:1536\n" \
"v_lshlrev_b32 v70, 16, v63\n" \
"v_lshlrev_b32 v78, 16, v64\n" \
"v_and_b32 v74, 0xffff0000, v63\n" \
"v_and_b32 v82, 0xffff0000, v64\n" \
"v_rcp_f32 v72, v74\n" \
"v_rcp_f32 v80, v82\n" \
"v_mul_f32 v74, v74, v74\n" \
"v_mul_f32 v82, v82, v82\n" \
"v_mul_f32 v76, s94, v72\n" \
"v_mul_f32 v84, s94, v80\n" \
"s_nop 0\n" \
"v_accvgpr_read_b32 v114, a92\n" \
"v_accvgpr_read_b32 v86, a108\n" \
"v_accvgpr_read_b32 v92, a132\n" \
"v_accvgpr_read_b32 v98, a148\n" \
"v_accvgpr_read_b32 v115, a93\n" \
"v_accvgpr_read_b32 v87, a109\n" \
"v_accvgpr_read_b32 v93, a133\n" \
"v_accvgpr_read_b32 v99, a149\n" \
"v_accvgpr_read_b32 v116, a94\n" \
"v_accvgpr_read_b32 v88, a110\n" \
"v_accvgpr_read_b32 v94, a134\n" \
"v_accvgpr_read_b32 v100, a150\n" \
"v_accvgpr_read_b32 v117, a95\n" \
"v_accvgpr_read_b32 v89, a111\n" \
"v_accvgpr_read_b32 v95, a135\n" \
"v_accvgpr_read_b32 v101, a151\n" \
"v_pk_fma_f32 v[114:115], v[32:33], v[70:71], v[114:115] op_sel_hi:[1,0,1] neg_lo:[0,1,0] neg_hi:[0,1,0]\n" \
"v_pk_fma_f32 v[86:87], v[48:49], v[70:71], v[86:87] op_sel_hi:[1,0,1] neg_lo:[0,1,0] neg_hi:[0,1,0]\n" \
"v_pk_fma_f32 v[92:93], v[32:33], v[78:79], v[92:93] op_sel_hi:[1,0,1] neg_lo:[0,1,0] neg_hi:[0,1,0]\n" \
"v_pk_fma_f32 v[98:99], v[48:49], v[78:79], v[98:99] op_sel_hi:[1,0,1] neg_lo:[0,1,0] neg_hi:[0,1,0]\n" \
"v_pk_fma_f32 v[116:117], v[34:35], v[70:71], v[116:117] op_sel_hi:[1,0,1] neg_lo:[0,1,0] neg_hi:[0,1,0]\n" \
"v_pk_fma_f32 v[88:89], v[50:51], v[70:71], v[88:89] op_sel_hi:[1,0,1] neg_lo:[0,1,0] neg_hi:[0,1,0]\n" \
"v_pk_fma_f32 v[94:95], v[34:35], v[78:79], v[94:95] op_sel_hi:[1,0,1] neg_lo:[0,1,0] neg_hi:[0,1,0]\n" \
"v_pk_fma_f32 v[100:101], v[50:51], v[78:79], v[100:101] op_sel_hi:[1,0,1] neg_lo:[0,1,0] neg_hi:[0,1,0]\n" \
"v_pk_mul_f32 v[114:115], v[114:115], v[72:73] op_sel_hi:[1,0]\n" \
"v_pk_mul_f32 v[86:87], v[86:87], v[72:73] op_sel_hi:[1,0]\n" \
"v_pk_mul_f32 v[92:93], v[92:93], v[80:81] op_sel_hi:[1,0]\n" \
"v_pk_mul_f32 v[98:99], v[98:99], v[80:81] op_sel_hi:[1,0]\n" \
"v_pk_mul_f32 v[116:117], v[116:117], v[72:73] op_sel_hi:[1,0]\n" \
"v_pk_mul_f32 v[88:89], v[88:89], v[72:73] op_sel_hi:[1,0]\n" \
"v_pk_mul_f32 v[94:95], v[94:95], v[80:81] op_sel_hi:[1,0]\n" \
"v_pk_mul_f32 v[100:101], v[100:101], v[80:81] op_sel_hi:[1,0]\n" \
"v_cvt_pk_bf16_f32 v118, v114, v115\n" \
"v_cvt_pk_bf16_f32 v90, v86, v87\n" \
"v_cvt_pk_bf16_f32 v96, v92, v93\n" \
"v_cvt_pk_bf16_f32 v102, v98, v99\n" \
"v_cvt_pk_bf16_f32 v119, v116, v117\n" \
"v_cvt_pk_bf16_f32 v91, v88, v89\n" \
"v_cvt_pk_bf16_f32 v97, v94, v95\n" \
"v_cvt_pk_bf16_f32 v103, v100, v101\n" \
"ds_write_b64 v251, v[118:119] offset:2048\n" \
"ds_write_b64 v251, v[90:91] offset:2560\n" \
"ds_write_b64 v251, v[96:97] offset:3072\n" \
"ds_write_b64 v251, v[102:103] offset:3584\n" \
"v_lshlrev_b32 v70, 16, v65\n" \
"v_lshlrev_b32 v78, 16, v66\n" \
"v_and_b32 v74, 0xffff0000, v65\n" \
"v_and_b32 v82, 0xffff0000, v66\n" \
"v_rcp_f32 v72, v74\n" \
"v_rcp_f32 v80, v82\n" \
"v_mul_f32 v74, v74, v74\n" \
"v_mul_f32 v82, v82, v82\n" \
"v_mul_f32 v76, s94, v72\n" \
"v_mul_f32 v84, s94, v80\n" \
"s_nop 0\n" \
"v_accvgpr_read_b32 v104, a172\n" \
"v_accvgpr_read_b32 v110, a188\n" \
"v_pk_fma_f32 v[136:137], v[32:33], v[78:79], v[136:137] op_sel_hi:[1,0,1] neg_lo:[0,1,0] neg_hi:[0,1,0]\n" \
"v_pk_fma_f32 v[152:153], v[48:49], v[78:79], v[152:153] op_sel_hi:[1,0,1] neg_lo:[0,1,0] neg_hi:[0,1,0]\n" \
"v_accvgpr_read_b32 v105, a173\n" \
"v_accvgpr_read_b32 v111, a189\n" \
"v_pk_fma_f32 v[138:139], v[34:35], v[78:79], v[138:139] op_sel_hi:[1,0,1] neg_lo:[0,1,0] neg_hi:[0,1,0]\n" \
"v_pk_fma_f32 v[154:155], v[50:51], v[78:79], v[154:155] op_sel_hi:[1,0,1] neg_lo:[0,1,0] neg_hi:[0,1,0]\n" \
"v_accvgpr_read_b32 v106, a174\n" \
"v_accvgpr_read_b32 v112, a190\n" \
"v_pk_mul_f32 v[136:137], v[136:137], v[80:81] op_sel_hi:[1,0]\n" \
"v_pk_mul_f32 v[152:153], v[152:153], v[80:81] op_sel_hi:[1,0]\n" \
"v_accvgpr_read_b32 v107, a175\n" \
"v_accvgpr_read_b32 v113, a191\n" \
"v_pk_mul_f32 v[138:139], v[138:139], v[80:81] op_sel_hi:[1,0]\n" \
"v_pk_mul_f32 v[154:155], v[154:155], v[80:81] op_sel_hi:[1,0]\n" \
"v_pk_fma_f32 v[104:105], v[32:33], v[70:71], v[104:105] op_sel_hi:[1,0,1] neg_lo:[0,1,0] neg_hi:[0,1,0]\n" \
"v_pk_fma_f32 v[110:111], v[48:49], v[70:71], v[110:111] op_sel_hi:[1,0,1] neg_lo:[0,1,0] neg_hi:[0,1,0]\n" \
"v_cvt_pk_bf16_f32 v116, v136, v137\n" \
"v_cvt_pk_bf16_f32 v118, v152, v153\n" \
"v_pk_fma_f32 v[106:107], v[34:35], v[70:71], v[106:107] op_sel_hi:[1,0,1] neg_lo:[0,1,0] neg_hi:[0,1,0]\n" \
"v_pk_fma_f32 v[112:113], v[50:51], v[70:71], v[112:113] op_sel_hi:[1,0,1] neg_lo:[0,1,0] neg_hi:[0,1,0]\n" \
"v_cvt_pk_bf16_f32 v117, v138, v139\n" \
"v_cvt_pk_bf16_f32 v119, v154, v155\n" \
"v_pk_mul_f32 v[104:105], v[104:105], v[72:73] op_sel_hi:[1,0]\n" \
"v_pk_mul_f32 v[110:111], v[110:111], v[72:73] op_sel_hi:[1,0]\n" \
"ds_write_b64 v251, v[116:117] offset:5120\n" \
"ds_write_b64 v251, v[118:119] offset:5632\n" \
"v_pk_mul_f32 v[106:107], v[106:107], v[72:73] op_sel_hi:[1,0]\n" \
"v_pk_mul_f32 v[112:113], v[112:113], v[72:73] op_sel_hi:[1,0]\n" \
"v_cvt_pk_bf16_f32 v108, v104, v105\n" \
"v_cvt_pk_bf16_f32 v114, v110, v111\n" \
"v_cvt_pk_bf16_f32 v109, v106, v107\n" \
"v_cvt_pk_bf16_f32 v115, v112, v113\n" \
"ds_write_b64 v251, v[108:109] offset:4096\n" \
"ds_write_b64 v251, v[114:115] offset:4608\n" \
"v_lshlrev_b32 v70, 16, v67\n" \
"v_lshlrev_b32 v78, 16, v68\n" \
"v_and_b32 v74, 0xffff0000, v67\n" \
"v_and_b32 v82, 0xffff0000, v68\n" \
"v_rcp_f32 v72, v74\n" \
"v_rcp_f32 v80, v82\n" \
"v_mul_f32 v74, v74, v74\n" \
"v_mul_f32 v82, v82, v82\n" \
"v_mul_f32 v76, s94, v72\n" \
"v_mul_f32 v84, s94, v80\n" \
"s_nop 0\n" \
"v_pk_fma_f32 v[176:177], v[32:33], v[70:71], v[176:177] op_sel_hi:[1,0,1] neg_lo:[0,1,0] neg_hi:[0,1,0]\n" \
"v_pk_fma_f32 v[192:193], v[48:49], v[70:71], v[192:193] op_sel_hi:[1,0,1] neg_lo:[0,1,0] neg_hi:[0,1,0]\n" \
"v_pk_fma_f32 v[216:217], v[32:33], v[78:79], v[216:217] op_sel_hi:[1,0,1] neg_lo:[0,1,0] neg_hi:[0,1,0]\n" \
"v_pk_fma_f32 v[232:233], v[48:49], v[78:79], v[232:233] op_sel_hi:[1,0,1] neg_lo:[0,1,0] neg_hi:[0,1,0]\n" \
"v_pk_fma_f32 v[178:179], v[34:35], v[70:71], v[178:179] op_sel_hi:[1,0,1] neg_lo:[0,1,0] neg_hi:[0,1,0]\n" \
"v_pk_fma_f32 v[194:195], v[50:51], v[70:71], v[194:195] op_sel_hi:[1,0,1] neg_lo:[0,1,0] neg_hi:[0,1,0]\n" \
"v_pk_fma_f32 v[218:219], v[34:35], v[78:79], v[218:219] op_sel_hi:[1,0,1] neg_lo:[0,1,0] neg_hi:[0,1,0]\n" \
"v_pk_fma_f32 v[234:235], v[50:51], v[78:79], v[234:235] op_sel_hi:[1,0,1] neg_lo:[0,1,0] neg_hi:[0,1,0]\n" \
"v_pk_mul_f32 v[176:177], v[176:177], v[72:73] op_sel_hi:[1,0]\n" \
"v_pk_mul_f32 v[192:193], v[192:193], v[72:73] op_sel_hi:[1,0]\n" \
"v_pk_mul_f32 v[216:217], v[216:217], v[80:81] op_sel_hi:[1,0]\n" \
"v_pk_mul_f32 v[232:233], v[232:233], v[80:81] op_sel_hi:[1,0]\n" \
"v_pk_mul_f32 v[178:179], v[178:179], v[72:73] op_sel_hi:[1,0]\n" \
"v_pk_mul_f32 v[194:195], v[194:195], v[72:73] op_sel_hi:[1,0]\n" \
"v_pk_mul_f32 v[218:219], v[218:219], v[80:81] op_sel_hi:[1,0]\n" \
"v_pk_mul_f32 v[234:235], v[234:235], v[80:81] op_sel_hi:[1,0]\n" \
"v_cvt_pk_bf16_f32 v86, v176, v177\n" \
"v_cvt_pk_bf16_f32 v88, v192, v193\n" \
"v_cvt_pk_bf16_f32 v90, v216, v217\n" \
"v_cvt_pk_bf16_f32 v92, v232, v233\n" \
"v_cvt_pk_bf16_f32 v87, v178, v179\n" \
"v_cvt_pk_bf16_f32 v89, v194, v195\n" \
"v_cvt_pk_bf16_f32 v91, v218, v219\n" \
"v_cvt_pk_bf16_f32 v93, v234, v235\n" \
"ds_write_b64 v251, v[86:87] offset:6144\n" \
"ds_write_b64 v251, v[88:89] offset:6656\n" \
"ds_write_b64 v251, v[90:91] offset:7168\n" \
"ds_write_b64 v251, v[92:93] offset:7680\n" \
"s_waitcnt lgkmcnt(0)\n" \
"s_barrier\n" \
"s_waitcnt vmcnt(0)\n" \
"v_lshlrev_b32 v70, 16, v61\n" \
"v_lshlrev_b32 v78, 16, v62\n" \
"v_and_b32 v74, 0xffff0000, v61\n" \
"v_and_b32 v82, 0xffff0000, v62\n" \
"v_rcp_f32 v72, v74\n" \
"v_rcp_f32 v80, v82\n" \
"v_mul_f32 v74, v74, v74\n" \
"v_mul_f32 v82, v82, v82\n" \
"v_mul_f32 v76, s94, v72\n" \
"v_mul_f32 v84, s94, v80\n" \
"s_nop 0\n" \
"v_accvgpr_read_b32 v94, a0\n" \
"v_accvgpr_read_b32 v106, a4\n" \
"v_accvgpr_read_b32 v95, a1\n" \
"v_accvgpr_read_b32 v107, a5\n" \
"v_accvgpr_read_b32 v96, a2\n" \
"v_accvgpr_read_b32 v108, a6\n" \
"v_accvgpr_read_b32 v97, a3\n" \
"v_accvgpr_read_b32 v109, a7\n" \
"v_accvgpr_read_b32 v98, a16\n" \
"v_accvgpr_read_b32 v110, a20\n" \
"v_accvgpr_read_b32 v99, a17\n" \
"v_accvgpr_read_b32 v111, a21\n" \
"v_accvgpr_read_b32 v100, a18\n" \
"v_accvgpr_read_b32 v112, a22\n" \
"v_accvgpr_read_b32 v101, a19\n" \
"v_accvgpr_read_b32 v113, a23\n" \
"v_pk_fma_f32 v[94:95], v[20:21], v[70:71], v[94:95] op_sel_hi:[1,0,1] neg_lo:[0,1,0] neg_hi:[0,1,0]\n" \
"v_pk_fma_f32 v[106:107], v[24:25], v[70:71], v[106:107] op_sel_hi:[1,0,1] neg_lo:[0,1,0] neg_hi:[0,1,0]\n" \
"v_pk_fma_f32 v[96:97], v[22:23], v[70:71], v[96:97] op_sel_hi:[1,0,1] neg_lo:[0,1,0] neg_hi:[0,1,0]\n" \
"v_pk_fma_f32 v[108:109], v[26:27], v[70:71], v[108:109] op_sel_hi:[1,0,1] neg_lo:[0,1,0] neg_hi:[0,1,0]\n" \
"v_pk_fma_f32 v[98:99], v[36:37], v[70:71], v[98:99] op_sel_hi:[1,0,1] neg_lo:[0,1,0] neg_hi:[0,1,0]\n" \
"v_pk_fma_f32 v[110:111], v[40:41], v[70:71], v[110:111] op_sel_hi:[1,0,1] neg_lo:[0,1,0] neg_hi:[0,1,0]\n" \
"v_pk_fma_f32 v[100:101], v[38:39], v[70:71], v[100:101] op_sel_hi:[1,0,1] neg_lo:[0,1,0] neg_hi:[0,1,0]\n" \
"v_pk_fma_f32 v[112:113], v[42:43], v[70:71], v[112:113] op_sel_hi:[1,0,1] neg_lo:[0,1,0] neg_hi:[0,1,0]\n" \
"v_pk_mul_f32 v[102:103], v[94:95], v[76:77] op_sel_hi:[1,0]\n" \
"v_pk_mul_f32 v[114:115], v[106:107], v[76:77] op_sel_hi:[1,0]\n" \
"v_pk_mul_f32 v[104:105], v[96:97], v[76:77] op_sel_hi:[1,0]\n" \
"v_pk_mul_f32 v[116:117], v[108:109], v[76:77] op_sel_hi:[1,0]\n" \
"v_exp_f32 v102, v102\n" \
"v_exp_f32 v114, v114\n" \
"v_exp_f32 v103, v103\n" \
"v_exp_f32 v115, v115\n" \
"v_exp_f32 v104, v104\n" \
"v_exp_f32 v116, v116\n" \
"v_exp_f32 v105, v105\n" \
"v_exp_f32 v117, v117\n" \
"v_pk_mul_f32 v[98:99], v[94:95], v[98:99]\n" \
"v_pk_mul_f32 v[110:111], v[106:107], v[110:111]\n" \
"v_pk_mul_f32 v[100:101], v[96:97], v[100:101]\n" \
"v_pk_mul_f32 v[112:113], v[108:109], v[112:113]\n" \
"v_pk_fma_f32 v[102:103], v[102:103], v[74:75], v[74:75] op_sel_hi:[1,0,0]\n" \
"v_pk_fma_f32 v[114:115], v[114:115], v[74:75], v[74:75] op_sel_hi:[1,0,0]\n" \
"v_pk_fma_f32 v[104:105], v[104:105], v[74:75], v[74:75] op_sel_hi:[1,0,0]\n" \
"v_pk_fma_f32 v[116:117], v[116:117], v[74:75], v[74:75] op_sel_hi:[1,0,0]\n" \
"v_rcp_f32 v102, v102\n" \
"v_rcp_f32 v114, v114\n" \
"v_rcp_f32 v103, v103\n" \
"v_rcp_f32 v115, v115\n" \
"v_rcp_f32 v104, v104\n" \
"v_rcp_f32 v116, v116\n" \
"v_rcp_f32 v105, v105\n" \
"v_rcp_f32 v117, v117\n" \
"v_pk_mul_f32 v[98:99], v[98:99], v[102:103]\n" \
"v_pk_mul_f32 v[110:111], v[110:111], v[114:115]\n" \
"v_pk_mul_f32 v[100:101], v[100:101], v[104:105]\n" \
"v_pk_mul_f32 v[112:113], v[112:113], v[116:117]\n" \
"v_cvt_pk_bf16_f32 v86, v98, v99\n" \
"v_cvt_pk_bf16_f32 v88, v110, v111\n" \
"v_cvt_pk_bf16_f32 v87, v100, v101\n" \
"v_cvt_pk_bf16_f32 v89, v112, v113\n" \
"s_nop 1\n" \
"v_permlane16_swap_b32 v86, v88\n" \
"v_permlane16_swap_b32 v87, v89\n" \
"global_store_dwordx4 v252, v[86:89], s[56:57] offset:0 sc1\n" \
"v_accvgpr_read_b32 v90, a8\n" \
"v_accvgpr_read_b32 v102, a48\n" \
"v_accvgpr_read_b32 v91, a9\n" \
"v_accvgpr_read_b32 v103, a49\n" \
"v_accvgpr_read_b32 v92, a10\n" \
"v_accvgpr_read_b32 v104, a50\n" \
"v_accvgpr_read_b32 v93, a11\n" \
"v_accvgpr_read_b32 v105, a51\n" \
"v_accvgpr_read_b32 v94, a24\n" \
"v_accvgpr_read_b32 v106, a64\n" \
"v_accvgpr_read_b32 v95, a25\n" \
"v_accvgpr_read_b32 v107, a65\n" \
"v_accvgpr_read_b32 v96, a26\n" \
"v_accvgpr_read_b32 v108, a66\n" \
"v_accvgpr_read_b32 v97, a27\n" \
"v_accvgpr_read_b32 v109, a67\n" \
"v_pk_fma_f32 v[90:91], v[28:29], v[70:71], v[90:91] op_sel_hi:[1,0,1] neg_lo:[0,1,0] neg_hi:[0,1,0]\n" \
"v_pk_fma_f32 v[102:103], v[28:29], v[78:79], v[102:103] op_sel_hi:[1,0,1] neg_lo:[0,1,0] neg_hi:[0,1,0]\n" \
"v_pk_fma_f32 v[92:93], v[30:31], v[70:71], v[92:93] op_sel_hi:[1,0,1] neg_lo:[0,1,0] neg_hi:[0,1,0]\n" \
"v_pk_fma_f32 v[104:105], v[30:31], v[78:79], v[104:105] op_sel_hi:[1,0,1] neg_lo:[0,1,0] neg_hi:[0,1,0]\n" \
"v_pk_fma_f32 v[94:95], v[44:45], v[70:71], v[94:95] op_sel_hi:[1,0,1] neg_lo:[0,1,0] neg_hi:[0,1,0]\n" \
"v_pk_fma_f32 v[106:107], v[44:45], v[78:79], v[106:107] op_sel_hi:[1,0,1] neg_lo:[0,1,0] neg_hi:[0,1,0]\n" \
"v_pk_fma_f32 v[96:97], v[46:47], v[70:71], v[96:97] op_sel_hi:[1,0,1] neg_lo:[0,1,0] neg_hi:[0,1,0]\n" \
"v_pk_fma_f32 v[108:109], v[46:47], v[78:79], v[108:109] op_sel_hi:[1,0,1] neg_lo:[0,1,0] neg_hi:[0,1,0]\n" \
"v_pk_mul_f32 v[98:99], v[90:91], v[76:77] op_sel_hi:[1,0]\n" \
"v_pk_mul_f32 v[110:111], v[102:103], v[84:85] op_sel_hi:[1,0]\n" \
"v_pk_mul_f32 v[100:101], v[92:93], v[76:77] op_sel_hi:[1,0]\n" \
"v_pk_mul_f32 v[112:113], v[104:105], v[84:85] op_sel_hi:[1,0]\n" \
"v_exp_f32 v98, v98\n" \
"v_exp_f32 v110, v110\n" \
"v_exp_f32 v99, v99\n" \
"v_exp_f32 v111, v111\n" \
"v_exp_f32 v100, v100\n" \
"v_exp_f32 v112, v112\n" \
"v_exp_f32 v101, v101\n" \
"v_exp_f32 v113, v113\n" \
"v_pk_mul_f32 v[94:95], v[90:91], v[94:95]\n" \
"v_pk_mul_f32 v[106:107], v[102:103], v[106:107]\n" \
"v_pk_mul_f32 v[96:97], v[92:93], v[96:97]\n" \
"v_pk_mul_f32 v[108:109], v[104:105], v[108:109]\n" \
"v_pk_fma_f32 v[98:99], v[98:99], v[74:75], v[74:75] op_sel_hi:[1,0,0]\n" \
"v_pk_fma_f32 v[110:111], v[110:111], v[82:83], v[82:83] op_sel_hi:[1,0,0]\n" \
"v_pk_fma_f32 v[100:101], v[100:101], v[74:75], v[74:75] op_sel_hi:[1,0,0]\n" \
"v_pk_fma_f32 v[112:113], v[112:113], v[82:83], v[82:83] op_sel_hi:[1,0,0]\n" \
"v_rcp_f32 v98, v98\n" \
"v_rcp_f32 v110, v110\n" \
"v_rcp_f32 v99, v99\n" \
"v_rcp_f32 v111, v111\n" \
"v_rcp_f32 v100, v100\n" \
"v_rcp_f32 v112, v112\n" \
"v_rcp_f32 v101, v101\n" \
"v_rcp_f32 v113, v113\n" \
"v_pk_mul_f32 v[94:95], v[94:95], v[98:99]\n" \
"v_pk_mul_f32 v[106:107], v[106:107], v[110:111]\n" \
"v_pk_mul_f32 v[96:97], v[96:97], v[100:101]\n" \
"v_pk_mul_f32 v[108:109], v[108:109], v[112:113]\n" \
"v_cvt_pk_bf16_f32 v114, v94, v95\n" \
"v_cvt_pk_bf16_f32 v116, v106, v107\n" \
"v_cvt_pk_bf16_f32 v115, v96, v97\n" \
"v_cvt_pk_bf16_f32 v117, v108, v109\n" \
"s_nop 1\n" \
"v_permlane16_swap_b32 v114, v116\n" \
"v_permlane16_swap_b32 v115, v117\n" \
"global_store_dwordx4 v253, v[114:117], s[56:57] offset:0 sc1\n" \
"s_add_u32 s56, s56, 114688\n" \
"s_addc_u32 s57, s57, 0\n" \
"v_accvgpr_read_b32 v86, a40\n" \
"v_accvgpr_read_b32 v98, a44\n" \
"v_accvgpr_read_b32 v87, a41\n" \
"v_accvgpr_read_b32 v99, a45\n" \
"v_accvgpr_read_b32 v88, a42\n" \
"v_accvgpr_read_b32 v100, a46\n" \
"v_accvgpr_read_b32 v89, a43\n" \
"v_accvgpr_read_b32 v101, a47\n" \
"v_accvgpr_read_b32 v90, a56\n" \
"v_accvgpr_read_b32 v102, a60\n" \
"v_accvgpr_read_b32 v91, a57\n" \
"v_accvgpr_read_b32 v103, a61\n" \
"v_accvgpr_read_b32 v92, a58\n" \
"v_accvgpr_read_b32 v104, a62\n" \
"v_accvgpr_read_b32 v93, a59\n" \
"v_accvgpr_read_b32 v105, a63\n" \
"v_pk_fma_f32 v[86:87], v[20:21], v[78:79], v[86:87] op_sel_hi:[1,0,1] neg_lo:[0,1,0] neg_hi:[0,1,0]\n" \
"v_pk_fma_f32 v[98:99], v[24:25], v[78:79], v[98:99] op_sel_hi:[1,0,1] neg_lo:[0,1,0] neg_hi:[0,1,0]\n" \
"v_pk_fma_f32 v[88:89], v[22:23], v[78:79], v[88:89] op_sel_hi:[1,0,1] neg_lo:[0,1,0] neg_hi:[0,1,0]\n" \
"v_pk_fma_f32 v[100:101], v[26:27], v[78:79], v[100:101] op_sel_hi:[1,0,1] neg_lo:[0,1,0] neg_hi:[0,1,0]\n" \
"v_pk_fma_f32 v[90:91], v[36:37], v[78:79], v[90:91] op_sel_hi:[1,0,1] neg_lo:[0,1,0] neg_hi:[0,1,0]\n" \
"v_pk_fma_f32 v[102:103], v[40:41], v[78:79], v[102:103] op_sel_hi:[1,0,1] neg_lo:[0,1,0] neg_hi:[0,1,0]\n" \
"v_pk_fma_f32 v[92:93], v[38:39], v[78:79], v[92:93] op_sel_hi:[1,0,1] neg_lo:[0,1,0] neg_hi:[0,1,0]\n" \
"v_pk_fma_f32 v[104:105], v[42:43], v[78:79], v[104:105] op_sel_hi:[1,0,1] neg_lo:[0,1,0] neg_hi:[0,1,0]\n" \
"v_pk_mul_f32 v[94:95], v[86:87], v[84:85] op_sel_hi:[1,0]\n" \
"v_pk_mul_f32 v[106:107], v[98:99], v[84:85] op_sel_hi:[1,0]\n" \
"v_pk_mul_f32 v[96:97], v[88:89], v[84:85] op_sel_hi:[1,0]\n" \
"v_pk_mul_f32 v[108:109], v[100:101], v[84:85] op_sel_hi:[1,0]\n" \
"v_exp_f32 v94, v94\n" \
"v_exp_f32 v106, v106\n" \
"v_exp_f32 v95, v95\n" \
"v_exp_f32 v107, v107\n" \
"v_exp_f32 v96, v96\n" \
"v_exp_f32 v108, v108\n" \
"v_exp_f32 v97, v97\n" \
"v_exp_f32 v109, v109\n" \
"v_pk_mul_f32 v[90:91], v[86:87], v[90:91]\n" \
"v_pk_mul_f32 v[102:103], v[98:99], v[102:103]\n" \
"v_pk_mul_f32 v[92:93], v[88:89], v[92:93]\n" \
"v_pk_mul_f32 v[104:105], v[100:101], v[104:105]\n" \
"v_pk_fma_f32 v[94:95], v[94:95], v[82:83], v[82:83] op_sel_hi:[1,0,0]\n" \
"v_pk_fma_f32 v[106:107], v[106:107], v[82:83], v[82:83] op_sel_hi:[1,0,0]\n" \
"v_pk_fma_f32 v[96:97], v[96:97], v[82:83], v[82:83] op_sel_hi:[1,0,0]\n" \
"v_pk_fma_f32 v[108:109], v[108:109], v[82:83], v[82:83] op_sel_hi:[1,0,0]\n" \
"v_rcp_f32 v94, v94\n" \
"v_rcp_f32 v106, v106\n" \
"v_rcp_f32 v95, v95\n" \
"v_rcp_f32 v107, v107\n" \
"v_rcp_f32 v96, v96\n" \
"v_rcp_f32 v108, v108\n" \
"v_rcp_f32 v97, v97\n" \
"v_rcp_f32 v109, v109\n" \
"v_pk_mul_f32 v[90:91], v[90:91], v[94:95]\n" \
"v_pk_mul_f32 v[102:103], v[102:103], v[106:107]\n" \
"v_pk_mul_f32 v[92:93], v[92:93], v[96:97]\n" \
"v_pk_mul_f32 v[104:105], v[104:105], v[108:109]\n" \
"v_cvt_pk_bf16_f32 v110, v90, v91\n" \
"v_cvt_pk_bf16_f32 v112, v102, v103\n" \
"v_cvt_pk_bf16_f32 v111, v92, v93\n" \
"v_cvt_pk_bf16_f32 v113, v104, v105\n" \
"s_nop 1\n" \
"v_permlane16_swap_b32 v110, v112\n" \
"v_permlane16_swap_b32 v111, v113\n" \
"global_store_dwordx4 v252, v[110:113], s[56:57] offset:0 sc1\n" \
"s_add_u32 s56, s56, 114688\n" \
"s_addc_u32 s57, s57, 0\n" \
"v_lshlrev_b32 v70, 16, v63\n" \
"v_lshlrev_b32 v78, 16, v64\n" \
"v_and_b32 v74, 0xffff0000, v63\n" \
"v_and_b32 v82, 0xffff0000, v64\n" \
"v_rcp_f32 v72, v74\n" \
"v_rcp_f32 v80, v82\n" \
"v_mul_f32 v74, v74, v74\n" \
"v_mul_f32 v82, v82, v82\n" \
"v_mul_f32 v76, s94, v72\n" \
"v_mul_f32 v84, s94, v80\n" \
"s_nop 0\n" \
"v_accvgpr_read_b32 v114, a80\n" \
"v_accvgpr_read_b32 v94, a84\n" \
"v_accvgpr_read_b32 v115, a81\n" \
"v_accvgpr_read_b32 v95, a85\n" \
"v_accvgpr_read_b32 v116, a82\n" \
"v_accvgpr_read_b32 v96, a86\n" \
"v_accvgpr_read_b32 v117, a83\n" \
"v_accvgpr_read_b32 v97, a87\n" \
"v_accvgpr_read_b32 v86, a96\n" \
"v_accvgpr_read_b32 v98, a100\n" \
"v_accvgpr_read_b32 v87, a97\n" \
"v_accvgpr_read_b32 v99, a101\n" \
"v_accvgpr_read_b32 v88, a98\n" \
"v_accvgpr_read_b32 v100, a102\n" \
"v_accvgpr_read_b32 v89, a99\n" \
"v_accvgpr_read_b32 v101, a103\n" \
"v_pk_fma_f32 v[114:115], v[20:21], v[70:71], v[114:115] op_sel_hi:[1,0,1] neg_lo:[0,1,0] neg_hi:[0,1,0]\n" \
"v_pk_fma_f32 v[94:95], v[24:25], v[70:71], v[94:95] op_sel_hi:[1,0,1] neg_lo:[0,1,0] neg_hi:[0,1,0]\n" \
"v_pk_fma_f32 v[116:117], v[22:23], v[70:71], v[116:117] op_sel_hi:[1,0,1] neg_lo:[0,1,0] neg_hi:[0,1,0]\n" \
"v_pk_fma_f32 v[96:97], v[26:27], v[70:71], v[96:97] op_sel_hi:[1,0,1] neg_lo:[0,1,0] neg_hi:[0,1,0]\n" \
"v_pk_fma_f32 v[86:87], v[36:37], v[70:71], v[86:87] op_sel_hi:[1,0,1] neg_lo:[0,1,0] neg_hi:[0,1,0]\n" \
"v_pk_fma_f32 v[98:99], v[40:41], v[70:71], v[98:99] op_sel_hi:[1,0,1] neg_lo:[0,1,0] neg_hi:[0,1,0]\n" \
"v_pk_fma_f32 v[88:89], v[38:39], v[70:71], v[88:89] op_sel_hi:[1,0,1] neg_lo:[0,1,0] neg_hi:[0,1,0]\n" \
"v_pk_fma_f32 v[100:101], v[42:43], v[70:71], v[100:101] op_sel_hi:[1,0,1] neg_lo:[0,1,0] neg_hi:[0,1,0]\n" \
"v_pk_mul_f32 v[90:91], v[114:115], v[76:77] op_sel_hi:[1,0]\n" \
"v_pk_mul_f32 v[102:103], v[94:95], v[76:77] op_sel_hi:[1,0]\n" \
"v_pk_mul_f32 v[92:93], v[116:117], v[76:77] op_sel_hi:[1,0]\n" \
"v_pk_mul_f32 v[104:105], v[96:97], v[76:77] op_sel_hi:[1,0]\n" \
"v_exp_f32 v90, v90\n" \
"v_exp_f32 v102, v102\n" \
"v_exp_f32 v91, v91\n" \
"v_exp_f32 v103, v103\n" \
"v_exp_f32 v92, v92\n" \
"v_exp_f32 v104, v104\n" \
"v_exp_f32 v93, v93\n" \
"v_exp_f32 v105, v105\n" \
"v_pk_mul_f32 v[86:87], v[114:115], v[86:87]\n" \
"v_pk_mul_f32 v[98:99], v[94:95], v[98:99]\n" \
"v_pk_mul_f32 v[88:89], v[116:117], v[88:89]\n" \
"v_pk_mul_f32 v[100:101], v[96:97], v[100:101]\n" \
"v_pk_fma_f32 v[90:91], v[90:91], v[74:75], v[74:75] op_sel_hi:[1,0,0]\n" \
"v_pk_fma_f32 v[102:103], v[102:103], v[74:75], v[74:75] op_sel_hi:[1,0,0]\n" \
"v_pk_fma_f32 v[92:93], v[92:93], v[74:75], v[74:75] op_sel_hi:[1,0,0]\n" \
"v_pk_fma_f32 v[104:105], v[104:105], v[74:75], v[74:75] op_sel_hi:[1,0,0]\n" \
"v_rcp_f32 v90, v90\n" \
"v_rcp_f32 v102, v102\n" \
"v_rcp_f32 v91, v91\n" \
"v_rcp_f32 v103, v103\n" \
"v_rcp_f32 v92, v92\n" \
"v_rcp_f32 v104, v104\n" \
"v_rcp_f32 v93, v93\n" \
"v_rcp_f32 v105, v105\n" \
"v_pk_mul_f32 v[86:87], v[86:87], v[90:91]\n" \
"v_pk_mul_f32 v[98:99], v[98:99], v[102:103]\n" \
"v_pk_mul_f32 v[88:89], v[88:89], v[92:93]\n" \
"v_pk_mul_f32 v[100:101], v[100:101], v[104:105]\n" \
"v_cvt_pk_bf16_f32 v106, v86, v87\n" \
"v_cvt_pk_bf16_f32 v108, v98, v99\n" \
"v_cvt_pk_bf16_f32 v107, v88, v89\n" \
"v_cvt_pk_bf16_f32 v109, v100, v101\n" \
"s_nop 1\n" \
"v_permlane16_swap_b32 v106, v108\n" \
"v_permlane16_swap_b32 v107, v109\n" \
"global_store_dwordx4 v252, v[106:109], s[56:57] offset:0 sc1\n" \
"v_accvgpr_read_b32 v110, a88\n" \
"v_accvgpr_read_b32 v90, a128\n" \
"v_accvgpr_read_b32 v111, a89\n" \
"v_accvgpr_read_b32 v91, a129\n" \
"v_accvgpr_read_b32 v112, a90\n" \
"v_accvgpr_read_b32 v92, a130\n" \
"v_accvgpr_read_b32 v113, a91\n" \
"v_accvgpr_read_b32 v93, a131\n" \
"v_accvgpr_read_b32 v114, a104\n" \
"v_accvgpr_read_b32 v94, a144\n" \
"v_accvgpr_read_b32 v115, a105\n" \
"v_accvgpr_read_b32 v95, a145\n" \
"v_accvgpr_read_b32 v116, a106\n" \
"v_accvgpr_read_b32 v96, a146\n" \
"v_accvgpr_read_b32 v117, a107\n" \
"v_accvgpr_read_b32 v97, a147\n" \
"v_pk_fma_f32 v[110:111], v[28:29], v[70:71], v[110:111] op_sel_hi:[1,0,1] neg_lo:[0,1,0] neg_hi:[0,1,0]\n" \
"v_pk_fma_f32 v[90:91], v[28:29], v[78:79], v[90:91] op_sel_hi:[1,0,1] neg_lo:[0,1,0] neg_hi:[0,1,0]\n" \
"v_pk_fma_f32 v[112:113], v[30:31], v[70:71], v[112:113] op_sel_hi:[1,0,1] neg_lo:[0,1,0] neg_hi:[0,1,0]\n" \
"v_pk_fma_f32 v[92:93], v[30:31], v[78:79], v[92:93] op_sel_hi:[1,0,1] neg_lo:[0,1,0] neg_hi:[0,1,0]\n" \
"v_pk_fma_f32 v[114:115], v[44:45], v[70:71], v[114:115] op_sel_hi:[1,0,1] neg_lo:[0,1,0] neg_hi:[0,1,0]\n" \
"v_pk_fma_f32 v[94:95], v[44:45], v[78:79], v[94:95] op_sel_hi:[1,0,1] neg_lo:[0,1,0] neg_hi:[0,1,0]\n" \
"v_pk_fma_f32 v[116:117], v[46:47], v[70:71], v[116:117] op_sel_hi:[1,0,1] neg_lo:[0,1,0] neg_hi:[0,1,0]\n" \
"v_pk_fma_f32 v[96:97], v[46:47], v[78:79], v[96:97] op_sel_hi:[1,0,1] neg_lo:[0,1,0] neg_hi:[0,1,0]\n" \
"v_pk_mul_f32 v[86:87], v[110:111], v[76:77] op_sel_hi:[1,0]\n" \
"v_pk_mul_f32 v[98:99], v[90:91], v[84:85] op_sel_hi:[1,0]\n" \
"v_pk_mul_f32 v[88:89], v[112:113], v[76:77] op_sel_hi:[1,0]\n" \
"v_pk_mul_f32 v[100:101], v[92:93], v[84:85] op_sel_hi:[1,0]\n" \
"v_exp_f32 v86, v86\n" \
"v_exp_f32 v98, v98\n" \
"v_exp_f32 v87, v87\n" \
"v_exp_f32 v99, v99\n" \
"v_exp_f32 v88, v88\n" \
"v_exp_f32 v100, v100\n" \
"v_exp_f32 v89, v89\n" \
"v_exp_f32 v101, v101\n" \
"v_pk_mul_f32 v[114:115], v[110:111], v[114:115]\n" \
"v_pk_mul_f32 v[94:95], v[90:91], v[94:95]\n" \
"v_pk_mul_f32 v[116:117], v[112:113], v[116:117]\n" \
"v_pk_mul_f32 v[96:97], v[92:93], v[96:97]\n" \
"v_pk_fma_f32 v[86:87], v[86:87], v[74:75], v[74:75] op_sel_hi:[1,0,0]\n" \
"v_pk_fma_f32 v[98:99], v[98:99], v[82:83], v[82:83] op_sel_hi:[1,0,0]\n" \
"v_pk_fma_f32 v[88:89], v[88:89], v[74:75], v[74:75] op_sel_hi:[1,0,0]\n" \
"v_pk_fma_f32 v[100:101], v[100:101], v[82:83], v[82:83] op_sel_hi:[1,0,0]\n" \
"v_rcp_f32 v86, v86\n" \
"v_rcp_f32 v98, v98\n" \
"v_rcp_f32 v87, v87\n" \
"v_rcp_f32 v99, v99\n" \
"v_rcp_f32 v88, v88\n" \
"v_rcp_f32 v100, v100\n" \
"v_rcp_f32 v89, v89\n" \
"v_rcp_f32 v101, v101\n" \
"v_pk_mul_f32 v[114:115], v[114:115], v[86:87]\n" \
"v_pk_mul_f32 v[94:95], v[94:95], v[98:99]\n" \
"v_pk_mul_f32 v[116:117], v[116:117], v[88:89]\n" \
"v_pk_mul_f32 v[96:97], v[96:97], v[100:101]\n" \
"v_cvt_pk_bf16_f32 v102, v114, v115\n" \
"v_cvt_pk_bf16_f32 v104, v94, v95\n" \
"v_cvt_pk_bf16_f32 v103, v116, v117\n" \
"v_cvt_pk_bf16_f32 v105, v96, v97\n" \
"s_nop 1\n" \
"v_permlane16_swap_b32 v102, v104\n" \
"v_permlane16_swap_b32 v103, v105\n" \
"global_store_dwordx4 v253, v[102:105], s[56:57] offset:0 sc1\n" \
"s_add_u32 s56, s56, 114688\n" \
"s_addc_u32 s57, s57, 0\n" \
"v_accvgpr_read_b32 v106, a120\n" \
"v_accvgpr_read_b32 v86, a124\n" \
"v_accvgpr_read_b32 v107, a121\n" \
"v_accvgpr_read_b32 v87, a125\n" \
"v_accvgpr_read_b32 v108, a122\n" \
"v_accvgpr_read_b32 v88, a126\n" \
"v_accvgpr_read_b32 v109, a123\n" \
"v_accvgpr_read_b32 v89, a127\n" \
"v_accvgpr_read_b32 v110, a136\n" \
"v_accvgpr_read_b32 v90, a140\n" \
"v_accvgpr_read_b32 v111, a137\n" \
"v_accvgpr_read_b32 v91, a141\n" \
"v_accvgpr_read_b32 v112, a138\n" \
"v_accvgpr_read_b32 v92, a142\n" \
"v_accvgpr_read_b32 v113, a139\n" \
"v_accvgpr_read_b32 v93, a143\n" \
"v_pk_fma_f32 v[106:107], v[20:21], v[78:79], v[106:107] op_sel_hi:[1,0,1] neg_lo:[0,1,0] neg_hi:[0,1,0]\n" \
"v_pk_fma_f32 v[86:87], v[24:25], v[78:79], v[86:87] op_sel_hi:[1,0,1] neg_lo:[0,1,0] neg_hi:[0,1,0]\n" \
"v_pk_fma_f32 v[108:109], v[22:23], v[78:79], v[108:109] op_sel_hi:[1,0,1] neg_lo:[0,1,0] neg_hi:[0,1,0]\n" \
"v_pk_fma_f32 v[88:89], v[26:27], v[78:79], v[88:89] op_sel_hi:[1,0,1] neg_lo:[0,1,0] neg_hi:[0,1,0]\n" \
"v_pk_fma_f32 v[110:111], v[36:37], v[78:79], v[110:111] op_sel_hi:[1,0,1] neg_lo:[0,1,0] neg_hi:[0,1,0]\n" \
"v_pk_fma_f32 v[90:91], v[40:41], v[78:79], v[90:91] op_sel_hi:[1,0,1] neg_lo:[0,1,0] neg_hi:[0,1,0]\n" \
"v_pk_fma_f32 v[112:113], v[38:39], v[78:79], v[112:113] op_sel_hi:[1,0,1] neg_lo:[0,1,0] neg_hi:[0,1,0]\n" \
"v_pk_fma_f32 v[92:93], v[42:43], v[78:79], v[92:93] op_sel_hi:[1,0,1] neg_lo:[0,1,0] neg_hi:[0,1,0]\n" \
"v_pk_mul_f32 v[114:115], v[106:107], v[84:85] op_sel_hi:[1,0]\n" \
"v_pk_mul_f32 v[94:95], v[86:87], v[84:85] op_sel_hi:[1,0]\n" \
"v_pk_mul_f32 v[116:117], v[108:109], v[84:85] op_sel_hi:[1,0]\n" \
"v_pk_mul_f32 v[96:97], v[88:89], v[84:85] op_sel_hi:[1,0]\n" \
"v_exp_f32 v114, v114\n" \
"v_exp_f32 v94, v94\n" \
"v_exp_f32 v115, v115\n" \
"v_exp_f32 v95, v95\n" \
"v_exp_f32 v116, v116\n" \
"v_exp_f32 v96, v96\n" \
"v_exp_f32 v117, v117\n" \
"v_exp_f32 v97, v97\n" \
"v_pk_mul_f32 v[110:111], v[106:107], v[110:111]\n" \
"v_pk_mul_f32 v[90:91], v[86:87], v[90:91]\n" \
"v_pk_mul_f32 v[112:113], v[108:109], v[112:113]\n" \
"v_pk_mul_f32 v[92:93], v[88:89], v[92:93]\n" \
"v_pk_fma_f32 v[114:115], v[114:115], v[82:83], v[82:83] op_sel_hi:[1,0,0]\n" \
"v_pk_fma_f32 v[94:95], v[94:95], v[82:83], v[82:83] op_sel_hi:[1,0,0]\n" \
"v_pk_fma_f32 v[116:117], v[116:117], v[82:83], v[82:83] op_sel_hi:[1,0,0]\n" \
"v_pk_fma_f32 v[96:97], v[96:97], v[82:83], v[82:83] op_sel_hi:[1,0,0]\n" \
"v_rcp_f32 v114, v114\n" \
"v_rcp_f32 v94, v94\n" \
"v_rcp_f32 v115, v115\n" \
"v_rcp_f32 v95, v95\n" \
"v_rcp_f32 v116, v116\n" \
"v_rcp_f32 v96, v96\n" \
"v_rcp_f32 v117, v117\n" \
"v_rcp_f32 v97, v97\n" \
"v_pk_mul_f32 v[110:111], v[110:111], v[114:115]\n" \
"v_pk_mul_f32 v[90:91], v[90:91], v[94:95]\n" \
"v_pk_mul_f32 v[112:113], v[112:113], v[116:117]\n" \
"v_pk_mul_f32 v[92:93], v[92:93], v[96:97]\n" \
"v_cvt_pk_bf16_f32 v98, v110, v111\n" \
"v_cvt_pk_bf16_f32 v100, v90, v91\n" \
"v_cvt_pk_bf16_f32 v99, v112, v113\n" \
"v_cvt_pk_bf16_f32 v101, v92, v93\n" \
"s_nop 1\n" \
"v_permlane16_swap_b32 v98, v100\n" \
"v_permlane16_swap_b32 v99, v101\n" \
"global_store_dwordx4 v252, v[98:101], s[56:57] offset:0 sc1\n" \
"s_add_u32 s56, s56, 114688\n" \
"s_addc_u32 s57, s57, 0\n" \
"v_lshlrev_b32 v70, 16, v65\n" \
"v_lshlrev_b32 v78, 16, v66\n" \
"v_and_b32 v74, 0xffff0000, v65\n" \
"v_and_b32 v82, 0xffff0000, v66\n" \
"v_rcp_f32 v72, v74\n" \
"v_rcp_f32 v80, v82\n" \
"v_mul_f32 v74, v74, v74\n" \
"v_mul_f32 v82, v82, v82\n" \
"v_mul_f32 v76, s94, v72\n" \
"v_mul_f32 v84, s94, v80\n" \
"s_nop 0\n" \
"v_accvgpr_read_b32 v102, a160\n" \
"v_accvgpr_read_b32 v114, a164\n" \
"v_accvgpr_read_b32 v103, a161\n" \
"v_accvgpr_read_b32 v115, a165\n" \
"v_accvgpr_read_b32 v104, a162\n" \
"v_accvgpr_read_b32 v116, a166\n" \
"v_accvgpr_read_b32 v105, a163\n" \
"v_accvgpr_read_b32 v117, a167\n" \
"v_accvgpr_read_b32 v106, a176\n" \
"v_accvgpr_read_b32 v86, a180\n" \
"v_accvgpr_read_b32 v107, a177\n" \
"v_accvgpr_read_b32 v87, a181\n" \
"v_accvgpr_read_b32 v108, a178\n" \
"v_accvgpr_read_b32 v88, a182\n" \
"v_accvgpr_read_b32 v109, a179\n" \
"v_accvgpr_read_b32 v89, a183\n" \
"v_pk_fma_f32 v[102:103], v[20:21], v[70:71], v[102:103] op_sel_hi:[1,0,1] neg_lo:[0,1,0] neg_hi:[0,1,0]\n" \
"v_pk_fma_f32 v[114:115], v[24:25], v[70:71], v[114:115] op_sel_hi:[1,0,1] neg_lo:[0,1,0] neg_hi:[0,1,0]\n" \
"v_pk_fma_f32 v[104:105], v[22:23], v[70:71], v[104:105] op_sel_hi:[1,0,1] neg_lo:[0,1,0] neg_hi:[0,1,0]\n" \
"v_pk_fma_f32 v[116:117], v[26:27], v[70:71], v[116:117] op_sel_hi:[1,0,1] neg_lo:[0,1,0] neg_hi:[0,1,0]\n" \
"v_pk_fma_f32 v[106:107], v[36:37], v[70:71], v[106:107] op_sel_hi:[1,0,1] neg_lo:[0,1,0] neg_hi:[0,1,0]\n" \
"v_pk_fma_f32 v[86:87], v[40:41], v[70:71], v[86:87] op_sel_hi:[1,0,1] neg_lo:[0,1,0] neg_hi:[0,1,0]\n" \
"v_pk_fma_f32 v[108:109], v[38:39], v[70:71], v[108:109] op_sel_hi:[1,0,1] neg_lo:[0,1,0] neg_hi:[0,1,0]\n" \
"v_pk_fma_f32 v[88:89], v[42:43], v[70:71], v[88:89] op_sel_hi:[1,0,1] neg_lo:[0,1,0] neg_hi:[0,1,0]\n" \
"v_pk_mul_f32 v[110:111], v[102:103], v[76:77] op_sel_hi:[1,0]\n" \
"v_pk_mul_f32 v[90:91], v[114:115], v[76:77] op_sel_hi:[1,0]\n" \
"v_pk_mul_f32 v[112:113], v[104:105], v[76:77] op_sel_hi:[1,0]\n" \
"v_pk_mul_f32 v[92:93], v[116:117], v[76:77] op_sel_hi:[1,0]\n" \
"v_exp_f32 v110, v110\n" \
"v_exp_f32 v90, v90\n" \
"v_exp_f32 v111, v111\n" \
"v_exp_f32 v91, v91\n" \
"v_exp_f32 v112, v112\n" \
"v_exp_f32 v92, v92\n" \
"v_exp_f32 v113, v113\n" \
"v_exp_f32 v93, v93\n" \
"v_pk_mul_f32 v[106:107], v[102:103], v[106:107]\n" \
"v_pk_mul_f32 v[86:87], v[114:115], v[86:87]\n" \
"v_pk_mul_f32 v[108:109], v[104:105], v[108:109]\n" \
"v_pk_mul_f32 v[88:89], v[116:117], v[88:89]\n" \
"v_pk_fma_f32 v[110:111], v[110:111], v[74:75], v[74:75] op_sel_hi:[1,0,0]\n" \
"v_pk_fma_f32 v[90:91], v[90:91], v[74:75], v[74:75] op_sel_hi:[1,0,0]\n" \
"v_pk_fma_f32 v[112:113], v[112:113], v[74:75], v[74:75] op_sel_hi:[1,0,0]\n" \
"v_pk_fma_f32 v[92:93], v[92:93], v[74:75], v[74:75] op_sel_hi:[1,0,0]\n" \
"v_rcp_f32 v110, v110\n" \
"v_rcp_f32 v90, v90\n" \
"v_rcp_f32 v111, v111\n" \
"v_rcp_f32 v91, v91\n" \
"v_rcp_f32 v112, v112\n" \
"v_rcp_f32 v92, v92\n" \
"v_rcp_f32 v113, v113\n" \
"v_rcp_f32 v93, v93\n" \
"v_pk_mul_f32 v[106:107], v[106:107], v[110:111]\n" \
"v_pk_mul_f32 v[86:87], v[86:87], v[90:91]\n" \
"v_pk_mul_f32 v[108:109], v[108:109], v[112:113]\n" \
"v_pk_mul_f32 v[88:89], v[88:89], v[92:93]\n" \
"v_cvt_pk_bf16_f32 v94, v106, v107\n" \
"v_cvt_pk_bf16_f32 v96, v86, v87\n" \
"v_cvt_pk_bf16_f32 v95, v108, v109\n" \
"v_cvt_pk_bf16_f32 v97, v88, v89\n" \
"s_nop 1\n" \
"v_permlane16_swap_b32 v94, v96\n" \
"v_permlane16_swap_b32 v95, v97\n" \
"global_store_dwordx4 v252, v[94:97], s[56:57] offset:0 sc1\n" \
"v_accvgpr_read_b32 v98, a168\n" \
"v_pk_fma_f32 v[132:133], v[28:29], v[78:79], v[132:133] op_sel_hi:[1,0,1] neg_lo:[0,1,0] neg_hi:[0,1,0]\n" \
"v_accvgpr_read_b32 v99, a169\n" \
"v_pk_fma_f32 v[134:135], v[30:31], v[78:79], v[134:135] op_sel_hi:[1,0,1] neg_lo:[0,1,0] neg_hi:[0,1,0]\n" \
"v_accvgpr_read_b32 v100, a170\n" \
"v_pk_fma_f32 v[148:149], v[44:45], v[78:79], v[148:149] op_sel_hi:[1,0,1] neg_lo:[0,1,0] neg_hi:[0,1,0]\n" \
"v_accvgpr_read_b32 v101, a171\n" \
"v_pk_fma_f32 v[150:151], v[46:47], v[78:79], v[150:151] op_sel_hi:[1,0,1] neg_lo:[0,1,0] neg_hi:[0,1,0]\n" \
"v_accvgpr_read_b32 v102, a184\n" \
"v_pk_mul_f32 v[110:111], v[132:133], v[84:85] op_sel_hi:[1,0]\n" \
"v_accvgpr_read_b32 v103, a185\n" \
"v_pk_mul_f32 v[112:113], v[134:135], v[84:85] op_sel_hi:[1,0]\n" \
"v_accvgpr_read_b32 v104, a186\n" \
"v_exp_f32 v110, v110\n" \
"v_accvgpr_read_b32 v105, a187\n" \
"v_exp_f32 v111, v111\n" \
"v_pk_fma_f32 v[98:99], v[28:29], v[70:71], v[98:99] op_sel_hi:[1,0,1] neg_lo:[0,1,0] neg_hi:[0,1,0]\n" \
"v_exp_f32 v112, v112\n" \
"v_pk_fma_f32 v[100:101], v[30:31], v[70:71], v[100:101] op_sel_hi:[1,0,1] neg_lo:[0,1,0] neg_hi:[0,1,0]\n" \
"v_exp_f32 v113, v113\n" \
"v_pk_fma_f32 v[102:103], v[44:45], v[70:71], v[102:103] op_sel_hi:[1,0,1] neg_lo:[0,1,0] neg_hi:[0,1,0]\n" \
"v_pk_mul_f32 v[148:149], v[132:133], v[148:149]\n" \
"v_pk_fma_f32 v[104:105], v[46:47], v[70:71], v[104:105] op_sel_hi:[1,0,1] neg_lo:[0,1,0] neg_hi:[0,1,0]\n" \
"v_pk_mul_f32 v[150:151], v[134:135], v[150:151]\n" \
"v_pk_mul_f32 v[106:107], v[98:99], v[76:77] op_sel_hi:[1,0]\n" \
"v_pk_fma_f32 v[110:111], v[110:111], v[82:83], v[82:83] op_sel_hi:[1,0,0]\n" \
"v_pk_mul_f32 v[108:109], v[100:101], v[76:77] op_sel_hi:[1,0]\n" \
"v_pk_fma_f32 v[112:113], v[112:113], v[82:83], v[82:83] op_sel_hi:[1,0,0]\n" \
"v_exp_f32 v106, v106\n" \
"v_rcp_f32 v110, v110\n" \
"v_exp_f32 v107, v107\n" \
"v_rcp_f32 v111, v111\n" \
"v_exp_f32 v108, v108\n" \
"v_rcp_f32 v112, v112\n" \
"v_exp_f32 v109, v109\n" \
"v_rcp_f32 v113, v113\n" \
"v_pk_mul_f32 v[102:103], v[98:99], v[102:103]\n" \
"v_pk_mul_f32 v[148:149], v[148:149], v[110:111]\n" \
"v_pk_mul_f32 v[104:105], v[100:101], v[104:105]\n" \
"v_pk_mul_f32 v[150:151], v[150:151], v[112:113]\n" \
"v_pk_fma_f32 v[106:107], v[106:107], v[74:75], v[74:75] op_sel_hi:[1,0,0]\n" \
"v_pk_fma_f32 v[108:109], v[108:109], v[74:75], v[74:75] op_sel_hi:[1,0,0]\n" \
"v_rcp_f32 v106, v106\n" \
"v_rcp_f32 v107, v107\n" \
"v_rcp_f32 v108, v108\n" \
"v_rcp_f32 v109, v109\n" \
"v_pk_mul_f32 v[102:103], v[102:103], v[106:107]\n" \
"v_pk_mul_f32 v[104:105], v[104:105], v[108:109]\n" \
"v_cvt_pk_bf16_f32 v114, v102, v103\n" \
"v_cvt_pk_bf16_f32 v116, v148, v149\n" \
"v_cvt_pk_bf16_f32 v115, v104, v105\n" \
"v_cvt_pk_bf16_f32 v117, v150, v151\n" \
"s_nop 1\n" \
"v_permlane16_swap_b32 v114, v116\n" \
"v_permlane16_swap_b32 v115, v117\n" \
"global_store_dwordx4 v253, v[114:117], s[56:57] offset:0 sc1\n" \
"s_add_u32 s56, s56, 114688\n" \
"s_addc_u32 s57, s57, 0\n" \
"v_pk_fma_f32 v[124:125], v[20:21], v[78:79], v[124:125] op_sel_hi:[1,0,1] neg_lo:[0,1,0] neg_hi:[0,1,0]\n" \
"v_pk_fma_f32 v[128:129], v[24:25], v[78:79], v[128:129] op_sel_hi:[1,0,1] neg_lo:[0,1,0] neg_hi:[0,1,0]\n" \
"v_pk_fma_f32 v[126:127], v[22:23], v[78:79], v[126:127] op_sel_hi:[1,0,1] neg_lo:[0,1,0] neg_hi:[0,1,0]\n" \
"v_pk_fma_f32 v[130:131], v[26:27], v[78:79], v[130:131] op_sel_hi:[1,0,1] neg_lo:[0,1,0] neg_hi:[0,1,0]\n" \
"v_pk_fma_f32 v[140:141], v[36:37], v[78:79], v[140:141] op_sel_hi:[1,0,1] neg_lo:[0,1,0] neg_hi:[0,1,0]\n" \
"v_pk_fma_f32 v[144:145], v[40:41], v[78:79], v[144:145] op_sel_hi:[1,0,1] neg_lo:[0,1,0] neg_hi:[0,1,0]\n" \
"v_pk_fma_f32 v[142:143], v[38:39], v[78:79], v[142:143] op_sel_hi:[1,0,1] neg_lo:[0,1,0] neg_hi:[0,1,0]\n" \
"v_pk_fma_f32 v[146:147], v[42:43], v[78:79], v[146:147] op_sel_hi:[1,0,1] neg_lo:[0,1,0] neg_hi:[0,1,0]\n" \
"v_pk_mul_f32 v[86:87], v[124:125], v[84:85] op_sel_hi:[1,0]\n" \
"v_pk_mul_f32 v[90:91], v[128:129], v[84:85] op_sel_hi:[1,0]\n" \
"v_pk_mul_f32 v[88:89], v[126:127], v[84:85] op_sel_hi:[1,0]\n" \
"v_pk_mul_f32 v[92:93], v[130:131], v[84:85] op_sel_hi:[1,0]\n" \
"v_exp_f32 v86, v86\n" \
"v_exp_f32 v90, v90\n" \
"v_exp_f32 v87, v87\n" \
"v_exp_f32 v91, v91\n" \
"v_exp_f32 v88, v88\n" \
"v_exp_f32 v92, v92\n" \
"v_exp_f32 v89, v89\n" \
"v_exp_f32 v93, v93\n" \
"v_pk_mul_f32 v[140:141], v[124:125], v[140:141]\n" \
"v_pk_mul_f32 v[144:145], v[128:129], v[144:145]\n" \
"v_pk_mul_f32 v[142:143], v[126:127], v[142:143]\n" \
"v_pk_mul_f32 v[146:147], v[130:131], v[146:147]\n" \
"v_pk_fma_f32 v[86:87], v[86:87], v[82:83], v[82:83] op_sel_hi:[1,0,0]\n" \
"v_pk_fma_f32 v[90:91], v[90:91], v[82:83], v[82:83] op_sel_hi:[1,0,0]\n" \
"v_pk_fma_f32 v[88:89], v[88:89], v[82:83], v[82:83] op_sel_hi:[1,0,0]\n" \
"v_pk_fma_f32 v[92:93], v[92:93], v[82:83], v[82:83] op_sel_hi:[1,0,0]\n" \
"v_rcp_f32 v86, v86\n" \
"v_rcp_f32 v90, v90\n" \
"v_rcp_f32 v87, v87\n" \
"v_rcp_f32 v91, v91\n" \
"v_rcp_f32 v88, v88\n" \
"v_rcp_f32 v92, v92\n" \
"v_rcp_f32 v89, v89\n" \
"v_rcp_f32 v93, v93\n" \
"v_pk_mul_f32 v[140:141], v[140:141], v[86:87]\n" \
"v_pk_mul_f32 v[144:145], v[144:145], v[90:91]\n" \
"v_pk_mul_f32 v[142:143], v[142:143], v[88:89]\n" \
"v_pk_mul_f32 v[146:147], v[146:147], v[92:93]\n" \
"v_cvt_pk_bf16_f32 v94, v140, v141\n" \
"v_cvt_pk_bf16_f32 v96, v144, v145\n" \
"v_cvt_pk_bf16_f32 v95, v142, v143\n" \
"v_cvt_pk_bf16_f32 v97, v146, v147\n" \
"s_nop 1\n" \
"v_permlane16_swap_b32 v94, v96\n" \
"v_permlane16_swap_b32 v95, v97\n" \
"global_store_dwordx4 v252, v[94:97], s[56:57] offset:0 sc1\n" \
"s_add_u32 s56, s56, 114688\n" \
"s_addc_u32 s57, s57, 0\n" \
"v_lshlrev_b32 v70, 16, v67\n" \
"v_lshlrev_b32 v78, 16, v68\n" \
"v_and_b32 v74, 0xffff0000, v67\n" \
"v_and_b32 v82, 0xffff0000, v68\n" \
"v_rcp_f32 v72, v74\n" \
"v_rcp_f32 v80, v82\n" \
"v_mul_f32 v74, v74, v74\n" \
"v_mul_f32 v82, v82, v82\n" \
"v_mul_f32 v76, s94, v72\n" \
"v_mul_f32 v84, s94, v80\n" \
"s_nop 0\n" \
"v_pk_fma_f32 v[164:165], v[20:21], v[70:71], v[164:165] op_sel_hi:[1,0,1] neg_lo:[0,1,0] neg_hi:[0,1,0]\n" \
"v_pk_fma_f32 v[168:169], v[24:25], v[70:71], v[168:169] op_sel_hi:[1,0,1] neg_lo:[0,1,0] neg_hi:[0,1,0]\n" \
"v_pk_fma_f32 v[166:167], v[22:23], v[70:71], v[166:167] op_sel_hi:[1,0,1] neg_lo:[0,1,0] neg_hi:[0,1,0]\n" \
"v_pk_fma_f32 v[170:171], v[26:27], v[70:71], v[170:171] op_sel_hi:[1,0,1] neg_lo:[0,1,0] neg_hi:[0,1,0]\n" \
"v_pk_fma_f32 v[180:181], v[36:37], v[70:71], v[180:181] op_sel_hi:[1,0,1] neg_lo:[0,1,0] neg_hi:[0,1,0]\n" \
"v_pk_fma_f32 v[184:185], v[40:41], v[70:71], v[184:185] op_sel_hi:[1,0,1] neg_lo:[0,1,0] neg_hi:[0,1,0]\n" \
"v_pk_fma_f32 v[182:183], v[38:39], v[70:71], v[182:183] op_sel_hi:[1,0,1] neg_lo:[0,1,0] neg_hi:[0,1,0]\n" \
"v_pk_fma_f32 v[186:187], v[42:43], v[70:71], v[186:187] op_sel_hi:[1,0,1] neg_lo:[0,1,0] neg_hi:[0,1,0]\n" \
"v_pk_mul_f32 v[98:99], v[164:165], v[76:77] op_sel_hi:[1,0]\n" \
"v_pk_mul_f32 v[102:103], v[168:169], v[76:77] op_sel_hi:[1,0]\n" \
"v_pk_mul_f32 v[100:101], v[166:167], v[76:77] op_sel_hi:[1,0]\n" \
"v_pk_mul_f32 v[104:105], v[170:171], v[76:77] op_sel_hi:[1,0]\n" \
"v_exp_f32 v98, v98\n" \
"v_exp_f32 v102, v102\n" \
"v_exp_f32 v99, v99\n" \
"v_exp_f32 v103, v103\n" \
"v_exp_f32 v100, v100\n" \
"v_exp_f32 v104, v104\n" \
"v_exp_f32 v101, v101\n" \
"v_exp_f32 v105, v105\n" \
"v_pk_mul_f32 v[180:181], v[164:165], v[180:181]\n" \
"v_pk_mul_f32 v[184:185], v[168:169], v[184:185]\n" \
"v_pk_mul_f32 v[182:183], v[166:167], v[182:183]\n" \
"v_pk_mul_f32 v[186:187], v[170:171], v[186:187]\n" \
"v_pk_fma_f32 v[98:99], v[98:99], v[74:75], v[74:75] op_sel_hi:[1,0,0]\n" \
"v_pk_fma_f32 v[102:103], v[102:103], v[74:75], v[74:75] op_sel_hi:[1,0,0]\n" \
"v_pk_fma_f32 v[100:101], v[100:101], v[74:75], v[74:75] op_sel_hi:[1,0,0]\n" \
"v_pk_fma_f32 v[104:105], v[104:105], v[74:75], v[74:75] op_sel_hi:[1,0,0]\n" \
"v_rcp_f32 v98, v98\n" \
"v_rcp_f32 v102, v102\n" \
"v_rcp_f32 v99, v99\n" \
"v_rcp_f32 v103, v103\n" \
"v_rcp_f32 v100, v100\n" \
"v_rcp_f32 v104, v104\n" \
"v_rcp_f32 v101, v101\n" \
"v_rcp_f32 v105, v105\n" \
"v_pk_mul_f32 v[180:181], v[180:181], v[98:99]\n" \
"v_pk_mul_f32 v[184:185], v[184:185], v[102:103]\n" \
"v_pk_mul_f32 v[182:183], v[182:183], v[100:101]\n" \
"v_pk_mul_f32 v[186:187], v[186:187], v[104:105]\n" \
"v_cvt_pk_bf16_f32 v106, v180, v181\n" \
"v_cvt_pk_bf16_f32 v108, v184, v185\n" \
"v_cvt_pk_bf16_f32 v107, v182, v183\n" \
"v_cvt_pk_bf16_f32 v109, v186, v187\n" \
"s_nop 1\n" \
"v_permlane16_swap_b32 v106, v108\n" \
"v_permlane16_swap_b32 v107, v109\n" \
"global_store_dwordx4 v252, v[106:109], s[56:57] offset:0 sc1\n" \
"v_pk_fma_f32 v[172:173], v[28:29], v[70:71], v[172:173] op_sel_hi:[1,0,1] neg_lo:[0,1,0] neg_hi:[0,1,0]\n" \
"v_pk_fma_f32 v[212:213], v[28:29], v[78:79], v[212:213] op_sel_hi:[1,0,1] neg_lo:[0,1,0] neg_hi:[0,1,0]\n" \
"v_pk_fma_f32 v[174:175], v[30:31], v[70:71], v[174:175] op_sel_hi:[1,0,1] neg_lo:[0,1,0] neg_hi:[0,1,0]\n" \
"v_pk_fma_f32 v[214:215], v[30:31], v[78:79], v[214:215] op_sel_hi:[1,0,1] neg_lo:[0,1,0] neg_hi:[0,1,0]\n" \
"v_pk_fma_f32 v[188:189], v[44:45], v[70:71], v[188:189] op_sel_hi:[1,0,1] neg_lo:[0,1,0] neg_hi:[0,1,0]\n" \
"v_pk_fma_f32 v[228:229], v[44:45], v[78:79], v[228:229] op_sel_hi:[1,0,1] neg_lo:[0,1,0] neg_hi:[0,1,0]\n" \
"v_pk_fma_f32 v[190:191], v[46:47], v[70:71], v[190:191] op_sel_hi:[1,0,1] neg_lo:[0,1,0] neg_hi:[0,1,0]\n" \
"v_pk_fma_f32 v[230:231], v[46:47], v[78:79], v[230:231] op_sel_hi:[1,0,1] neg_lo:[0,1,0] neg_hi:[0,1,0]\n" \
"v_pk_mul_f32 v[110:111], v[172:173], v[76:77] op_sel_hi:[1,0]\n" \
"v_pk_mul_f32 v[114:115], v[212:213], v[84:85] op_sel_hi:[1,0]\n" \
"v_pk_mul_f32 v[112:113], v[174:175], v[76:77] op_sel_hi:[1,0]\n" \
"v_pk_mul_f32 v[116:117], v[214:215], v[84:85] op_sel_hi:[1,0]\n" \
"v_exp_f32 v110, v110\n" \
"v_exp_f32 v114, v114\n" \
"v_exp_f32 v111, v111\n" \
"v_exp_f32 v115, v115\n" \
"v_exp_f32 v112, v112\n" \
"v_exp_f32 v116, v116\n" \
"v_exp_f32 v113, v113\n" \
"v_exp_f32 v117, v117\n" \
"v_pk_mul_f32 v[188:189], v[172:173], v[188:189]\n" \
"v_pk_mul_f32 v[228:229], v[212:213], v[228:229]\n" \
"v_pk_mul_f32 v[190:191], v[174:175], v[190:191]\n" \
"v_pk_mul_f32 v[230:231], v[214:215], v[230:231]\n" \
"v_pk_fma_f32 v[110:111], v[110:111], v[74:75], v[74:75] op_sel_hi:[1,0,0]\n" \
"v_pk_fma_f32 v[114:115], v[114:115], v[82:83], v[82:83] op_sel_hi:[1,0,0]\n" \
"v_pk_fma_f32 v[112:113], v[112:113], v[74:75], v[74:75] op_sel_hi:[1,0,0]\n" \
"v_pk_fma_f32 v[116:117], v[116:117], v[82:83], v[82:83] op_sel_hi:[1,0,0]\n" \
"v_rcp_f32 v110, v110\n" \
"v_rcp_f32 v114, v114\n" \
"v_rcp_f32 v111, v111\n" \
"v_rcp_f32 v115, v115\n" \
"v_rcp_f32 v112, v112\n" \
"v_rcp_f32 v116, v116\n" \
"v_rcp_f32 v113, v113\n" \
"v_rcp_f32 v117, v117\n" \
"v_pk_mul_f32 v[188:189], v[188:189], v[110:111]\n" \
"v_pk_mul_f32 v[228:229], v[228:229], v[114:115]\n" \
"v_pk_mul_f32 v[190:191], v[190:191], v[112:113]\n" \
"v_pk_mul_f32 v[230:231], v[230:231], v[116:117]\n" \
"v_cvt_pk_bf16_f32 v86, v188, v189\n" \
"v_cvt_pk_bf16_f32 v88, v228, v229\n" \
"v_cvt_pk_bf16_f32 v87, v190, v191\n" \
"v_cvt_pk_bf16_f32 v89, v230, v231\n" \
"s_nop 1\n" \
"v_permlane16_swap_b32 v86, v88\n" \
"v_permlane16_swap_b32 v87, v89\n" \
"global_store_dwordx4 v253, v[86:89], s[56:57] offset:0 sc1\n" \
"s_add_u32 s56, s56, 114688\n" \
"s_addc_u32 s57, s57, 0\n" \
"v_pk_fma_f32 v[204:205], v[20:21], v[78:79], v[204:205] op_sel_hi:[1,0,1] neg_lo:[0,1,0] neg_hi:[0,1,0]\n" \
"v_pk_fma_f32 v[208:209], v[24:25], v[78:79], v[208:209] op_sel_hi:[1,0,1] neg_lo:[0,1,0] neg_hi:[0,1,0]\n" \
"v_pk_fma_f32 v[206:207], v[22:23], v[78:79], v[206:207] op_sel_hi:[1,0,1] neg_lo:[0,1,0] neg_hi:[0,1,0]\n" \
"v_pk_fma_f32 v[210:211], v[26:27], v[78:79], v[210:211] op_sel_hi:[1,0,1] neg_lo:[0,1,0] neg_hi:[0,1,0]\n" \
"v_pk_fma_f32 v[220:221], v[36:37], v[78:79], v[220:221] op_sel_hi:[1,0,1] neg_lo:[0,1,0] neg_hi:[0,1,0]\n" \
"v_pk_fma_f32 v[224:225], v[40:41], v[78:79], v[224:225] op_sel_hi:[1,0,1] neg_lo:[0,1,0] neg_hi:[0,1,0]\n" \
"v_pk_fma_f32 v[222:223], v[38:39], v[78:79], v[222:223] op_sel_hi:[1,0,1] neg_lo:[0,1,0] neg_hi:[0,1,0]\n" \
"v_pk_fma_f32 v[226:227], v[42:43], v[78:79], v[226:227] op_sel_hi:[1,0,1] neg_lo:[0,1,0] neg_hi:[0,1,0]\n" \
"v_pk_mul_f32 v[90:91], v[204:205], v[84:85] op_sel_hi:[1,0]\n" \
"v_pk_mul_f32 v[94:95], v[208:209], v[84:85] op_sel_hi:[1,0]\n" \
"v_pk_mul_f32 v[92:93], v[206:207], v[84:85] op_sel_hi:[1,0]\n" \
"v_pk_mul_f32 v[96:97], v[210:211], v[84:85] op_sel_hi:[1,0]\n" \
"v_exp_f32 v90, v90\n" \
"v_exp_f32 v94, v94\n" \
"v_exp_f32 v91, v91\n" \
"v_exp_f32 v95, v95\n" \
"v_exp_f32 v92, v92\n" \
"v_exp_f32 v96, v96\n" \
"v_exp_f32 v93, v93\n" \
"v_exp_f32 v97, v97\n" \
"v_pk_mul_f32 v[220:221], v[204:205], v[220:221]\n" \
"v_pk_mul_f32 v[224:225], v[208:209], v[224:225]\n" \
"v_pk_mul_f32 v[222:223], v[206:207], v[222:223]\n" \
"v_pk_mul_f32 v[226:227], v[210:211], v[226:227]\n" \
"v_pk_fma_f32 v[90:91], v[90:91], v[82:83], v[82:83] op_sel_hi:[1,0,0]\n" \
"v_pk_fma_f32 v[94:95], v[94:95], v[82:83], v[82:83] op_sel_hi:[1,0,0]\n" \
"v_pk_fma_f32 v[92:93], v[92:93], v[82:83], v[82:83] op_sel_hi:[1,0,0]\n" \
"v_pk_fma_f32 v[96:97], v[96:97], v[82:83], v[82:83] op_sel_hi:[1,0,0]\n" \
"v_rcp_f32 v90, v90\n" \
"v_rcp_f32 v94, v94\n" \
"v_rcp_f32 v91, v91\n" \
"v_rcp_f32 v95, v95\n" \
"v_rcp_f32 v92, v92\n" \
"v_rcp_f32 v96, v96\n" \
"v_rcp_f32 v93, v93\n" \
"v_rcp_f32 v97, v97\n" \
"v_pk_mul_f32 v[220:221], v[220:221], v[90:91]\n" \
"v_pk_mul_f32 v[224:225], v[224:225], v[94:95]\n" \
"v_pk_mul_f32 v[222:223], v[222:223], v[92:93]\n" \
"v_pk_mul_f32 v[226:227], v[226:227], v[96:97]\n" \
"v_cvt_pk_bf16_f32 v98, v220, v221\n" \
"v_cvt_pk_bf16_f32 v100, v224, v225\n" \
"v_cvt_pk_bf16_f32 v99, v222, v223\n" \
"v_cvt_pk_bf16_f32 v101, v226, v227\n" \
"s_nop 1\n" \
"v_permlane16_swap_b32 v98, v100\n" \
"v_permlane16_swap_b32 v99, v101\n" \
"global_store_dwordx4 v252, v[98:101], s[56:57] offset:0 sc1\n" \
"s_add_u32 s56, s56, 114688\n" \
"s_addc_u32 s57, s57, 0\n" \
"v_mbcnt_lo_u32_b32 v8, -1, 0\n" \
"v_mbcnt_hi_u32_b32 v8, -1, v8\n" \
"v_and_b32 v9, 15, v8\n" \
"v_lshrrev_b32 v10, 5, v8\n" \
"v_bfe_u32 v11, v8, 4, 1\n" \
"v_lshlrev_b32 v14, 9, v10\n" \
"v_lshl_add_u32 v14, v9, 5, v14\n" \
"v_lshl_add_u32 v14, v11, 4, v14\n" \
"v_lshl_add_u32 v15, v10, 13, v14\n" \
"v_add_u32 v15, s68, v15\n" \
"v_add_u32 v14, s67, v14\n" \
"v_lshrrev_b32 v16, 2, v8\n" \
"v_lshlrev_b32 v16, 5, v16\n" \
"v_and_b32 v12, 3, v8\n" \
"v_lshl_add_u32 v16, v12, 3, v16\n" \
"v_add_u32 v16, s69, v16\n" \
"v_lshlrev_b32 v17, 9, v11\n" \
"v_lshl_add_u32 v17, v9, 5, v17\n" \
"v_lshl_add_u32 v17, v10, 4, v17\n" \
"v_add_u32 v17, s70, v17\n" \
"v_lshrrev_b32 v12, 4, v8\n" \
"v_lshlrev_b32 v12, 2, v12\n" \
"v_sub_u32 v12, v9, v12\n" \
"v_and_b32 v10, 1, v9\n" \
"v_cmp_eq_u32 s[40:41], 0, v10\n" \
"v_cmp_ne_u32 s[42:43], 0, v10\n" \
"s_mov_b64 s[88:89], s[58:59]\n" \
"s_mov_b32 s96, 0x3e8293ee\n" \
"v_add_u32 v11, 16, v12\n" \
"v_cmp_ge_u32 s[56:57], 14, v11\n" \
"s_nop 0\n" \
"s_and_b64 s[56:57], s[56:57], s[40:41]\n" \
"v_add_u32 v11, 15, v12\n" \
"v_cmp_ge_u32 s[58:59], 14, v11\n" \
"s_nop 0\n" \
"s_and_b64 s[58:59], s[58:59], s[42:43]\n" \
"v_add_u32 v11, 14, v12\n" \
"v_cmp_ge_u32 s[60:61], 14, v11\n" \
"s_nop 0\n" \
"s_and_b64 s[60:61], s[60:61], s[40:41]\n" \
"v_add_u32 v11, 13, v12\n" \
"v_cmp_ge_u32 s[62:63], 14, v11\n" \
"s_nop 0\n" \
"s_and_b64 s[62:63], s[62:63], s[42:43]\n" \
"v_add_u32 v11, 0, v12\n" \
"v_cmp_ge_u32 s[64:65], 14, v11\n" \
"s_nop 0\n" \
"s_and_b64 s[64:65], s[64:65], s[40:41]\n" \
"v_add_u32 v11, -1, v12\n" \
"v_cmp_ge_u32 s[66:67], 14, v11\n" \
"s_nop 0\n" \
"s_and_b64 s[66:67], s[66:67], s[42:43]\n" \
"v_add_u32 v11, -2, v12\n" \
"v_cmp_ge_u32 s[68:69], 14, v11\n" \
"s_nop 0\n" \
"s_and_b64 s[68:69], s[68:69], s[40:41]\n" \
"v_add_u32 v11, -3, v12\n" \
"v_cmp_ge_u32 s[70:71], 14, v11\n" \
"s_nop 0\n" \
"s_and_b64 s[70:71], s[70:71], s[42:43]\n" \
"s_mov_b32 s81, s80\n" \
"s_and_b64 s[40:41], s[56:57], s[80:81]\n" \
"s_and_b64 s[42:43], s[58:59], s[80:81]\n" \
"s_and_b64 s[44:45], s[60:61], s[80:81]\n" \
"s_and_b64 s[46:47], s[62:63], s[80:81]\n" \
"s_nop 1\n" \
"v_mov_b32 v20, 0x3f803f80\n" \
"v_mov_b32 v21, 0x3f803f80\n" \
"v_mov_b32 v22, 0x3f803f80\n" \
"v_mov_b32 v23, 0x3f803f80\n" \
"v_mov_b32 v19, 0xff800000\n" \
"ds_read_b128 v[24:27], v14 offset:0\n" \
"ds_read_b128 v[28:31], v15 offset:0\n" \
"ds_read_b128 v[32:35], v15 offset:512\n" \
"ds_read_b128 v[68:71], v14 offset:1024\n" \
"ds_read_b128 v[72:75], v15 offset:512\n" \
"ds_read_b128 v[76:79], v15 offset:1024\n" \
"ds_read_b128 v[112:115], v14 offset:2048\n" \
"ds_read_b128 v[116:119], v15 offset:1024\n" \
"ds_read_b128 v[120:123], v15 offset:1536\n" \
"ds_read_b128 v[156:159], v14 offset:3072\n" \
"ds_read_b128 v[160:163], v15 offset:1536\n" \
"ds_read_b128 v[164:167], v15 offset:2048\n" \
"ds_read_b128 v[200:203], v14 offset:4096\n" \
"ds_read_b128 v[204:207], v15 offset:2048\n" \
"ds_read_b128 v[208:211], v15 offset:2560\n" \
"ds_read_b64_tr_b16 v[36:37], v16 offset:0\n" \
"ds_read_b64_tr_b16 v[38:39], v16 offset:512\n" \
"ds_read_b64_tr_b16 v[40:41], v16 offset:8704\n" \
"ds_read_b64_tr_b16 v[42:43], v16 offset:9216\n" \
"ds_read_b64_tr_b16 v[80:81], v16 offset:512\n" \
"ds_read_b64_tr_b16 v[82:83], v16 offset:1024\n" \
"ds_read_b64_tr_b16 v[84:85], v16 offset:9216\n" \
"ds_read_b64_tr_b16 v[86:87], v16 offset:9728\n" \
"ds_read_b64_tr_b16 v[124:125], v16 offset:1024\n" \
"ds_read_b64_tr_b16 v[126:127], v16 offset:1536\n" \
"ds_read_b64_tr_b16 v[128:129], v16 offset:9728\n" \
"ds_read_b64_tr_b16 v[130:131], v16 offset:10240\n" \
"ds_read_b64_tr_b16 v[168:169], v16 offset:1536\n" \
"ds_read_b64_tr_b16 v[170:171], v16 offset:2048\n" \
"ds_read_b64_tr_b16 v[172:173], v16 offset:10240\n" \
"ds_read_b64_tr_b16 v[174:175], v16 offset:10752\n" \
"ds_read_b64_tr_b16 v[212:213], v16 offset:2048\n" \
"ds_read_b64_tr_b16 v[214:215], v16 offset:2560\n" \
"ds_read_b64_tr_b16 v[216:217], v16 offset:10752\n" \
"ds_read_b64_tr_b16 v[218:219], v16 offset:11264\n" \
"s_waitcnt lgkmcnt(15)\n" \
"v_mfma_f32_16x16x32_bf16 v[44:47], v[28:31], v[24:27], 0\n" \
"v_mfma_f32_16x16x32_bf16 v[88:91], v[72:75], v[68:71], 0\n" \
"v_mfma_f32_16x16x32_bf16 v[132:135], v[116:119], v[112:115], 0\n" \
"v_mfma_f32_16x16x32_bf16 v[176:179], v[160:163], v[156:159], 0\n" \
"v_mfma_f32_16x16x32_bf16 v[220:223], v[204:207], v[200:203], 0\n" \
"v_mfma_f32_16x16x32_bf16 v[48:51], v[32:35], v[24:27], 0\n" \
"v_mfma_f32_16x16x32_bf16 v[92:95], v[76:79], v[68:71], 0\n" \
"v_mfma_f32_16x16x32_bf16 v[136:139], v[120:123], v[112:115], 0\n" \
"v_mfma_f32_16x16x32_bf16 v[180:183], v[164:167], v[156:159], 0\n" \
"v_mfma_f32_16x16x32_bf16 v[224:227], v[208:211], v[200:203], 0\n" \
"s_waitcnt lgkmcnt(0)\n" \
"v_cndmask_b32 v88, v19, v88, s[56:57]\n" \
"v_cndmask_b32 v132, v19, v132, s[56:57]\n" \
"v_cndmask_b32 v176, v19, v176, s[56:57]\n" \
"v_cndmask_b32 v220, v19, v220, s[56:57]\n" \
"v_cndmask_b32 v44, v19, v44, s[40:41]\n" \
"v_cndmask_b32 v89, v19, v89, s[58:59]\n" \
"v_cndmask_b32 v133, v19, v133, s[58:59]\n" \
"v_cndmask_b32 v177, v19, v177, s[58:59]\n" \
"v_cndmask_b32 v221, v19, v221, s[58:59]\n" \
"v_cndmask_b32 v45, v19, v45, s[42:43]\n" \
"v_cndmask_b32 v90, v19, v90, s[60:61]\n" \
"v_cndmask_b32 v134, v19, v134, s[60:61]\n" \
"v_cndmask_b32 v178, v19, v178, s[60:61]\n" \
"v_cndmask_b32 v222, v19, v222, s[60:61]\n" \
"v_cndmask_b32 v46, v19, v46, s[44:45]\n" \
"v_cndmask_b32 v91, v19, v91, s[62:63]\n" \
"v_cndmask_b32 v135, v19, v135, s[62:63]\n" \
"v_cndmask_b32 v179, v19, v179, s[62:63]\n" \
"v_cndmask_b32 v223, v19, v223, s[62:63]\n" \
"v_cndmask_b32 v47, v19, v47, s[46:47]\n" \
"v_cndmask_b32 v92, v19, v92, s[64:65]\n" \
"v_cndmask_b32 v136, v19, v136, s[64:65]\n" \
"v_cndmask_b32 v180, v19, v180, s[64:65]\n" \
"v_cndmask_b32 v224, v19, v224, s[64:65]\n" \
"v_cndmask_b32 v48, v19, v48, s[64:65]\n" \
"v_cndmask_b32 v93, v19, v93, s[66:67]\n" \
"v_cndmask_b32 v137, v19, v137, s[66:67]\n" \
"v_cndmask_b32 v181, v19, v181, s[66:67]\n" \
"v_cndmask_b32 v225, v19, v225, s[66:67]\n" \
"v_cndmask_b32 v49, v19, v49, s[66:67]\n" \
"v_cndmask_b32 v94, v19, v94, s[68:69]\n" \
"v_cndmask_b32 v138, v19, v138, s[68:69]\n" \
"v_cndmask_b32 v182, v19, v182, s[68:69]\n" \
"v_cndmask_b32 v226, v19, v226, s[68:69]\n" \
"v_cndmask_b32 v50, v19, v50, s[68:69]\n" \
"v_cndmask_b32 v95, v19, v95, s[70:71]\n" \
"v_cndmask_b32 v139, v19, v139, s[70:71]\n" \
"v_cndmask_b32 v183, v19, v183, s[70:71]\n" \
"v_cndmask_b32 v227, v19, v227, s[70:71]\n" \
"v_cndmask_b32 v51, v19, v51, s[70:71]\n" \
"v_max3_f32 v96, v88, v89, v90\n" \
"v_max3_f32 v140, v132, v133, v134\n" \
"v_max3_f32 v184, v176, v177, v178\n" \
"v_max3_f32 v228, v220, v221, v222\n" \
"v_max3_f32 v52, v44, v45, v46\n" \
"v_max3_f32 v97, v91, v92, v93\n" \
"v_max3_f32 v141, v135, v136, v137\n" \
"v_max3_f32 v185, v179, v180, v181\n" \
"v_max3_f32 v229, v223, v224, v225\n" \
"v_max3_f32 v53, v47, v48, v49\n" \
"v_max3_f32 v96, v96, v94, v95\n" \
"v_max3_f32 v140, v140, v138, v139\n" \
"v_max3_f32 v184, v184, v182, v183\n" \
"v_max3_f32 v228, v228, v226, v227\n" \
"v_max3_f32 v52, v52, v50, v51\n" \
"v_max_f32 v96, v96, v97\n" \
"v_max_f32 v140, v140, v141\n" \
"v_max_f32 v184, v184, v185\n" \
"v_max_f32 v228, v228, v229\n" \
"v_max_f32 v52, v52, v53\n" \
"v_mov_b32 v97, v96\n" \
"v_mov_b32 v141, v140\n" \
"v_mov_b32 v185, v184\n" \
"v_mov_b32 v229, v228\n" \
"v_mov_b32 v53, v52\n" \
"v_permlane16_swap_b32 v96, v97\n" \
"v_permlane16_swap_b32 v140, v141\n" \
"v_permlane16_swap_b32 v184, v185\n" \
"v_permlane16_swap_b32 v228, v229\n" \
"v_permlane16_swap_b32 v52, v53\n" \
"v_max_f32 v96, v96, v97\n" \
"v_max_f32 v140, v140, v141\n" \
"v_max_f32 v184, v184, v185\n" \
"v_max_f32 v228, v228, v229\n" \
"v_max_f32 v52, v52, v53\n" \
"v_mov_b32 v97, v96\n" \
"v_mov_b32 v141, v140\n" \
"v_mov_b32 v185, v184\n" \
"v_mov_b32 v229, v228\n" \
"v_mov_b32 v53, v52\n" \
"v_permlane32_swap_b32 v96, v97\n" \
"v_permlane32_swap_b32 v140, v141\n" \
"v_permlane32_swap_b32 v184, v185\n" \
"v_permlane32_swap_b32 v228, v229\n" \
"v_permlane32_swap_b32 v52, v53\n" \
"v_max_f32 v96, v96, v97\n" \
"v_max_f32 v140, v140, v141\n" \
"v_max_f32 v184, v184, v185\n" \
"v_max_f32 v228, v228, v229\n" \
"v_max_f32 v52, v52, v53\n" \
"v_mul_f32 v96, s96, v96\n" \
"v_mul_f32 v140, s96, v140\n" \
"v_mul_f32 v184, s96, v184\n" \
"v_mul_f32 v228, s96, v228\n" \
"v_mul_f32 v52, s96, v52\n" \
"v_fma_f32 v88, v88, s96, -v96\n" \
"v_fma_f32 v132, v132, s96, -v140\n" \
"v_fma_f32 v176, v176, s96, -v184\n" \
"v_fma_f32 v220, v220, s96, -v228\n" \
"v_fma_f32 v44, v44, s96, -v52\n" \
"v_fma_f32 v89, v89, s96, -v96\n" \
"v_fma_f32 v133, v133, s96, -v140\n" \
"v_fma_f32 v177, v177, s96, -v184\n" \
"v_fma_f32 v221, v221, s96, -v228\n" \
"v_fma_f32 v45, v45, s96, -v52\n" \
"v_fma_f32 v90, v90, s96, -v96\n" \
"v_fma_f32 v134, v134, s96, -v140\n" \
"v_fma_f32 v178, v178, s96, -v184\n" \
"v_fma_f32 v222, v222, s96, -v228\n" \
"v_fma_f32 v46, v46, s96, -v52\n" \
"v_fma_f32 v91, v91, s96, -v96\n" \
"v_fma_f32 v135, v135, s96, -v140\n" \
"v_fma_f32 v179, v179, s96, -v184\n" \
"v_fma_f32 v223, v223, s96, -v228\n" \
"v_fma_f32 v47, v47, s96, -v52\n" \
"v_fma_f32 v92, v92, s96, -v96\n" \
"v_fma_f32 v136, v136, s96, -v140\n" \
"v_fma_f32 v180, v180, s96, -v184\n" \
"v_fma_f32 v224, v224, s96, -v228\n" \
"v_fma_f32 v48, v48, s96, -v52\n" \
"v_fma_f32 v93, v93, s96, -v96\n" \
"v_fma_f32 v137, v137, s96, -v140\n" \
"v_fma_f32 v181, v181, s96, -v184\n" \
"v_fma_f32 v225, v225, s96, -v228\n" \
"v_fma_f32 v49, v49, s96, -v52\n" \
"v_fma_f32 v94, v94, s96, -v96\n" \
"v_fma_f32 v138, v138, s96, -v140\n" \
"v_fma_f32 v182, v182, s96, -v184\n" \
"v_fma_f32 v226, v226, s96, -v228\n" \
"v_fma_f32 v50, v50, s96, -v52\n" \
"v_fma_f32 v95, v95, s96, -v96\n" \
"v_fma_f32 v139, v139, s96, -v140\n" \
"v_fma_f32 v183, v183, s96, -v184\n" \
"v_fma_f32 v227, v227, s96, -v228\n" \
"v_fma_f32 v51, v51, s96, -v52\n" \
"v_exp_f32 v88, v88\n" \
"v_exp_f32 v132, v132\n" \
"v_exp_f32 v176, v176\n" \
"v_exp_f32 v220, v220\n" \
"v_exp_f32 v44, v44\n" \
"v_exp_f32 v89, v89\n" \
"v_exp_f32 v133, v133\n" \
"v_exp_f32 v177, v177\n" \
"v_exp_f32 v221, v221\n" \
"v_exp_f32 v45, v45\n" \
"v_exp_f32 v90, v90\n" \
"v_exp_f32 v134, v134\n" \
"v_exp_f32 v178, v178\n" \
"v_exp_f32 v222, v222\n" \
"v_exp_f32 v46, v46\n" \
"v_exp_f32 v91, v91\n" \
"v_exp_f32 v135, v135\n" \
"v_exp_f32 v179, v179\n" \
"v_exp_f32 v223, v223\n" \
"v_exp_f32 v47, v47\n" \
"v_exp_f32 v92, v92\n" \
"v_exp_f32 v136, v136\n" \
"v_exp_f32 v180, v180\n" \
"v_exp_f32 v224, v224\n" \
"v_exp_f32 v48, v48\n" \
"v_exp_f32 v93, v93\n" \
"v_exp_f32 v137, v137\n" \
"v_exp_f32 v181, v181\n" \
"v_exp_f32 v225, v225\n" \
"v_exp_f32 v49, v49\n" \
"v_exp_f32 v94, v94\n" \
"v_exp_f32 v138, v138\n" \
"v_exp_f32 v182, v182\n" \
"v_exp_f32 v226, v226\n" \
"v_exp_f32 v50, v50\n" \
"v_exp_f32 v95, v95\n" \
"v_exp_f32 v139, v139\n" \
"v_exp_f32 v183, v183\n" \
"v_exp_f32 v227, v227\n" \
"v_exp_f32 v51, v51\n" \
"v_cvt_pk_bf16_f32 v100, v88, v89\n" \
"v_cvt_pk_bf16_f32 v144, v132, v133\n" \
"v_cvt_pk_bf16_f32 v188, v176, v177\n" \
"v_cvt_pk_bf16_f32 v232, v220, v221\n" \
"v_cvt_pk_bf16_f32 v56, v44, v45\n" \
"v_cvt_pk_bf16_f32 v101, v90, v91\n" \
"v_cvt_pk_bf16_f32 v145, v134, v135\n" \
"v_cvt_pk_bf16_f32 v189, v178, v179\n" \
"v_cvt_pk_bf16_f32 v233, v222, v223\n" \
"v_cvt_pk_bf16_f32 v57, v46, v47\n" \
"v_cvt_pk_bf16_f32 v102, v92, v93\n" \
"v_cvt_pk_bf16_f32 v146, v136, v137\n" \
"v_cvt_pk_bf16_f32 v190, v180, v181\n" \
"v_cvt_pk_bf16_f32 v234, v224, v225\n" \
"v_cvt_pk_bf16_f32 v58, v48, v49\n" \
"v_cvt_pk_bf16_f32 v103, v94, v95\n" \
"v_cvt_pk_bf16_f32 v147, v138, v139\n" \
"v_cvt_pk_bf16_f32 v191, v182, v183\n" \
"v_cvt_pk_bf16_f32 v235, v226, v227\n" \
"v_cvt_pk_bf16_f32 v59, v50, v51\n" \
"v_mfma_f32_16x16x32_bf16 v[104:107], v[20:23], v[100:103], 0\n" \
"v_mfma_f32_16x16x32_bf16 v[148:151], v[20:23], v[144:147], 0\n" \
"v_mfma_f32_16x16x32_bf16 v[192:195], v[20:23], v[188:191], 0\n" \
"v_mfma_f32_16x16x32_bf16 v[236:239], v[20:23], v[232:235], 0\n" \
"v_mfma_f32_16x16x32_bf16 v[60:63], v[20:23], v[56:59], 0\n" \
"v_mfma_f32_16x16x32_bf16 v[72:75], v[80:83], v[100:103], 0\n" \
"v_mfma_f32_16x16x32_bf16 v[116:119], v[124:127], v[144:147], 0\n" \
"v_mfma_f32_16x16x32_bf16 v[160:163], v[168:171], v[188:191], 0\n" \
"v_mfma_f32_16x16x32_bf16 v[204:207], v[212:215], v[232:235], 0\n" \
"v_mfma_f32_16x16x32_bf16 v[28:31], v[36:39], v[56:59], 0\n" \
"v_mfma_f32_16x16x32_bf16 v[76:79], v[84:87], v[100:103], 0\n" \
"v_mfma_f32_16x16x32_bf16 v[120:123], v[128:131], v[144:147], 0\n" \
"v_mfma_f32_16x16x32_bf16 v[164:167], v[172:175], v[188:191], 0\n" \
"v_mfma_f32_16x16x32_bf16 v[208:211], v[216:219], v[232:235], 0\n" \
"v_mfma_f32_16x16x32_bf16 v[32:35], v[40:43], v[56:59], 0\n" \
"v_rcp_f32 v98, v104\n" \
"v_rcp_f32 v142, v148\n" \
"v_rcp_f32 v186, v192\n" \
"v_rcp_f32 v230, v236\n" \
"v_rcp_f32 v54, v60\n" \
"v_pk_mul_f32 v[72:73], v[72:73], v[98:99] op_sel_hi:[1,0]\n" \
"v_pk_mul_f32 v[116:117], v[116:117], v[142:143] op_sel_hi:[1,0]\n" \
"v_pk_mul_f32 v[160:161], v[160:161], v[186:187] op_sel_hi:[1,0]\n" \
"v_pk_mul_f32 v[204:205], v[204:205], v[230:231] op_sel_hi:[1,0]\n" \
"v_pk_mul_f32 v[28:29], v[28:29], v[54:55] op_sel_hi:[1,0]\n" \
"v_pk_mul_f32 v[74:75], v[74:75], v[98:99] op_sel_hi:[1,0]\n" \
"v_pk_mul_f32 v[118:119], v[118:119], v[142:143] op_sel_hi:[1,0]\n" \
"v_pk_mul_f32 v[162:163], v[162:163], v[186:187] op_sel_hi:[1,0]\n" \
"v_pk_mul_f32 v[206:207], v[206:207], v[230:231] op_sel_hi:[1,0]\n" \
"v_pk_mul_f32 v[30:31], v[30:31], v[54:55] op_sel_hi:[1,0]\n" \
"v_pk_mul_f32 v[76:77], v[76:77], v[98:99] op_sel_hi:[1,0]\n" \
"v_pk_mul_f32 v[120:121], v[120:121], v[142:143] op_sel_hi:[1,0]\n" \
"v_pk_mul_f32 v[164:165], v[164:165], v[186:187] op_sel_hi:[1,0]\n" \
"v_pk_mul_f32 v[208:209], v[208:209], v[230:231] op_sel_hi:[1,0]\n" \
"v_pk_mul_f32 v[32:33], v[32:33], v[54:55] op_sel_hi:[1,0]\n" \
"v_pk_mul_f32 v[78:79], v[78:79], v[98:99] op_sel_hi:[1,0]\n" \
"v_pk_mul_f32 v[122:123], v[122:123], v[142:143] op_sel_hi:[1,0]\n" \
"v_pk_mul_f32 v[166:167], v[166:167], v[186:187] op_sel_hi:[1,0]\n" \
"v_pk_mul_f32 v[210:211], v[210:211], v[230:231] op_sel_hi:[1,0]\n" \
"v_pk_mul_f32 v[34:35], v[34:35], v[54:55] op_sel_hi:[1,0]\n" \
"v_cvt_pk_bf16_f32 v68, v72, v73\n" \
"v_cvt_pk_bf16_f32 v112, v116, v117\n" \
"v_cvt_pk_bf16_f32 v156, v160, v161\n" \
"v_cvt_pk_bf16_f32 v200, v204, v205\n" \
"v_cvt_pk_bf16_f32 v24, v28, v29\n" \
"v_cvt_pk_bf16_f32 v70, v76, v77\n" \
"v_cvt_pk_bf16_f32 v114, v120, v121\n" \
"v_cvt_pk_bf16_f32 v158, v164, v165\n" \
"v_cvt_pk_bf16_f32 v202, v208, v209\n" \
"v_cvt_pk_bf16_f32 v26, v32, v33\n" \
"v_cvt_pk_bf16_f32 v69, v74, v75\n" \
"v_cvt_pk_bf16_f32 v113, v118, v119\n" \
"v_cvt_pk_bf16_f32 v157, v162, v163\n" \
"v_cvt_pk_bf16_f32 v201, v206, v207\n" \
"v_cvt_pk_bf16_f32 v25, v30, v31\n" \
"v_cvt_pk_bf16_f32 v71, v78, v79\n" \
"v_cvt_pk_bf16_f32 v115, v122, v123\n" \
"v_cvt_pk_bf16_f32 v159, v166, v167\n" \
"v_cvt_pk_bf16_f32 v203, v210, v211\n" \
"v_cvt_pk_bf16_f32 v27, v34, v35\n" \
"v_permlane16_swap_b32 v68, v70\n" \
"v_permlane16_swap_b32 v112, v114\n" \
"v_permlane16_swap_b32 v156, v158\n" \
"v_permlane16_swap_b32 v200, v202\n" \
"v_permlane16_swap_b32 v24, v26\n" \
"v_permlane16_swap_b32 v69, v71\n" \
"v_permlane16_swap_b32 v113, v115\n" \
"v_permlane16_swap_b32 v157, v159\n" \
"v_permlane16_swap_b32 v201, v203\n" \
"v_permlane16_swap_b32 v25, v27\n" \
"v_add_u32 v108, 32768, v17\n" \
"v_add_u32 v152, 65536, v17\n" \
"v_add_u32 v196, 98304, v17\n" \
"v_add_u32 v240, 131072, v17\n" \
"v_add_u32 v64, 0, v17\n" \
"global_store_dwordx4 v108, v[68:71], s[88:89] sc1\n" \
"global_store_dwordx4 v152, v[112:115], s[88:89] sc1\n" \
"global_store_dwordx4 v196, v[156:159], s[88:89] sc1\n" \
"global_store_dwordx4 v240, v[200:203], s[88:89] sc1\n" \
"global_store_dwordx4 v64, v[24:27], s[88:89] sc1\n" \
"LE2%=:\n" \
"s_branch LEND%=\n" \
"LKD%=:\n" \
"s_waitcnt lgkmcnt(9)\n" \
"v_mfma_f32_16x16x32_bf16 a[0:3], v[92:95], v[20:23], 0\n" \
"ds_read_b64_tr_b16 v[104:105], v4 offset:0\n" \
"v_mfma_f32_16x16x32_bf16 a[40:43], v[92:95], v[24:27], 0\n" \
"s_add_u32 m0, s49, 79872\n" \
"v_mfma_f32_16x16x32_bf16 a[80:83], v[92:95], v[28:31], 0\n" \
"ds_read_b64_tr_b16 v[106:107], v5 offset:0\n" \
"global_load_lds_dwordx4 v10, s[40:41] offset:0\n" \
"v_mfma_f32_16x16x32_bf16 a[120:123], v[92:95], v[32:35], 0\n" \
"s_waitcnt lgkmcnt(7)\n" \
"v_mfma_f32_16x16x32_bf16 a[160:163], v[92:95], v[36:39], 0\n" \
"v_mfma_f32_16x16x32_bf16 v[124:127], v[92:95], v[40:43], 0\n" \
"global_load_lds_dwordx4 v11, s[40:41] offset:1024\n" \
"v_mfma_f32_16x16x32_bf16 v[164:167], v[92:95], v[44:47], 0\n" \
"v_mfma_f32_16x16x32_bf16 v[204:207], v[92:95], v[48:51], 0\n" \
"global_load_lds_dwordx4 v12, s[40:41] offset:2048\n" \
"s_waitcnt lgkmcnt(4)\n" \
"v_mfma_f32_16x16x32_bf16 a[4:7], v[96:99], v[20:23], 0\n" \
"ds_read_b64_tr_b16 v[108:109], v1 offset:8192\n" \
"v_mfma_f32_16x16x32_bf16 a[44:47], v[96:99], v[24:27], 0\n" \
"v_mfma_f32_16x16x32_bf16 a[84:87], v[96:99], v[28:31], 0\n" \
"ds_read_b64_tr_b16 v[110:111], v1 offset:9216\n" \
"global_load_lds_dwordx4 v13, s[40:41] offset:3072\n" \
"v_mfma_f32_16x16x32_bf16 a[124:127], v[96:99], v[32:35], 0\n" \
"s_add_u32 s40, s40, 64\n" \
"s_addc_u32 s41, s41, 0\n" \
"v_mfma_f32_16x16x32_bf16 a[164:167], v[96:99], v[36:39], 0\n" \
"s_add_u32 m0, s50, 79872\n" \
"s_nop 0\n" \
"global_load_lds_dwordx4 v14, s[42:43] offset:0\n" \
"v_mfma_f32_16x16x32_bf16 v[128:131], v[96:99], v[40:43], 0\n" \
"v_mfma_f32_16x16x32_bf16 v[168:171], v[96:99], v[44:47], 0\n" \
"v_mfma_f32_16x16x32_bf16 v[208:211], v[96:99], v[48:51], 0\n" \
"global_load_lds_dwordx4 v15, s[42:43] offset:1024\n" \
"s_waitcnt lgkmcnt(4)\n" \
"v_mfma_f32_16x16x32_bf16 a[8:11], v[100:103], v[20:23], 0\n" \
"s_add_u32 s42, s42, 229376\n" \
"s_addc_u32 s43, s43, 0\n" \
"v_mfma_f32_16x16x32_bf16 a[48:51], v[100:103], v[24:27], 0\n" \
"ds_read_b64_tr_b16 v[92:93], v2 offset:8192\n" \
"s_add_u32 m0, s51, 79872\n" \
"v_mfma_f32_16x16x32_bf16 a[88:91], v[100:103], v[28:31], 0\n" \
"global_load_lds_dwordx4 v14, s[44:45] offset:0\n" \
"ds_read_b64_tr_b16 v[94:95], v2 offset:9216\n" \
"v_mfma_f32_16x16x32_bf16 a[128:131], v[100:103], v[32:35], 0\n" \
"v_mfma_f32_16x16x32_bf16 a[168:171], v[100:103], v[36:39], 0\n" \
"global_load_lds_dwordx4 v15, s[44:45] offset:1024\n" \
"v_mfma_f32_16x16x32_bf16 v[132:135], v[100:103], v[40:43], 0\n" \
"s_add_u32 s44, s44, 229376\n" \
"s_addc_u32 s45, s45, 0\n" \
"v_mfma_f32_16x16x32_bf16 v[172:175], v[100:103], v[44:47], 0\n" \
"s_add_u32 m0, s52, 79872\n" \
"s_nop 0\n" \
"global_load_lds_dwordx4 v16, s[46:47]\n" \
"v_mfma_f32_16x16x32_bf16 v[212:215], v[100:103], v[48:51], 0\n" \
"s_waitcnt lgkmcnt(4)\n" \
"v_mfma_f32_16x16x32_bf16 a[12:15], v[104:107], v[20:23], 0\n" \
"ds_read_b64_tr_b16 v[96:97], v3 offset:8192\n" \
"v_mfma_f32_16x16x32_bf16 a[52:55], v[104:107], v[24:27], 0\n" \
"s_cmp_eq_u32 s53, 0\n" \
"s_cbranch_scc1 LZ%=_83\n" \
"global_load_lds_dwordx4 v17, s[46:47] offset:1024\n" \
"LZ%=_83:\n" \
"v_mfma_f32_16x16x32_bf16 a[92:95], v[104:107], v[28:31], 0\n" \
"s_add_u32 s46, s46, s48\n" \
"s_addc_u32 s47, s47, 0\n" \
"v_mfma_f32_16x16x32_bf16 a[132:135], v[104:107], v[32:35], 0\n" \
"ds_read_b64_tr_b16 v[98:99], v3 offset:9216\n" \
"s_add_u32 m0, s49, 119808\n" \
"v_mfma_f32_16x16x32_bf16 a[172:175], v[104:107], v[36:39], 0\n" \
"global_load_lds_dwordx4 v10, s[40:41] offset:0\n" \
"v_mfma_f32_16x16x32_bf16 v[136:139], v[104:107], v[40:43], 0\n" \
"v_mfma_f32_16x16x32_bf16 v[176:179], v[104:107], v[44:47], 0\n" \
"global_load_lds_dwordx4 v11, s[40:41] offset:1024\n" \
"v_mfma_f32_16x16x32_bf16 v[216:219], v[104:107], v[48:51], 0\n" \
"s_waitcnt lgkmcnt(4)\n" \
"v_mfma_f32_16x16x32_bf16 a[16:19], v[108:111], v[20:23], 0\n" \
"ds_read_b64_tr_b16 v[100:101], v6 offset:0\n" \
"global_load_lds_dwordx4 v12, s[40:41] offset:2048\n" \
"v_mfma_f32_16x16x32_bf16 a[56:59], v[108:111], v[24:27], 0\n" \
"v_mfma_f32_16x16x32_bf16 a[96:99], v[108:111], v[28:31], 0\n" \
"ds_read_b64_tr_b16 v[102:103], v7 offset:0\n" \
"v_mfma_f32_16x16x32_bf16 a[136:139], v[108:111], v[32:35], 0\n" \
"global_load_lds_dwordx4 v13, s[40:41] offset:3072\n" \
"v_mfma_f32_16x16x32_bf16 a[176:179], v[108:111], v[36:39], 0\n" \
"s_add_u32 s40, s40, 64\n" \
"s_addc_u32 s41, s41, 0\n" \
"v_mfma_f32_16x16x32_bf16 v[140:143], v[108:111], v[40:43], 0\n" \
"s_add_u32 m0, s50, 119808\n" \
"s_nop 0\n" \
"global_load_lds_dwordx4 v14, s[42:43] offset:0\n" \
"v_mfma_f32_16x16x32_bf16 v[180:183], v[108:111], v[44:47], 0\n" \
"v_mfma_f32_16x16x32_bf16 v[220:223], v[108:111], v[48:51], 0\n" \
"s_waitcnt lgkmcnt(4)\n" \
"v_mfma_f32_16x16x32_bf16 a[20:23], v[92:95], v[20:23], 0\n" \
"ds_read_b64_tr_b16 v[104:105], v8 offset:0\n" \
"global_load_lds_dwordx4 v15, s[42:43] offset:1024\n" \
"v_mfma_f32_16x16x32_bf16 a[60:63], v[92:95], v[24:27], 0\n" \
"s_add_u32 s42, s42, 229376\n" \
"s_addc_u32 s43, s43, 0\n" \
"v_mfma_f32_16x16x32_bf16 a[100:103], v[92:95], v[28:31], 0\n" \
"s_add_u32 m0, s51, 119808\n" \
"ds_read_b64_tr_b16 v[106:107], v8 offset:768\n" \
"v_mfma_f32_16x16x32_bf16 a[140:143], v[92:95], v[32:35], 0\n" \
"global_load_lds_dwordx4 v14, s[44:45] offset:0\n" \
"v_mfma_f32_16x16x32_bf16 a[180:183], v[92:95], v[36:39], 0\n" \
"v_mfma_f32_16x16x32_bf16 v[144:147], v[92:95], v[40:43], 0\n" \
"global_load_lds_dwordx4 v15, s[44:45] offset:1024\n" \
"v_mfma_f32_16x16x32_bf16 v[184:187], v[92:95], v[44:47], 0\n" \
"s_add_u32 s44, s44, 229376\n" \
"s_addc_u32 s45, s45, 0\n" \
"v_mfma_f32_16x16x32_bf16 v[224:227], v[92:95], v[48:51], 0\n" \
"s_add_u32 m0, s52, 119808\n" \
"v_mfma_f32_16x16x32_bf16 a[216:219], a[196:199], v[92:95], 0\n" \
"s_waitcnt lgkmcnt(4)\n" \
"v_mfma_f32_16x16x32_bf16 a[24:27], v[96:99], v[20:23], 0\n" \
"global_load_lds_dwordx4 v16, s[46:47]\n" \
"ds_read_b64_tr_b16 v[108:109], v9 offset:0\n" \
"v_mfma_f32_16x16x32_bf16 a[64:67], v[96:99], v[24:27], 0\n" \
"v_mfma_f32_16x16x32_bf16 a[104:107], v[96:99], v[28:31], 0\n" \
"ds_read_b64_tr_b16 v[110:111], v9 offset:768\n" \
"v_mfma_f32_16x16x32_bf16 a[144:147], v[96:99], v[32:35], 0\n" \
"s_cmp_eq_u32 s53, 0\n" \
"s_cbranch_scc1 LZ%=_93\n" \
"global_load_lds_dwordx4 v17, s[46:47] offset:1024\n" \
"LZ%=_93:\n" \
"v_mfma_f32_16x16x32_bf16 a[184:187], v[96:99], v[36:39], 0\n" \
"s_add_u32 s46, s46, s48\n" \
"s_addc_u32 s47, s47, 0\n" \
"v_mfma_f32_16x16x32_bf16 v[148:151], v[96:99], v[40:43], 0\n" \
"v_mfma_f32_16x16x32_bf16 v[188:191], v[96:99], v[44:47], 0\n" \
"v_mfma_f32_16x16x32_bf16 v[228:231], v[96:99], v[48:51], 0\n" \
"v_mfma_f32_16x16x32_bf16 a[216:219], a[200:203], v[96:99], a[216:219]\n" \
"s_waitcnt lgkmcnt(0)\n" \
"s_waitcnt vmcnt(18)\n" \
"s_barrier\n" \
"v_mfma_f32_16x16x32_bf16 a[28:31], v[100:103], v[20:23], 0\n" \
"v_add_u32 v1, 39936, v1\n" \
"ds_read_b64_tr_b16 v[92:93], v1 offset:0\n" \
"v_mfma_f32_16x16x32_bf16 a[68:71], v[100:103], v[24:27], 0\n" \
"ds_read_b64_tr_b16 v[94:95], v1 offset:1024\n" \
"v_mfma_f32_16x16x32_bf16 a[108:111], v[100:103], v[28:31], 0\n" \
"v_add_u32 v0, 39936, v0\n" \
"ds_read_b128 v[56:59], v0 offset:1024\n" \
"v_mfma_f32_16x16x32_bf16 a[148:151], v[100:103], v[32:35], 0\n" \
"v_add_u32 v4, 39936, v4\n" \
"ds_read_b128 v[60:63], v0 offset:2048\n" \
"v_mfma_f32_16x16x32_bf16 a[188:191], v[100:103], v[36:39], 0\n" \
"ds_read_b128 v[64:67], v0 offset:3072\n" \
"v_add_u32 v5, 39936, v5\n" \
"v_mfma_f32_16x16x32_bf16 v[152:155], v[100:103], v[40:43], 0\n" \
"ds_read_b128 v[68:71], v0 offset:4096\n" \
"v_mfma_f32_16x16x32_bf16 v[192:195], v[100:103], v[44:47], 0\n" \
"ds_read_b128 v[72:75], v0 offset:5120\n" \
"v_add_u32 v6, 39936, v6\n" \
"v_mfma_f32_16x16x32_bf16 v[232:235], v[100:103], v[48:51], 0\n" \
"ds_read_b128 v[76:79], v0 offset:6144\n" \
"v_mfma_f32_16x16x32_bf16 a[216:219], a[204:207], v[100:103], a[216:219]\n" \
"v_mfma_f32_16x16x32_bf16 a[32:35], v[104:107], v[20:23], 0\n" \
"ds_read_b128 v[80:83], v0 offset:7168\n" \
"v_add_u32 v7, 39936, v7\n" \
"v_mfma_f32_16x16x32_bf16 a[72:75], v[104:107], v[24:27], 0\n" \
"ds_read_b128 v[84:87], v0 offset:8192\n" \
"v_mfma_f32_16x16x32_bf16 a[112:115], v[104:107], v[28:31], 0\n" \
"v_add_u32 v18, 39936, v18\n" \
"ds_read_b128 v[88:91], v18 offset:0\n" \
"v_mfma_f32_16x16x32_bf16 a[152:155], v[104:107], v[32:35], 0\n" \
"v_add_u32 v8, 39936, v8\n" \
"v_add_u32 v2, 39936, v2\n" \
"v_mfma_f32_16x16x32_bf16 a[192:195], v[104:107], v[36:39], 0\n" \
"ds_read_b64_tr_b16 v[96:97], v2 offset:0\n" \
"ds_read_b64_tr_b16 v[98:99], v2 offset:1024\n" \
"v_mfma_f32_16x16x32_bf16 v[156:159], v[104:107], v[40:43], 0\n" \
"v_add_u32 v9, 39936, v9\n" \
"v_add_u32 v3, 39936, v3\n" \
"v_mfma_f32_16x16x32_bf16 v[196:199], v[104:107], v[44:47], 0\n" \
"ds_read_b64_tr_b16 v[100:101], v3 offset:0\n" \
"ds_read_b64_tr_b16 v[102:103], v3 offset:1024\n" \
"v_mfma_f32_16x16x32_bf16 v[236:239], v[104:107], v[48:51], 0\n" \
"v_mfma_f32_16x16x32_bf16 v[244:247], v[104:107], v[52:55], 0\n" \
"v_mfma_f32_16x16x32_bf16 a[216:219], a[208:211], v[104:107], a[216:219]\n" \
"v_mfma_f32_16x16x32_bf16 a[36:39], v[108:111], v[20:23], 0\n" \
"v_mfma_f32_16x16x32_bf16 a[76:79], v[108:111], v[24:27], 0\n" \
"v_mfma_f32_16x16x32_bf16 a[116:119], v[108:111], v[28:31], 0\n" \
"v_mfma_f32_16x16x32_bf16 a[156:159], v[108:111], v[32:35], 0\n" \
"v_mfma_f32_16x16x32_bf16 v[120:123], v[108:111], v[36:39], 0\n" \
"v_mfma_f32_16x16x32_bf16 v[160:163], v[108:111], v[40:43], 0\n" \
"s_cmp_lg_u32 s49, 0\n" \
"s_cbranch_scc1 LPC%=_73\n" \
"v_mbcnt_lo_u32_b32 v113, -1, 0\n" \
"v_mbcnt_hi_u32_b32 v113, -1, v113\n" \
"v_lshlrev_b32 v113, 7, v113\n" \
"s_getpc_b64 s[88:89]\n" \
"LPD%=_73:\n" \
"s_mov_b32 s86, " CODE_END "-LPD%=_73-4\n" \
"v_min_u32 v114, s86, v113\n" \
"global_load_dword v255, v114, s[88:89]\n" \
"v_add_u32 v113, 8192, v113\n" \
"v_min_u32 v114, s86, v113\n" \
"global_load_dword v255, v114, s[88:89]\n" \
"v_add_u32 v113, 8192, v113\n" \
"v_min_u32 v114, s86, v113\n" \
"global_load_dword v255, v114, s[88:89]\n" \
"v_add_u32 v113, 8192, v113\n" \
"v_min_u32 v114, s86, v113\n" \
"global_load_dword v255, v114, s[88:89]\n" \
"v_add_u32 v113, 8192, v113\n" \
"v_min_u32 v114, s86, v113\n" \
"global_load_dword v255, v114, s[88:89]\n" \
"v_add_u32 v113, 8192, v113\n" \
"v_min_u32 v114, s86, v113\n" \
"global_load_dword v255, v114, s[88:89]\n" \
"v_add_u32 v113, 8192, v113\n" \
"v_min_u32 v114, s86, v113\n" \
"global_load_dword v255, v114, s[88:89]\n" \
"v_add_u32 v113, 8192, v113\n" \
"v_min_u32 v114, s86, v113\n" \
"global_load_dword v255, v114, s[88:89]\n" \
"v_add_u32 v113, 8192, v113\n" \
"LPC%=_73:\n" \
"v_mfma_f32_16x16x32_bf16 v[200:203], v[108:111], v[44:47], 0\n" \
"v_mfma_f32_16x16x32_bf16 v[240:243], v[108:111], v[48:51], 0\n" \
"v_mfma_f32_16x16x32_bf16 a[216:219], a[212:215], v[108:111], a[216:219]\n" \
"s_waitcnt lgkmcnt(9)\n" \
"v_mfma_f32_16x16x32_bf16 a[0:3], v[92:95], v[56:59], a[0:3]\n" \
"ds_read_b64_tr_b16 v[104:105], v4 offset:0\n" \
"v_mfma_f32_16x16x32_bf16 a[40:43], v[92:95], v[60:63], a[40:43]\n" \
"s_cmp_eq_u32 s87, s93\n" \
"s_cbranch_scc1 LWR%=_104\n" \
"LWS%=_104:\n" \
"v_mfma_f32_16x16x32_bf16 a[80:83], v[92:95], v[64:67], a[80:83]\n" \
"ds_read_b64_tr_b16 v[106:107], v5 offset:0\n" \
"s_add_u32 m0, s49, 0\n" \
"v_mfma_f32_16x16x32_bf16 a[120:123], v[92:95], v[68:71], a[120:123]\n" \
"global_load_lds_dwordx4 v10, s[40:41] offset:0\n" \
"s_waitcnt lgkmcnt(7)\n" \
"v_mfma_f32_16x16x32_bf16 a[160:163], v[92:95], v[72:75], a[160:163]\n" \
"v_mfma_f32_16x16x32_bf16 v[124:127], v[92:95], v[76:79], v[124:127]\n" \
"v_mfma_f32_16x16x32_bf16 v[164:167], v[92:95], v[80:83], v[164:167]\n" \
"v_mfma_f32_16x16x32_bf16 v[204:207], v[92:95], v[84:87], v[204:207]\n" \
"s_waitcnt lgkmcnt(4)\n" \
"v_mfma_f32_16x16x32_bf16 a[4:7], v[96:99], v[56:59], a[4:7]\n" \
"ds_read_b64_tr_b16 v[108:109], v1 offset:8192\n" \
"global_load_lds_dwordx4 v11, s[40:41] offset:1024\n" \
"v_mfma_f32_16x16x32_bf16 a[44:47], v[96:99], v[60:63], a[44:47]\n" \
"v_mfma_f32_16x16x32_bf16 a[84:87], v[96:99], v[64:67], a[84:87]\n" \
"ds_read_b64_tr_b16 v[110:111], v1 offset:9216\n" \
"v_mfma_f32_16x16x32_bf16 a[124:127], v[96:99], v[68:71], a[124:127]\n" \
"v_mfma_f32_16x16x32_bf16 a[164:167], v[96:99], v[72:75], a[164:167]\n" \
"v_mfma_f32_16x16x32_bf16 v[128:131], v[96:99], v[76:79], v[128:131]\n" \
"global_load_lds_dwordx4 v12, s[40:41] offset:2048\n" \
"v_mfma_f32_16x16x32_bf16 v[168:171], v[96:99], v[80:83], v[168:171]\n" \
"v_mfma_f32_16x16x32_bf16 v[208:211], v[96:99], v[84:87], v[208:211]\n" \
"s_waitcnt lgkmcnt(4)\n" \
"v_mfma_f32_16x16x32_bf16 a[8:11], v[100:103], v[56:59], a[8:11]\n" \
"ds_read_b64_tr_b16 v[92:93], v2 offset:8192\n" \
"v_mfma_f32_16x16x32_bf16 a[48:51], v[100:103], v[60:63], a[48:51]\n" \
"v_mfma_f32_16x16x32_bf16 a[88:91], v[100:103], v[64:67], a[88:91]\n" \
"ds_read_b64_tr_b16 v[94:95], v2 offset:9216\n" \
"global_load_lds_dwordx4 v13, s[40:41] offset:3072\n" \
"v_mfma_f32_16x16x32_bf16 a[128:131], v[100:103], v[68:71], a[128:131]\n" \
"s_add_u32 s40, s40, 64\n" \
"s_addc_u32 s41, s41, 0\n" \
"v_mfma_f32_16x16x32_bf16 a[168:171], v[100:103], v[72:75], a[168:171]\n" \
"v_mfma_f32_16x16x32_bf16 v[132:135], v[100:103], v[76:79], v[132:135]\n" \
"v_mfma_f32_16x16x32_bf16 v[172:175], v[100:103], v[80:83], v[172:175]\n" \
"s_add_u32 m0, s50, 0\n" \
"v_mfma_f32_16x16x32_bf16 v[212:215], v[100:103], v[84:87], v[212:215]\n" \
"global_load_lds_dwordx4 v14, s[42:43] offset:0\n" \
"s_waitcnt lgkmcnt(4)\n" \
"v_mfma_f32_16x16x32_bf16 a[12:15], v[104:107], v[56:59], a[12:15]\n" \
"ds_read_b64_tr_b16 v[96:97], v3 offset:8192\n" \
"v_mfma_f32_16x16x32_bf16 a[52:55], v[104:107], v[60:63], a[52:55]\n" \
"v_mfma_f32_16x16x32_bf16 a[92:95], v[104:107], v[64:67], a[92:95]\n" \
"ds_read_b64_tr_b16 v[98:99], v3 offset:9216\n" \
"v_mfma_f32_16x16x32_bf16 a[132:135], v[104:107], v[68:71], a[132:135]\n" \
"v_mfma_f32_16x16x32_bf16 a[172:175], v[104:107], v[72:75], a[172:175]\n" \
"global_load_lds_dwordx4 v15, s[42:43] offset:1024\n" \
"v_mfma_f32_16x16x32_bf16 v[136:139], v[104:107], v[76:79], v[136:139]\n" \
"s_add_u32 s42, s42, 229376\n" \
"s_addc_u32 s43, s43, 0\n" \
"v_mfma_f32_16x16x32_bf16 v[176:179], v[104:107], v[80:83], v[176:179]\n" \
"v_mfma_f32_16x16x32_bf16 v[216:219], v[104:107], v[84:87], v[216:219]\n" \
"s_waitcnt lgkmcnt(4)\n" \
"v_mfma_f32_16x16x32_bf16 a[16:19], v[108:111], v[56:59], a[16:19]\n" \
"ds_read_b64_tr_b16 v[100:101], v6 offset:0\n" \
"s_add_u32 m0, s51, 0\n" \
"v_mfma_f32_16x16x32_bf16 a[56:59], v[108:111], v[60:63], a[56:59]\n" \
"global_load_lds_dwordx4 v14, s[44:45] offset:0\n" \
"v_mfma_f32_16x16x32_bf16 a[96:99], v[108:111], v[64:67], a[96:99]\n" \
"ds_read_b64_tr_b16 v[102:103], v7 offset:0\n" \
"v_mfma_f32_16x16x32_bf16 a[136:139], v[108:111], v[68:71], a[136:139]\n" \
"v_mfma_f32_16x16x32_bf16 a[176:179], v[108:111], v[72:75], a[176:179]\n" \
"v_mfma_f32_16x16x32_bf16 v[140:143], v[108:111], v[76:79], v[140:143]\n" \
"v_mfma_f32_16x16x32_bf16 v[180:183], v[108:111], v[80:83], v[180:183]\n" \
"global_load_lds_dwordx4 v15, s[44:45] offset:1024\n" \
"v_mfma_f32_16x16x32_bf16 v[220:223], v[108:111], v[84:87], v[220:223]\n" \
"s_add_u32 s44, s44, 229376\n" \
"s_addc_u32 s45, s45, 0\n" \
"s_waitcnt lgkmcnt(4)\n" \
"v_mfma_f32_16x16x32_bf16 a[20:23], v[92:95], v[56:59], a[20:23]\n" \
"ds_read_b64_tr_b16 v[104:105], v8 offset:0\n" \
"v_mfma_f32_16x16x32_bf16 a[60:63], v[92:95], v[60:63], a[60:63]\n" \
"v_mfma_f32_16x16x32_bf16 a[100:103], v[92:95], v[64:67], a[100:103]\n" \
"ds_read_b64_tr_b16 v[106:107], v8 offset:768\n" \
"s_add_u32 m0, s52, 0\n" \
"v_mfma_f32_16x16x32_bf16 a[140:143], v[92:95], v[68:71], a[140:143]\n" \
"global_load_lds_dwordx4 v16, s[46:47]\n" \
"v_mfma_f32_16x16x32_bf16 a[180:183], v[92:95], v[72:75], a[180:183]\n" \
"v_mfma_f32_16x16x32_bf16 v[144:147], v[92:95], v[76:79], v[144:147]\n" \
"v_mfma_f32_16x16x32_bf16 v[184:187], v[92:95], v[80:83], v[184:187]\n" \
"v_mfma_f32_16x16x32_bf16 v[224:227], v[92:95], v[84:87], v[224:227]\n" \
"v_mfma_f32_16x16x32_bf16 a[216:219], a[196:199], v[92:95], a[216:219]\n" \
"s_waitcnt lgkmcnt(4)\n" \
"v_mfma_f32_16x16x32_bf16 a[24:27], v[96:99], v[56:59], a[24:27]\n" \
"ds_read_b64_tr_b16 v[108:109], v9 offset:0\n" \
"v_mfma_f32_16x16x32_bf16 a[64:67], v[96:99], v[60:63], a[64:67]\n" \
"s_cmp_eq_u32 s53, 0\n" \
"s_cbranch_scc1 LZ%=_103\n" \
"global_load_lds_dwordx4 v17, s[46:47] offset:1024\n" \
"LZ%=_103:\n" \
"v_mfma_f32_16x16x32_bf16 a[104:107], v[96:99], v[64:67], a[104:107]\n" \
"s_add_u32 s46, s46, s48\n" \
"s_addc_u32 s47, s47, 0\n" \
"v_mfma_f32_16x16x32_bf16 a[144:147], v[96:99], v[68:71], a[144:147]\n" \
"ds_read_b64_tr_b16 v[110:111], v9 offset:768\n" \
"v_mfma_f32_16x16x32_bf16 a[184:187], v[96:99], v[72:75], a[184:187]\n" \
"v_mfma_f32_16x16x32_bf16 v[148:151], v[96:99], v[76:79], v[148:151]\n" \
"v_mfma_f32_16x16x32_bf16 v[188:191], v[96:99], v[80:83], v[188:191]\n" \
"v_mfma_f32_16x16x32_bf16 v[228:231], v[96:99], v[84:87], v[228:231]\n" \
"v_mfma_f32_16x16x32_bf16 a[216:219], a[200:203], v[96:99], a[216:219]\n" \
"s_waitcnt lgkmcnt(0)\n" \
"s_waitcnt vmcnt(18)\n" \
"s_barrier\n" \
"v_mfma_f32_16x16x32_bf16 a[28:31], v[100:103], v[56:59], a[28:31]\n" \
"v_add_u32 v1, 39936, v1\n" \
"ds_read_b64_tr_b16 v[92:93], v1 offset:0\n" \
"v_mfma_f32_16x16x32_bf16 a[68:71], v[100:103], v[60:63], a[68:71]\n" \
"ds_read_b64_tr_b16 v[94:95], v1 offset:1024\n" \
"v_mfma_f32_16x16x32_bf16 a[108:111], v[100:103], v[64:67], a[108:111]\n" \
"v_add_u32 v0, 39936, v0\n" \
"ds_read_b128 v[20:23], v0 offset:1024\n" \
"v_mfma_f32_16x16x32_bf16 a[148:151], v[100:103], v[68:71], a[148:151]\n" \
"v_add_u32 v4, 39936, v4\n" \
"ds_read_b128 v[24:27], v0 offset:2048\n" \
"v_mfma_f32_16x16x32_bf16 a[188:191], v[100:103], v[72:75], a[188:191]\n" \
"ds_read_b128 v[28:31], v0 offset:3072\n" \
"v_add_u32 v5, 39936, v5\n" \
"v_mfma_f32_16x16x32_bf16 v[152:155], v[100:103], v[76:79], v[152:155]\n" \
"ds_read_b128 v[32:35], v0 offset:4096\n" \
"v_mfma_f32_16x16x32_bf16 v[192:195], v[100:103], v[80:83], v[192:195]\n" \
"ds_read_b128 v[36:39], v0 offset:5120\n" \
"v_add_u32 v6, 39936, v6\n" \
"v_mfma_f32_16x16x32_bf16 v[232:235], v[100:103], v[84:87], v[232:235]\n" \
"ds_read_b128 v[40:43], v0 offset:6144\n" \
"v_mfma_f32_16x16x32_bf16 a[216:219], a[204:207], v[100:103], a[216:219]\n" \
"v_mfma_f32_16x16x32_bf16 a[32:35], v[104:107], v[56:59], a[32:35]\n" \
"ds_read_b128 v[44:47], v0 offset:7168\n" \
"v_add_u32 v7, 39936, v7\n" \
"v_mfma_f32_16x16x32_bf16 a[72:75], v[104:107], v[60:63], a[72:75]\n" \
"ds_read_b128 v[48:51], v0 offset:8192\n" \
"v_mfma_f32_16x16x32_bf16 a[112:115], v[104:107], v[64:67], a[112:115]\n" \
"v_add_u32 v18, 39936, v18\n" \
"ds_read_b128 v[52:55], v18 offset:0\n" \
"v_mfma_f32_16x16x32_bf16 a[152:155], v[104:107], v[68:71], a[152:155]\n" \
"v_add_u32 v8, 39936, v8\n" \
"v_add_u32 v2, 39936, v2\n" \
"v_mfma_f32_16x16x32_bf16 a[192:195], v[104:107], v[72:75], a[192:195]\n" \
"ds_read_b64_tr_b16 v[96:97], v2 offset:0\n" \
"ds_read_b64_tr_b16 v[98:99], v2 offset:1024\n" \
"v_mfma_f32_16x16x32_bf16 v[156:159], v[104:107], v[76:79], v[156:159]\n" \
"v_add_u32 v9, 39936, v9\n" \
"v_add_u32 v3, 39936, v3\n" \
"v_mfma_f32_16x16x32_bf16 v[196:199], v[104:107], v[80:83], v[196:199]\n" \
"ds_read_b64_tr_b16 v[100:101], v3 offset:0\n" \
"ds_read_b64_tr_b16 v[102:103], v3 offset:1024\n" \
"v_mfma_f32_16x16x32_bf16 v[236:239], v[104:107], v[84:87], v[236:239]\n" \
"v_mfma_f32_16x16x32_bf16 v[244:247], v[104:107], v[88:91], v[244:247]\n" \
"v_mfma_f32_16x16x32_bf16 a[216:219], a[208:211], v[104:107], a[216:219]\n" \
"v_mfma_f32_16x16x32_bf16 a[36:39], v[108:111], v[56:59], a[36:39]\n" \
"v_mfma_f32_16x16x32_bf16 a[76:79], v[108:111], v[60:63], a[76:79]\n" \
"v_mfma_f32_16x16x32_bf16 a[116:119], v[108:111], v[64:67], a[116:119]\n" \
"v_mfma_f32_16x16x32_bf16 a[156:159], v[108:111], v[68:71], a[156:159]\n" \
"v_mfma_f32_16x16x32_bf16 v[120:123], v[108:111], v[72:75], v[120:123]\n" \
"v_mfma_f32_16x16x32_bf16 v[160:163], v[108:111], v[76:79], v[160:163]\n" \
"v_mfma_f32_16x16x32_bf16 v[200:203], v[108:111], v[80:83], v[200:203]\n" \
"v_mfma_f32_16x16x32_bf16 v[240:243], v[108:111], v[84:87], v[240:243]\n" \
"v_mfma_f32_16x16x32_bf16 a[216:219], a[212:215], v[108:111], a[216:219]\n" \
"s_waitcnt lgkmcnt(9)\n" \
"v_mfma_f32_16x16x32_bf16 a[0:3], v[92:95], v[20:23], a[0:3]\n" \
"ds_read_b64_tr_b16 v[104:105], v4 offset:0\n" \
"v_mfma_f32_16x16x32_bf16 a[40:43], v[92:95], v[24:27], a[40:43]\n" \
"v_mfma_f32_16x16x32_bf16 a[80:83], v[92:95], v[28:31], a[80:83]\n" \
"ds_read_b64_tr_b16 v[106:107], v5 offset:0\n" \
"s_add_u32 m0, s49, 39936\n" \
"v_mfma_f32_16x16x32_bf16 a[120:123], v[92:95], v[32:35], a[120:123]\n" \
"global_load_lds_dwordx4 v10, s[40:41] offset:0\n" \
"s_waitcnt lgkmcnt(7)\n" \
"v_mfma_f32_16x16x32_bf16 a[160:163], v[92:95], v[36:39], a[160:163]\n" \
"v_mfma_f32_16x16x32_bf16 v[124:127], v[92:95], v[40:43], v[124:127]\n" \
"v_mfma_f32_16x16x32_bf16 v[164:167], v[92:95], v[44:47], v[164:167]\n" \
"v_mfma_f32_16x16x32_bf16 v[204:207], v[92:95], v[48:51], v[204:207]\n" \
"s_waitcnt lgkmcnt(4)\n" \
"v_mfma_f32_16x16x32_bf16 a[4:7], v[96:99], v[20:23], a[4:7]\n" \
"ds_read_b64_tr_b16 v[108:109], v1 offset:8192\n" \
"global_load_lds_dwordx4 v11, s[40:41] offset:1024\n" \
"v_mfma_f32_16x16x32_bf16 a[44:47], v[96:99], v[24:27], a[44:47]\n" \
"v_mfma_f32_16x16x32_bf16 a[84:87], v[96:99], v[28:31], a[84:87]\n" \
"ds_read_b64_tr_b16 v[110:111], v1 offset:9216\n" \
"v_mfma_f32_16x16x32_bf16 a[124:127], v[96:99], v[32:35], a[124:127]\n" \
"v_mfma_f32_16x16x32_bf16 a[164:167], v[96:99], v[36:39], a[164:167]\n" \
"v_mfma_f32_16x16x32_bf16 v[128:131], v[96:99], v[40:43], v[128:131]\n" \
"global_load_lds_dwordx4 v12, s[40:41] offset:2048\n" \
"v_mfma_f32_16x16x32_bf16 v[168:171], v[96:99], v[44:47], v[168:171]\n" \
"v_mfma_f32_16x16x32_bf16 v[208:211], v[96:99], v[48:51], v[208:211]\n" \
"s_waitcnt lgkmcnt(4)\n" \
"v_mfma_f32_16x16x32_bf16 a[8:11], v[100:103], v[20:23], a[8:11]\n" \
"ds_read_b64_tr_b16 v[92:93], v2 offset:8192\n" \
"v_mfma_f32_16x16x32_bf16 a[48:51], v[100:103], v[24:27], a[48:51]\n" \
"v_mfma_f32_16x16x32_bf16 a[88:91], v[100:103], v[28:31], a[88:91]\n" \
"ds_read_b64_tr_b16 v[94:95], v2 offset:9216\n" \
"global_load_lds_dwordx4 v13, s[40:41] offset:3072\n" \
"v_mfma_f32_16x16x32_bf16 a[128:131], v[100:103], v[32:35], a[128:131]\n" \
"s_add_u32 s40, s40, 64\n" \
"s_addc_u32 s41, s41, 0\n" \
"v_mfma_f32_16x16x32_bf16 a[168:171], v[100:103], v[36:39], a[168:171]\n" \
"v_mfma_f32_16x16x32_bf16 v[132:135], v[100:103], v[40:43], v[132:135]\n" \
"v_mfma_f32_16x16x32_bf16 v[172:175], v[100:103], v[44:47], v[172:175]\n" \
"s_add_u32 m0, s50, 39936\n" \
"v_mfma_f32_16x16x32_bf16 v[212:215], v[100:103], v[48:51], v[212:215]\n" \
"global_load_lds_dwordx4 v14, s[42:43] offset:0\n" \
"s_waitcnt lgkmcnt(4)\n" \
"v_mfma_f32_16x16x32_bf16 a[12:15], v[104:107], v[20:23], a[12:15]\n" \
"ds_read_b64_tr_b16 v[96:97], v3 offset:8192\n" \
"v_mfma_f32_16x16x32_bf16 a[52:55], v[104:107], v[24:27], a[52:55]\n" \
"v_mfma_f32_16x16x32_bf16 a[92:95], v[104:107], v[28:31], a[92:95]\n" \
"ds_read_b64_tr_b16 v[98:99], v3 offset:9216\n" \
"v_mfma_f32_16x16x32_bf16 a[132:135], v[104:107], v[32:35], a[132:135]\n" \
"v_mfma_f32_16x16x32_bf16 a[172:175], v[104:107], v[36:39], a[172:175]\n" \
"global_load_lds_dwordx4 v15, s[42:43] offset:1024\n" \
"v_mfma_f32_16x16x32_bf16 v[136:139], v[104:107], v[40:43], v[136:139]\n" \
"s_add_u32 s42, s42, 229376\n" \
"s_addc_u32 s43, s43, 0\n" \
"v_mfma_f32_16x16x32_bf16 v[176:179], v[104:107], v[44:47], v[176:179]\n" \
"v_mfma_f32_16x16x32_bf16 v[216:219], v[104:107], v[48:51], v[216:219]\n" \
"s_waitcnt lgkmcnt(4)\n" \
"v_mfma_f32_16x16x32_bf16 a[16:19], v[108:111], v[20:23], a[16:19]\n" \
"ds_read_b64_tr_b16 v[100:101], v6 offset:0\n" \
"s_add_u32 m0, s51, 39936\n" \
"v_mfma_f32_16x16x32_bf16 a[56:59], v[108:111], v[24:27], a[56:59]\n" \
"global_load_lds_dwordx4 v14, s[44:45] offset:0\n" \
"v_mfma_f32_16x16x32_bf16 a[96:99], v[108:111], v[28:31], a[96:99]\n" \
"ds_read_b64_tr_b16 v[102:103], v7 offset:0\n" \
"v_mfma_f32_16x16x32_bf16 a[136:139], v[108:111], v[32:35], a[136:139]\n" \
"v_mfma_f32_16x16x32_bf16 a[176:179], v[108:111], v[36:39], a[176:179]\n" \
"v_mfma_f32_16x16x32_bf16 v[140:143], v[108:111], v[40:43], v[140:143]\n" \
"v_mfma_f32_16x16x32_bf16 v[180:183], v[108:111], v[44:47], v[180:183]\n" \
"global_load_lds_dwordx4 v15, s[44:45] offset:1024\n" \
"v_mfma_f32_16x16x32_bf16 v[220:223], v[108:111], v[48:51], v[220:223]\n" \
"s_add_u32 s44, s44, 229376\n" \
"s_addc_u32 s45, s45, 0\n" \
"s_waitcnt lgkmcnt(4)\n" \
"v_mfma_f32_16x16x32_bf16 a[20:23], v[92:95], v[20:23], a[20:23]\n" \
"ds_read_b64_tr_b16 v[104:105], v8 offset:0\n" \
"v_mfma_f32_16x16x32_bf16 a[60:63], v[92:95], v[24:27], a[60:63]\n" \
"v_mfma_f32_16x16x32_bf16 a[100:103], v[92:95], v[28:31], a[100:103]\n" \
"ds_read_b64_tr_b16 v[106:107], v8 offset:768\n" \
"s_add_u32 m0, s52, 39936\n" \
"v_mfma_f32_16x16x32_bf16 a[140:143], v[92:95], v[32:35], a[140:143]\n" \
"global_load_lds_dwordx4 v16, s[46:47]\n" \
"v_mfma_f32_16x16x32_bf16 a[180:183], v[92:95], v[36:39], a[180:183]\n" \
"v_mfma_f32_16x16x32_bf16 v[144:147], v[92:95], v[40:43], v[144:147]\n" \
"v_mfma_f32_16x16x32_bf16 v[184:187], v[92:95], v[44:47], v[184:187]\n" \
"v_mfma_f32_16x16x32_bf16 v[224:227], v[92:95], v[48:51], v[224:227]\n" \
"v_mfma_f32_16x16x32_bf16 a[216:219], a[196:199], v[92:95], a[216:219]\n" \
"s_waitcnt lgkmcnt(4)\n" \
"v_mfma_f32_16x16x32_bf16 a[24:27], v[96:99], v[20:23], a[24:27]\n" \
"ds_read_b64_tr_b16 v[108:109], v9 offset:0\n" \
"v_mfma_f32_16x16x32_bf16 a[64:67], v[96:99], v[24:27], a[64:67]\n" \
"s_cmp_eq_u32 s53, 0\n" \
"s_cbranch_scc1 LZ%=_114\n" \
"global_load_lds_dwordx4 v17, s[46:47] offset:1024\n" \
"LZ%=_114:\n" \
"v_mfma_f32_16x16x32_bf16 a[104:107], v[96:99], v[28:31], a[104:107]\n" \
"s_add_u32 s46, s46, s48\n" \
"s_addc_u32 s47, s47, 0\n" \
"v_mfma_f32_16x16x32_bf16 a[144:147], v[96:99], v[32:35], a[144:147]\n" \
"ds_read_b64_tr_b16 v[110:111], v9 offset:768\n" \
"v_mfma_f32_16x16x32_bf16 a[184:187], v[96:99], v[36:39], a[184:187]\n" \
"v_mfma_f32_16x16x32_bf16 v[148:151], v[96:99], v[40:43], v[148:151]\n" \
"v_mfma_f32_16x16x32_bf16 v[188:191], v[96:99], v[44:47], v[188:191]\n" \
"v_mfma_f32_16x16x32_bf16 v[228:231], v[96:99], v[48:51], v[228:231]\n" \
"v_mfma_f32_16x16x32_bf16 a[216:219], a[200:203], v[96:99], a[216:219]\n" \
"s_waitcnt lgkmcnt(0)\n" \
"s_waitcnt vmcnt(18)\n" \
"s_barrier\n" \
"v_mfma_f32_16x16x32_bf16 a[28:31], v[100:103], v[20:23], a[28:31]\n" \
"v_add_u32 v1, 39936, v1\n" \
"ds_read_b64_tr_b16 v[92:93], v1 offset:0\n" \
"v_mfma_f32_16x16x32_bf16 a[68:71], v[100:103], v[24:27], a[68:71]\n" \
"ds_read_b64_tr_b16 v[94:95], v1 offset:1024\n" \
"v_mfma_f32_16x16x32_bf16 a[108:111], v[100:103], v[28:31], a[108:111]\n" \
"v_add_u32 v0, 39936, v0\n" \
"ds_read_b128 v[56:59], v0 offset:1024\n" \
"v_mfma_f32_16x16x32_bf16 a[148:151], v[100:103], v[32:35], a[148:151]\n" \
"v_add_u32 v4, 39936, v4\n" \
"ds_read_b128 v[60:63], v0 offset:2048\n" \
"v_mfma_f32_16x16x32_bf16 a[188:191], v[100:103], v[36:39], a[188:191]\n" \
"s_add_u32 s88, s78, 16384\n" \
"ds_read_b128 v[64:67], v0 offset:3072\n" \
"v_mfma_f32_16x16x32_bf16 v[152:155], v[100:103], v[40:43], v[152:155]\n" \
"v_add_u32 v5, 39936, v5\n" \
"ds_read_b128 v[68:71], v0 offset:4096\n" \
"v_mfma_f32_16x16x32_bf16 v[192:195], v[100:103], v[44:47], v[192:195]\n" \
"s_cmp_eq_u32 s87, 0\n" \
"s_cselect_b32 s88, s78, s88\n" \
"v_mfma_f32_16x16x32_bf16 v[232:235], v[100:103], v[48:51], v[232:235]\n" \
"ds_read_b128 v[72:75], v0 offset:5120\n" \
"v_mfma_f32_16x16x32_bf16 a[216:219], a[204:207], v[100:103], a[216:219]\n" \
"v_mfma_f32_16x16x32_bf16 a[32:35], v[104:107], v[20:23], a[32:35]\n" \
"v_add_u32 v6, 39936, v6\n" \
"ds_read_b128 v[76:79], v0 offset:6144\n" \
"v_mfma_f32_16x16x32_bf16 a[72:75], v[104:107], v[24:27], a[72:75]\n" \
"s_bitcmp1_b32 s54, s87\n" \
"s_cselect_b32 s88, s88, s92\n" \
"v_mfma_f32_16x16x32_bf16 a[112:115], v[104:107], v[28:31], a[112:115]\n" \
"ds_read_b128 v[80:83], v0 offset:7168\n" \
"v_add_u32 v7, 39936, v7\n" \
"v_mfma_f32_16x16x32_bf16 a[152:155], v[104:107], v[32:35], a[152:155]\n" \
"ds_read_b128 v[84:87], v0 offset:8192\n" \
"v_mov_b32 v116, s88\n" \
"v_mfma_f32_16x16x32_bf16 a[192:195], v[104:107], v[36:39], a[192:195]\n" \
"v_add_u32 v18, 39936, v18\n" \
"ds_read_b128 v[88:91], v18 offset:0\n" \
"v_mfma_f32_16x16x32_bf16 v[156:159], v[104:107], v[40:43], v[156:159]\n" \
"v_add_u32 v8, 39936, v8\n" \
"v_add_u32 v2, 39936, v2\n" \
"v_mfma_f32_16x16x32_bf16 v[196:199], v[104:107], v[44:47], v[196:199]\n" \
"ds_read_b64_tr_b16 v[96:97], v2 offset:0\n" \
"v_mov_b32 v118, s84\n" \
"v_mfma_f32_16x16x32_bf16 v[236:239], v[104:107], v[48:51], v[236:239]\n" \
"ds_read_b64_tr_b16 v[98:99], v2 offset:1024\n" \
"v_add_u32 v9, 39936, v9\n" \
"v_mfma_f32_16x16x32_bf16 v[244:247], v[104:107], v[52:55], v[244:247]\n" \
"v_add_u32 v3, 39936, v3\n" \
"v_mfma_f32_16x16x32_bf16 a[216:219], a[208:211], v[104:107], a[216:219]\n" \
"v_mfma_f32_16x16x32_bf16 a[36:39], v[108:111], v[20:23], a[36:39]\n" \
"ds_read_b64_tr_b16 v[100:101], v3 offset:0\n" \
"v_mov_b32 v119, s85\n" \
"v_mfma_f32_16x16x32_bf16 a[76:79], v[108:111], v[24:27], a[76:79]\n" \
"ds_read_b64_tr_b16 v[102:103], v3 offset:1024\n" \
"global_store_dwordx2 v116, v[118:119], s[74:75] sc1\n" \
"v_mfma_f32_16x16x32_bf16 a[116:119], v[108:111], v[28:31], a[116:119]\n" \
"v_mfma_f32_16x16x32_bf16 a[156:159], v[108:111], v[32:35], a[156:159]\n" \
"v_mfma_f32_16x16x32_bf16 v[120:123], v[108:111], v[36:39], v[120:123]\n" \
"v_mfma_f32_16x16x32_bf16 v[160:163], v[108:111], v[40:43], v[160:163]\n" \
"v_mfma_f32_16x16x32_bf16 v[200:203], v[108:111], v[44:47], v[200:203]\n" \
"v_mfma_f32_16x16x32_bf16 v[240:243], v[108:111], v[48:51], v[240:243]\n" \
"v_mfma_f32_16x16x32_bf16 a[216:219], a[212:215], v[108:111], a[216:219]\n" \
"s_waitcnt lgkmcnt(9)\n" \
"v_mfma_f32_16x16x32_bf16 a[0:3], v[92:95], v[56:59], a[0:3]\n" \
"ds_read_b64_tr_b16 v[104:105], v4 offset:0\n" \
"v_mfma_f32_16x16x32_bf16 a[40:43], v[92:95], v[60:63], a[40:43]\n" \
"v_mfma_f32_16x16x32_bf16 a[80:83], v[92:95], v[64:67], a[80:83]\n" \
"ds_read_b64_tr_b16 v[106:107], v5 offset:0\n" \
"s_add_u32 m0, s49, 79872\n" \
"v_mfma_f32_16x16x32_bf16 a[120:123], v[92:95], v[68:71], a[120:123]\n" \
"global_load_lds_dwordx4 v10, s[40:41] offset:0\n" \
"s_waitcnt lgkmcnt(7)\n" \
"v_mfma_f32_16x16x32_bf16 a[160:163], v[92:95], v[72:75], a[160:163]\n" \
"v_mfma_f32_16x16x32_bf16 v[124:127], v[92:95], v[76:79], v[124:127]\n" \
"v_mfma_f32_16x16x32_bf16 v[164:167], v[92:95], v[80:83], v[164:167]\n" \
"v_mfma_f32_16x16x32_bf16 v[204:207], v[92:95], v[84:87], v[204:207]\n" \
"s_waitcnt lgkmcnt(4)\n" \
"v_mfma_f32_16x16x32_bf16 a[4:7], v[96:99], v[56:59], a[4:7]\n" \
"ds_read_b64_tr_b16 v[108:109], v1 offset:8192\n" \
"global_load_lds_dwordx4 v11, s[40:41] offset:1024\n" \
"v_mfma_f32_16x16x32_bf16 a[44:47], v[96:99], v[60:63], a[44:47]\n" \
"v_mfma_f32_16x16x32_bf16 a[84:87], v[96:99], v[64:67], a[84:87]\n" \
"ds_read_b64_tr_b16 v[110:111], v1 offset:9216\n" \
"v_mfma_f32_16x16x32_bf16 a[124:127], v[96:99], v[68:71], a[124:127]\n" \
"v_mfma_f32_16x16x32_bf16 a[164:167], v[96:99], v[72:75], a[164:167]\n" \
"v_mfma_f32_16x16x32_bf16 v[128:131], v[96:99], v[76:79], v[128:131]\n" \
"global_load_lds_dwordx4 v12, s[40:41] offset:2048\n" \
"v_mfma_f32_16x16x32_bf16 v[168:171], v[96:99], v[80:83], v[168:171]\n" \
"v_mfma_f32_16x16x32_bf16 v[208:211], v[96:99], v[84:87], v[208:211]\n" \
"s_waitcnt lgkmcnt(4)\n" \
"v_mfma_f32_16x16x32_bf16 a[8:11], v[100:103], v[56:59], a[8:11]\n" \
"ds_read_b64_tr_b16 v[92:93], v2 offset:8192\n" \
"v_mfma_f32_16x16x32_bf16 a[48:51], v[100:103], v[60:63], a[48:51]\n" \
"v_mfma_f32_16x16x32_bf16 a[88:91], v[100:103], v[64:67], a[88:91]\n" \
"ds_read_b64_tr_b16 v[94:95], v2 offset:9216\n" \
"global_load_lds_dwordx4 v13, s[40:41] offset:3072\n" \
"v_mfma_f32_16x16x32_bf16 a[128:131], v[100:103], v[68:71], a[128:131]\n" \
"s_add_u32 s40, s40, 64\n" \
"s_addc_u32 s41, s41, 0\n" \
"v_mfma_f32_16x16x32_bf16 a[168:171], v[100:103], v[72:75], a[168:171]\n" \
"v_mfma_f32_16x16x32_bf16 v[132:135], v[100:103], v[76:79], v[132:135]\n" \
"v_mfma_f32_16x16x32_bf16 v[172:175], v[100:103], v[80:83], v[172:175]\n" \
"s_add_u32 m0, s50, 79872\n" \
"v_mfma_f32_16x16x32_bf16 v[212:215], v[100:103], v[84:87], v[212:215]\n" \
"global_load_lds_dwordx4 v14, s[42:43] offset:0\n" \
"s_waitcnt lgkmcnt(4)\n" \
"v_mfma_f32_16x16x32_bf16 a[12:15], v[104:107], v[56:59], a[12:15]\n" \
"ds_read_b64_tr_b16 v[96:97], v3 offset:8192\n" \
"v_mfma_f32_16x16x32_bf16 a[52:55], v[104:107], v[60:63], a[52:55]\n" \
"v_mfma_f32_16x16x32_bf16 a[92:95], v[104:107], v[64:67], a[92:95]\n" \
"ds_read_b64_tr_b16 v[98:99], v3 offset:9216\n" \
"v_mfma_f32_16x16x32_bf16 a[132:135], v[104:107], v[68:71], a[132:135]\n" \
"v_mfma_f32_16x16x32_bf16 a[172:175], v[104:107], v[72:75], a[172:175]\n" \
"global_load_lds_dwordx4 v15, s[42:43] offset:1024\n" \
"v_mfma_f32_16x16x32_bf16 v[136:139], v[104:107], v[76:79], v[136:139]\n" \
"s_add_u32 s42, s42, 229376\n" \
"s_addc_u32 s43, s43, 0\n" \
"v_mfma_f32_16x16x32_bf16 v[176:179], v[104:107], v[80:83], v[176:179]\n" \
"v_mfma_f32_16x16x32_bf16 v[216:219], v[104:107], v[84:87], v[216:219]\n" \
"s_waitcnt lgkmcnt(4)\n" \
"v_mfma_f32_16x16x32_bf16 a[16:19], v[108:111], v[56:59], a[16:19]\n" \
"ds_read_b64_tr_b16 v[100:101], v6 offset:0\n" \
"s_add_u32 m0, s51, 79872\n" \
"v_mfma_f32_16x16x32_bf16 a[56:59], v[108:111], v[60:63], a[56:59]\n" \
"global_load_lds_dwordx4 v14, s[44:45] offset:0\n" \
"v_mfma_f32_16x16x32_bf16 a[96:99], v[108:111], v[64:67], a[96:99]\n" \
"ds_read_b64_tr_b16 v[102:103], v7 offset:0\n" \
"v_mfma_f32_16x16x32_bf16 a[136:139], v[108:111], v[68:71], a[136:139]\n" \
"v_mfma_f32_16x16x32_bf16 a[176:179], v[108:111], v[72:75], a[176:179]\n" \
"v_mfma_f32_16x16x32_bf16 v[140:143], v[108:111], v[76:79], v[140:143]\n" \
"v_mfma_f32_16x16x32_bf16 v[180:183], v[108:111], v[80:83], v[180:183]\n" \
"global_load_lds_dwordx4 v15, s[44:45] offset:1024\n" \
"v_mfma_f32_16x16x32_bf16 v[220:223], v[108:111], v[84:87], v[220:223]\n" \
"s_add_u32 s44, s44, 229376\n" \
"s_addc_u32 s45, s45, 0\n" \
"s_waitcnt lgkmcnt(4)\n" \
"v_mfma_f32_16x16x32_bf16 a[20:23], v[92:95], v[56:59], a[20:23]\n" \
"ds_read_b64_tr_b16 v[104:105], v8 offset:0\n" \
"v_mfma_f32_16x16x32_bf16 a[60:63], v[92:95], v[60:63], a[60:63]\n" \
"v_mfma_f32_16x16x32_bf16 a[100:103], v[92:95], v[64:67], a[100:103]\n" \
"ds_read_b64_tr_b16 v[106:107], v8 offset:768\n" \
"s_add_u32 m0, s52, 79872\n" \
"v_mfma_f32_16x16x32_bf16 a[140:143], v[92:95], v[68:71], a[140:143]\n" \
"global_load_lds_dwordx4 v16, s[46:47]\n" \
"v_mfma_f32_16x16x32_bf16 a[180:183], v[92:95], v[72:75], a[180:183]\n" \
"v_mfma_f32_16x16x32_bf16 v[144:147], v[92:95], v[76:79], v[144:147]\n" \
"v_mfma_f32_16x16x32_bf16 v[184:187], v[92:95], v[80:83], v[184:187]\n" \
"v_mfma_f32_16x16x32_bf16 v[224:227], v[92:95], v[84:87], v[224:227]\n" \
"v_mfma_f32_16x16x32_bf16 a[216:219], a[196:199], v[92:95], a[216:219]\n" \
"s_waitcnt lgkmcnt(4)\n" \
"v_mfma_f32_16x16x32_bf16 a[24:27], v[96:99], v[56:59], a[24:27]\n" \
"ds_read_b64_tr_b16 v[108:109], v9 offset:0\n" \
"v_mfma_f32_16x16x32_bf16 a[64:67], v[96:99], v[60:63], a[64:67]\n" \
"s_cmp_eq_u32 s53, 0\n" \
"s_cbranch_scc1 LZ%=_124\n" \
"global_load_lds_dwordx4 v17, s[46:47] offset:1024\n" \
"LZ%=_124:\n" \
"v_mfma_f32_16x16x32_bf16 a[104:107], v[96:99], v[64:67], a[104:107]\n" \
"s_add_u32 s46, s46, s48\n" \
"s_addc_u32 s47, s47, 0\n" \
"v_mfma_f32_16x16x32_bf16 a[144:147], v[96:99], v[68:71], a[144:147]\n" \
"ds_read_b64_tr_b16 v[110:111], v9 offset:768\n" \
"v_mfma_f32_16x16x32_bf16 a[184:187], v[96:99], v[72:75], a[184:187]\n" \
"v_mfma_f32_16x16x32_bf16 v[148:151], v[96:99], v[76:79], v[148:151]\n" \
"v_mfma_f32_16x16x32_bf16 v[188:191], v[96:99], v[80:83], v[188:191]\n" \
"v_mfma_f32_16x16x32_bf16 v[228:231], v[96:99], v[84:87], v[228:231]\n" \
"v_mfma_f32_16x16x32_bf16 a[216:219], a[200:203], v[96:99], a[216:219]\n" \
"s_waitcnt lgkmcnt(0)\n" \
"s_waitcnt vmcnt(18)\n" \
"s_barrier\n" \
"v_mfma_f32_16x16x32_bf16 a[28:31], v[100:103], v[56:59], a[28:31]\n" \
"v_add_u32 v1, 4294847488, v1\n" \
"ds_read_b64_tr_b16 v[92:93], v1 offset:0\n" \
"v_mfma_f32_16x16x32_bf16 a[68:71], v[100:103], v[60:63], a[68:71]\n" \
"ds_read_b64_tr_b16 v[94:95], v1 offset:1024\n" \
"v_mfma_f32_16x16x32_bf16 a[108:111], v[100:103], v[64:67], a[108:111]\n" \
"v_add_u32 v0, 4294847488, v0\n" \
"ds_read_b128 v[20:23], v0 offset:1024\n" \
"v_mfma_f32_16x16x32_bf16 a[148:151], v[100:103], v[68:71], a[148:151]\n" \
"v_add_u32 v4, 4294847488, v4\n" \
"ds_read_b128 v[24:27], v0 offset:2048\n" \
"v_mfma_f32_16x16x32_bf16 a[188:191], v[100:103], v[72:75], a[188:191]\n" \
"ds_read_b128 v[28:31], v0 offset:3072\n" \
"v_add_u32 v5, 4294847488, v5\n" \
"v_mfma_f32_16x16x32_bf16 v[152:155], v[100:103], v[76:79], v[152:155]\n" \
"ds_read_b128 v[32:35], v0 offset:4096\n" \
"v_mfma_f32_16x16x32_bf16 v[192:195], v[100:103], v[80:83], v[192:195]\n" \
"ds_read_b128 v[36:39], v0 offset:5120\n" \
"v_add_u32 v6, 4294847488, v6\n" \
"v_mfma_f32_16x16x32_bf16 v[232:235], v[100:103], v[84:87], v[232:235]\n" \
"ds_read_b128 v[40:43], v0 offset:6144\n" \
"v_mfma_f32_16x16x32_bf16 a[216:219], a[204:207], v[100:103], a[216:219]\n" \
"v_mfma_f32_16x16x32_bf16 a[32:35], v[104:107], v[56:59], a[32:35]\n" \
"ds_read_b128 v[44:47], v0 offset:7168\n" \
"v_add_u32 v7, 4294847488, v7\n" \
"v_mfma_f32_16x16x32_bf16 a[72:75], v[104:107], v[60:63], a[72:75]\n" \
"ds_read_b128 v[48:51], v0 offset:8192\n" \
"v_mfma_f32_16x16x32_bf16 a[112:115], v[104:107], v[64:67], a[112:115]\n" \
"v_add_u32 v18, 4294847488, v18\n" \
"ds_read_b128 v[52:55], v18 offset:0\n" \
"v_mfma_f32_16x16x32_bf16 a[152:155], v[104:107], v[68:71], a[152:155]\n" \
"v_add_u32 v8, 4294847488, v8\n" \
"v_add_u32 v2, 4294847488, v2\n" \
"v_mfma_f32_16x16x32_bf16 a[192:195], v[104:107], v[72:75], a[192:195]\n" \
"ds_read_b64_tr_b16 v[96:97], v2 offset:0\n" \
"ds_read_b64_tr_b16 v[98:99], v2 offset:1024\n" \
"v_mfma_f32_16x16x32_bf16 v[156:159], v[104:107], v[76:79], v[156:159]\n" \
"v_add_u32 v9, 4294847488, v9\n" \
"v_add_u32 v3, 4294847488, v3\n" \
"v_mfma_f32_16x16x32_bf16 v[196:199], v[104:107], v[80:83], v[196:199]\n" \
"ds_read_b64_tr_b16 v[100:101], v3 offset:0\n" \
"ds_read_b64_tr_b16 v[102:103], v3 offset:1024\n" \
"v_mfma_f32_16x16x32_bf16 v[236:239], v[104:107], v[84:87], v[236:239]\n" \
"v_mfma_f32_16x16x32_bf16 v[244:247], v[104:107], v[88:91], v[244:247]\n" \
"v_mfma_f32_16x16x32_bf16 a[216:219], a[208:211], v[104:107], a[216:219]\n" \
"v_mfma_f32_16x16x32_bf16 a[36:39], v[108:111], v[56:59], a[36:39]\n" \
"v_mfma_f32_16x16x32_bf16 a[76:79], v[108:111], v[60:63], a[76:79]\n" \
"v_mfma_f32_16x16x32_bf16 a[116:119], v[108:111], v[64:67], a[116:119]\n" \
"v_mfma_f32_16x16x32_bf16 a[156:159], v[108:111], v[68:71], a[156:159]\n" \
"v_mfma_f32_16x16x32_bf16 v[120:123], v[108:111], v[72:75], v[120:123]\n" \
"v_mfma_f32_16x16x32_bf16 v[160:163], v[108:111], v[76:79], v[160:163]\n" \
"v_mfma_f32_16x16x32_bf16 v[200:203], v[108:111], v[80:83], v[200:203]\n" \
"v_mfma_f32_16x16x32_bf16 v[240:243], v[108:111], v[84:87], v[240:243]\n" \
"v_mfma_f32_16x16x32_bf16 a[216:219], a[212:215], v[108:111], a[216:219]\n" \
"s_branch LKE%=\n" \
"LWR%=_52:\n" \
"s_sub_u32 s40, s40, 2048\n" \
"s_subb_u32 s41, s41, 0\n" \
"s_sub_u32 s42, s42, 7340032\n" \
"s_subb_u32 s43, s43, 0\n" \
"s_sub_u32 s44, s44, 7340032\n" \
"s_subb_u32 s45, s45, 0\n" \
"s_sub_u32 s46, s46, s90\n" \
"s_subb_u32 s47, s47, 0\n" \
"s_branch LWS%=_52\n" \
"LWR%=_104:\n" \
"s_sub_u32 s40, s40, 2048\n" \
"s_subb_u32 s41, s41, 0\n" \
"s_sub_u32 s42, s42, 7340032\n" \
"s_subb_u32 s43, s43, 0\n" \
"s_sub_u32 s44, s44, 7340032\n" \
"s_subb_u32 s45, s45, 0\n" \
"s_sub_u32 s46, s46, s90\n" \
"s_subb_u32 s47, s47, 0\n" \
"s_branch LWS%=_104\n" \
"LWR%=_145:\n" \
"s_sub_u32 s40, s40, 2048\n" \
"s_subb_u32 s41, s41, 0\n" \
"s_sub_u32 s42, s42, 7340032\n" \
"s_subb_u32 s43, s43, 0\n" \
"s_sub_u32 s44, s44, 7340032\n" \
"s_subb_u32 s45, s45, 0\n" \
"s_sub_u32 s46, s46, s90\n" \
"s_subb_u32 s47, s47, 0\n" \
"s_branch LWS%=_145\n" \
"LEND%=:\n"
#define K1_ASM_CLOB "v36", "v37", "v38", "v39", "v40", "v41", "v42", "v43", "v44", "v45", "v46", "v47", "v48", "v49", "v50", "v51", "v52", "v53", "v54", "v55", "v56", "v57", "v58", "v59", "v60", "v61", "v62", "v63", "v64", "v65", "v66", "v67", "v68", "v69", "v70", "v71", "v72", "v73", "v74", "v75", "v76", "v77", "v78", "v79", "v80", "v81", "v82", "v83", "v84", "v85", "v86", "v87", "v88", "v89", "v90", "v91", "v92", "v93", "v94", "v95", "v96", "v97", "v98", "v99", "v100", "v101", "v102", "v103", "v104", "v105", "v106", "v107", "v108", "v109", "v110", "v111", "v114", "v115", "v116", "v117", "v118", "v119", "v120", "v121", "v122", "v123", "v124", "v125", "v126", "v127", "v128", "v129", "v130", "v131", "v132", "v133", "v134", "v135", "v136", "v137", "v138", "v139", "v140", "v141", "v142", "v143", "v144", "v145", "v146", "v147", "v148", "v149", "v150", "v151", "v152", "v153", "v154", "v155", "v156", "v157", "v158", "v159", "v160", "v161", "v162", "v163", "v164", "v165", "v166", "v167", "v168", "v169", "v170", "v171", "v172", "v173", "v174", "v175", "v176", "v177", "v178", "v179", "v180", "v181", "v182", "v183", "v184", "v185", "v186", "v187", "v188", "v189", "v190", "v191", "v192", "v193", "v194", "v195", "v196", "v197", "v198", "v199", "v200", "v201", "v202", "v203", "v204", "v205", "v206", "v207", "v208", "v209", "v210", "v211", "v212", "v213", "v214", "v215", "v216", "v217", "v218", "v219", "v220", "v221", "v222", "v223", "v224", "v225", "v226", "v227", "v228", "v229", "v230", "v231", "v232", "v233", "v234", "v235", "v236", "v237", "v238", "v239", "v240", "v241", "v242", "v243", "v244", "v245", "v246", "v247", "a0", "a1", "a2", "a3", "a4", "a5", "a6", "a7", "a8", "a9", "a10", "a11", "a12", "a13", "a14", "a15", "a16", "a17", "a18", "a19", "a20", "a21", "a22", "a23", "a24", "a25", "a26", "a27", "a28", "a29", "a30", "a31", "a32", "a33", "a34", "a35", "a36", "a37", "a38", "a39", "a40", "a41", "a42", "a43", "a44", "a45", "a46", "a47", "a48", "a49", "a50", "a51", "a52", "a53", "a54", "a55", "a56", "a57", "a58", "a59", "a60", "a61", "a62", "a63", "a64", "a65", "a66", "a67", "a68", "a69", "a70", "a71", "a72", "a73", "a74", "a75", "a76", "a77", "a78", "a79", "a80", "a81", "a82", "a83", "a84", "a85", "a86", "a87", "a88", "a89", "a90", "a91", "a92", "a93", "a94", "a95", "a96", "a97", "a98", "a99", "a100", "a101", "a102", "a103", "a104", "a105", "a106", "a107", "a108", "a109", "a110", "a111", "a112", "a113", "a114", "a115", "a116", "a117", "a118", "a119", "a120", "a121", "a122", "a123", "a124", "a125", "a126", "a127", "a128", "a129", "a130", "a131", "a132", "a133", "a134", "a135", "a136", "a137", "a138", "a139", "a140", "a141", "a142", "a143", "a144", "a145", "a146", "a147", "a148", "a149", "a150", "a151", "a152", "a153", "a154", "a155", "a156", "a157", "a158", "a159", "a160", "a161", "a162", "a163", "a164", "a165", "a166", "a167", "a168", "a169", "a170", "a171", "a172", "a173", "a174", "a175", "a176", "a177", "a178", "a179", "a180", "a181", "a182", "a183", "a184", "a185", "a186", "a187", "a188", "a189", "a190", "a191", "a192", "a193", "a194", "a195", "a196", "a197", "a198", "a199", "a200", "a201", "a202", "a203", "a204", "a205", "a206", "a207", "a208", "a209", "a210", "a211", "a212", "a213", "a214", "a215", "a216", "a217", "a218", "a219", "a220", "a221", "a222", "a223", "a224", "a225", "a226", "a227", "a228", "a229", "a230", "a231", "a232", "a233", "a234", "a235", "a236", "a237", "a238", "a239", "a240", "a241", "a242", "a243", "a244", "a245", "a246", "a247", "a248", "a249", "a250", "a251", "a252", "a253", "a254", "a255", "s86", "s87", "s88", "s89", "s90", "s91", "s92", "s93", "s94", "s95", "s96", "s97", "vcc", "scc", "memory"
#define K1_ASM_CLOB_A "v36", "v37", "v38", "scc", "memory"

#define K1_STAMP1
#define K1_STAMP2
#define K1_STAMP3
#define CODE_END "k_code_end"

DI u32x2 pk4(const f32x4& v) { return (u32x2){cvtpk(v[0], v[1]), cvtpk(v[2], v[3])}; }
DI u32x2 hfrag(const f32x4& ga, const f32x4& ua) {
  float h0 = silu_f(ga[0]) * ua[0], h1 = silu_f(ga[1]) * ua[1], h2 = silu_f(ga[2]) * ua[2], h3 = silu_f(ga[3]) * ua[3];
  return (u32x2){cvtpk(h0, h1), cvtpk(h2, h3)};
}
DI u32x4 swap16(const u32x2& A, const u32x2& B) {
  auto s0 = __builtin_amdgcn_permlane16_swap(A[0], B[0], false, false); auto s1 = __builtin_amdgcn_permlane16_swap(A[1], B[1], false, false);
  return (u32x4){s0[0], s1[0], s0[1], s1[1]};
}
#define K2_ASM_BODY \
"v_mov_b32 v18, 0\n" \
"s_mov_b32 s51, 0\n" \
"s_mov_b32 s53, 4294836224\n" \
"s_waitcnt vmcnt(0) lgkmcnt(0)\n" \
"s_barrier\n" \
K2_STAMP0 \
"s_cmp_eq_u32 s65, 0\n" \
"s_cbranch_scc1 LW0%=\n" \
"v_mov_b32 v16, s68\n" \
"v_mov_b32 v17, s69\n" \
"v_mov_b32 v116, s64\n" \
"s_mov_b64 exec, 1\n" \
"global_store_dwordx2 v116, v[16:17], s[60:61] sc1\n" \
"s_mov_b64 exec, -1\n" \
"s_nop 1\n" \
"global_load_dwordx2 v[16:17], v19, s[60:61] sc1\n" \
"LW0%=:\n" \
"s_add_u32 m0, s50, 0\n" \
"s_nop 0\n" \
"global_load_lds_dwordx4 v11, s[42:43] offset:0\n" \
"global_load_lds_dwordx4 v12, s[42:43] offset:1024\n" \
"global_load_lds_dwordx4 v13, s[42:43] offset:2048\n" \
"global_load_lds_dwordx4 v14, s[42:43] offset:3072\n" \
"s_add_u32 s42, s42, 131072\n" \
"s_addc_u32 s43, s43, 0\n" \
"s_add_u32 m0, s50, 32768\n" \
"s_nop 0\n" \
"global_load_lds_dwordx4 v11, s[42:43] offset:0\n" \
"global_load_lds_dwordx4 v12, s[42:43] offset:1024\n" \
"global_load_lds_dwordx4 v13, s[42:43] offset:2048\n" \
"global_load_lds_dwordx4 v14, s[42:43] offset:3072\n" \
"s_add_u32 s42, s42, 131072\n" \
"s_addc_u32 s43, s43, 0\n" \
"s_add_u32 m0, s50, 65536\n" \
"s_nop 0\n" \
"global_load_lds_dwordx4 v11, s[42:43] offset:0\n" \
"global_load_lds_dwordx4 v12, s[42:43] offset:1024\n" \
"global_load_lds_dwordx4 v13, s[42:43] offset:2048\n" \
"global_load_lds_dwordx4 v14, s[42:43] offset:3072\n" \
"s_add_u32 s42, s42, 131072\n" \
"s_addc_u32 s43, s43, 0\n" \
"s_add_u32 m0, s50, 98304\n" \
"s_nop 0\n" \
"global_load_lds_dwordx4 v11, s[42:43] offset:0\n" \
"global_load_lds_dwordx4 v12, s[42:43] offset:1024\n" \
"global_load_lds_dwordx4 v13, s[42:43] offset:2048\n" \
"global_load_lds_dwordx4 v14, s[42:43] offset:3072\n" \
"s_add_u32 s42, s42, 131072\n" \
"s_addc_u32 s43, s43, 0\n" \
"global_load_dwordx2 v[212:213], v15, s[70:71] offset:0\n" \
"global_load_dwordx2 v[214:215], v15, s[70:71] offset:32\n" \
"global_load_dwordx2 v[216:217], v15, s[70:71] offset:64\n" \
"global_load_dwordx2 v[218:219], v15, s[70:71] offset:96\n" \
"v_add_u32 v15, 32768, v15\n" \
"global_load_dwordx2 v[220:221], v15, s[70:71] offset:0\n" \
"global_load_dwordx2 v[222:223], v15, s[70:71] offset:32\n" \
"global_load_dwordx2 v[224:225], v15, s[70:71] offset:64\n" \
"global_load_dwordx2 v[226:227], v15, s[70:71] offset:96\n" \
"v_mov_b32 v164, 0\n" \
"v_mov_b32 v165, 0\n" \
"v_mov_b32 v166, 0\n" \
"v_mov_b32 v167, 0\n" \
"s_nop 1\n" \
"v_mfma_f32_16x16x32_bf16 v[20:23], v[164:167], v[164:167], 0\n" \
"v_mfma_f32_16x16x32_bf16 v[24:27], v[164:167], v[164:167], 0\n" \
"v_mfma_f32_16x16x32_bf16 v[28:31], v[164:167], v[164:167], 0\n" \
"v_mfma_f32_16x16x32_bf16 v[32:35], v[164:167], v[164:167], 0\n" \
"v_mfma_f32_16x16x32_bf16 v[36:39], v[164:167], v[164:167], 0\n" \
"v_mfma_f32_16x16x32_bf16 v[40:43], v[164:167], v[164:167], 0\n" \
"v_mfma_f32_16x16x32_bf16 v[44:47], v[164:167], v[164:167], 0\n" \
"v_mfma_f32_16x16x32_bf16 v[48:51], v[164:167], v[164:167], 0\n" \
"v_mfma_f32_16x16x32_bf16 v[52:55], v[164:167], v[164:167], 0\n" \
"v_mfma_f32_16x16x32_bf16 v[56:59], v[164:167], v[164:167], 0\n" \
"v_mfma_f32_16x16x32_bf16 v[60:63], v[164:167], v[164:167], 0\n" \
"v_mfma_f32_16x16x32_bf16 v[64:67], v[164:167], v[164:167], 0\n" \
"v_mfma_f32_16x16x32_bf16 v[68:71], v[164:167], v[164:167], 0\n" \
"v_mfma_f32_16x16x32_bf16 v[72:75], v[164:167], v[164:167], 0\n" \
"v_mfma_f32_16x16x32_bf16 v[76:79], v[164:167], v[164:167], 0\n" \
"v_mfma_f32_16x16x32_bf16 v[80:83], v[164:167], v[164:167], 0\n" \
"v_mfma_f32_16x16x32_bf16 v[84:87], v[164:167], v[164:167], 0\n" \
"v_mfma_f32_16x16x32_bf16 v[88:91], v[164:167], v[164:167], 0\n" \
"v_mfma_f32_16x16x32_bf16 v[92:95], v[164:167], v[164:167], 0\n" \
"v_mfma_f32_16x16x32_bf16 v[96:99], v[164:167], v[164:167], 0\n" \
"v_mfma_f32_16x16x32_bf16 v[100:103], v[164:167], v[164:167], 0\n" \
"v_mfma_f32_16x16x32_bf16 v[104:107], v[164:167], v[164:167], 0\n" \
"v_mfma_f32_16x16x32_bf16 v[108:111], v[164:167], v[164:167], 0\n" \
"v_mfma_f32_16x16x32_bf16 v[112:115], v[164:167], v[164:167], 0\n" \
"s_cmp_eq_u32 s65, 0\n" \
"s_cbranch_scc1 LW3%=\n" \
"s_mov_b32 s67, 0\n" \
"s_waitcnt vmcnt(24)\n" \
"LW1%=:\n" \
"v_cmp_ne_u64 vcc, s[68:69], v[16:17]\n" \
"s_nop 4\n" \
"s_cbranch_vccz LW2%=\n" \
"s_add_u32 s67, s67, 1\n" \
"s_cmp_gt_u32 s67, 0x100000\n" \
"s_cbranch_scc1 LW2%=\n" \
"global_load_dwordx2 v[16:17], v19, s[60:61] sc1\n" \
"s_waitcnt vmcnt(0)\n" \
"s_branch LW1%=\n" \
"LW2%=:\n" \
K2_STAMP3 \
"LW3%=:\n" \
"s_barrier\n" \
"s_add_u32 m0, s48, 0\n" \
"s_nop 0\n" \
"global_load_lds_dwordx4 v9, s[40:41] offset:0 sc1\n" \
"global_load_lds_dwordx4 v9, s[40:41] offset:1024 sc1\n" \
"global_load_lds_dwordx4 v10, s[40:41] offset:2048 sc1\n" \
"global_load_lds_dwordx4 v10, s[40:41] offset:3072 sc1\n" \
"s_add_u32 s40, s40, 2048\n" \
"s_addc_u32 s41, s41, 0\n" \
"s_add_u32 m0, s48, 32768\n" \
"s_nop 0\n" \
"global_load_lds_dwordx4 v9, s[40:41] offset:0 sc1\n" \
"global_load_lds_dwordx4 v9, s[40:41] offset:1024 sc1\n" \
"global_load_lds_dwordx4 v10, s[40:41] offset:2048 sc1\n" \
"global_load_lds_dwordx4 v10, s[40:41] offset:3072 sc1\n" \
"s_add_u32 s40, s40, 2048\n" \
"s_addc_u32 s41, s41, 0\n" \
"s_add_u32 m0, s48, 65536\n" \
"s_nop 0\n" \
"global_load_lds_dwordx4 v9, s[40:41] offset:0 sc1\n" \
"global_load_lds_dwordx4 v9, s[40:41] offset:1024 sc1\n" \
"global_load_lds_dwordx4 v10, s[40:41] offset:2048 sc1\n" \
"global_load_lds_dwordx4 v10, s[40:41] offset:3072 sc1\n" \
"s_add_u32 s40, s40, 2048\n" \
"s_addc_u32 s41, s41, 0\n" \
"s_add_u32 m0, s48, 98304\n" \
"s_nop 0\n" \
"global_load_lds_dwordx4 v9, s[40:41] offset:0 sc1\n" \
"global_load_lds_dwordx4 v9, s[40:41] offset:1024 sc1\n" \
"global_load_lds_dwordx4 v10, s[40:41] offset:2048 sc1\n" \
"global_load_lds_dwordx4 v10, s[40:41] offset:3072 sc1\n" \
"s_add_u32 s40, s40, 2048\n" \
"s_addc_u32 s41, s41, 0\n" \
"s_waitcnt vmcnt(8)\n" \
"s_barrier\n" \
K2_STAMP1 \
"ds_read_b64_tr_b16 v[132:133], v3 offset:0\n" \
"ds_read_b64_tr_b16 v[134:135], v3 offset:1024\n" \
"ds_read_b128 v[116:119], v0 offset:0\n" \
"ds_read_b64_tr_b16 v[136:137], v4 offset:0\n" \
"ds_read_b64_tr_b16 v[138:139], v4 offset:1024\n" \
"ds_read_b64_tr_b16 v[140:141], v5 offset:0\n" \
"ds_read_b64_tr_b16 v[142:143], v5 offset:1024\n" \
"ds_read_b64_tr_b16 v[144:145], v6 offset:0\n" \
"ds_read_b64_tr_b16 v[146:147], v6 offset:1024\n" \
"ds_read_b128 v[120:123], v0 offset:2048\n" \
"ds_read_b128 v[124:127], v1 offset:0\n" \
"ds_read_b128 v[128:131], v1 offset:2048\n" \
"s_mov_b32 s54, 131072\n" \
"s_waitcnt lgkmcnt(9)\n" \
"v_mfma_f32_16x16x32_bf16 v[20:23], v[132:135], v[116:119], v[20:23]\n" \
"s_add_u32 m0, s48, s54\n" \
"ds_read_b64_tr_b16 v[180:181], v3 offset:8192\n" \
"s_waitcnt lgkmcnt(8)\n" \
"v_mfma_f32_16x16x32_bf16 v[24:27], v[136:139], v[116:119], v[24:27]\n" \
"ds_read_b64_tr_b16 v[182:183], v3 offset:9216\n" \
"s_waitcnt lgkmcnt(7)\n" \
"v_mfma_f32_16x16x32_bf16 v[28:31], v[140:143], v[116:119], v[28:31]\n" \
"global_load_lds_dwordx4 v9, s[40:41] offset:0 sc1\n" \
"s_waitcnt lgkmcnt(5)\n" \
"v_mfma_f32_16x16x32_bf16 v[32:35], v[144:147], v[116:119], v[32:35]\n" \
"ds_read_b128 v[164:167], v0 offset:1024\n" \
"s_waitcnt lgkmcnt(5)\n" \
"v_mfma_f32_16x16x32_bf16 v[36:39], v[132:135], v[120:123], v[36:39]\n" \
"ds_read_b64_tr_b16 v[184:185], v4 offset:8192\n" \
"v_mfma_f32_16x16x32_bf16 v[40:43], v[136:139], v[120:123], v[40:43]\n" \
"global_load_lds_dwordx4 v9, s[40:41] offset:1024 sc1\n" \
"v_mfma_f32_16x16x32_bf16 v[44:47], v[140:143], v[120:123], v[44:47]\n" \
"ds_read_b64_tr_b16 v[186:187], v4 offset:9216\n" \
"v_mfma_f32_16x16x32_bf16 v[48:51], v[144:147], v[120:123], v[48:51]\n" \
"ds_read_b64_tr_b16 v[188:189], v5 offset:8192\n" \
"s_waitcnt lgkmcnt(7)\n" \
"v_mfma_f32_16x16x32_bf16 v[52:55], v[132:135], v[124:127], v[52:55]\n" \
"global_load_lds_dwordx4 v10, s[40:41] offset:2048 sc1\n" \
"ds_read_b64_tr_b16 v[190:191], v5 offset:9216\n" \
"v_mfma_f32_16x16x32_bf16 v[56:59], v[136:139], v[124:127], v[56:59]\n" \
"ds_read_b64_tr_b16 v[192:193], v6 offset:8192\n" \
"v_mfma_f32_16x16x32_bf16 v[60:63], v[140:143], v[124:127], v[60:63]\n" \
"global_load_lds_dwordx4 v10, s[40:41] offset:3072 sc1\n" \
"v_mfma_f32_16x16x32_bf16 v[64:67], v[144:147], v[124:127], v[64:67]\n" \
"ds_read_b64_tr_b16 v[194:195], v6 offset:9216\n" \
"s_waitcnt lgkmcnt(9)\n" \
"v_mfma_f32_16x16x32_bf16 v[68:71], v[132:135], v[128:131], v[68:71]\n" \
"ds_read_b128 v[168:171], v0 offset:3072\n" \
"v_mfma_f32_16x16x32_bf16 v[72:75], v[136:139], v[128:131], v[72:75]\n" \
"s_add_u32 s40, s40, 2048\n" \
"s_addc_u32 s41, s41, 0\n" \
"v_mfma_f32_16x16x32_bf16 v[76:79], v[140:143], v[128:131], v[76:79]\n" \
"ds_read_b128 v[172:175], v1 offset:1024\n" \
"v_mfma_f32_16x16x32_bf16 v[80:83], v[144:147], v[128:131], v[80:83]\n" \
"ds_read_b128 v[176:179], v1 offset:3072\n" \
"s_waitcnt lgkmcnt(9)\n" \
"v_mfma_f32_16x16x32_bf16 v[20:23], v[180:183], v[164:167], v[20:23]\n" \
"s_add_u32 m0, s50, s54\n" \
"s_cmp_eq_u32 s51, 131072\n" \
"s_cselect_b32 s52, s53, 32768\n" \
"s_waitcnt lgkmcnt(7)\n" \
"v_mfma_f32_16x16x32_bf16 v[24:27], v[184:187], v[164:167], v[24:27]\n" \
"s_mov_b32 s55, s51\n" \
"s_add_u32 s51, s51, s52\n" \
"s_waitcnt lgkmcnt(5)\n" \
"v_mfma_f32_16x16x32_bf16 v[28:31], v[188:191], v[164:167], v[28:31]\n" \
"v_add_u32 v3, s52, v3\n" \
"global_load_lds_dwordx4 v11, s[42:43] offset:0\n" \
"s_waitcnt lgkmcnt(3)\n" \
"v_mfma_f32_16x16x32_bf16 v[32:35], v[192:195], v[164:167], v[32:35]\n" \
"ds_read_b64_tr_b16 v[132:133], v3 offset:0\n" \
"ds_read_b64_tr_b16 v[134:135], v3 offset:1024\n" \
"s_waitcnt lgkmcnt(4)\n" \
"v_mfma_f32_16x16x32_bf16 v[36:39], v[180:183], v[168:171], v[36:39]\n" \
"v_add_u32 v0, s52, v0\n" \
"global_load_lds_dwordx4 v12, s[42:43] offset:1024\n" \
"v_mfma_f32_16x16x32_bf16 v[40:43], v[184:187], v[168:171], v[40:43]\n" \
"ds_read_b128 v[116:119], v0 offset:0\n" \
"v_add_u32 v4, s52, v4\n" \
"v_mfma_f32_16x16x32_bf16 v[44:47], v[188:191], v[168:171], v[44:47]\n" \
"ds_read_b64_tr_b16 v[136:137], v4 offset:0\n" \
"global_load_lds_dwordx4 v13, s[42:43] offset:2048\n" \
"v_mfma_f32_16x16x32_bf16 v[48:51], v[192:195], v[168:171], v[48:51]\n" \
"ds_read_b64_tr_b16 v[138:139], v4 offset:1024\n" \
"s_waitcnt lgkmcnt(6)\n" \
"v_mfma_f32_16x16x32_bf16 v[52:55], v[180:183], v[172:175], v[52:55]\n" \
"v_add_u32 v5, s52, v5\n" \
"ds_read_b64_tr_b16 v[140:141], v5 offset:0\n" \
"v_mfma_f32_16x16x32_bf16 v[56:59], v[184:187], v[172:175], v[56:59]\n" \
"global_load_lds_dwordx4 v14, s[42:43] offset:3072\n" \
"ds_read_b64_tr_b16 v[142:143], v5 offset:1024\n" \
"v_mfma_f32_16x16x32_bf16 v[60:63], v[188:191], v[172:175], v[60:63]\n" \
"v_add_u32 v6, s52, v6\n" \
"ds_read_b64_tr_b16 v[144:145], v6 offset:0\n" \
"v_mfma_f32_16x16x32_bf16 v[64:67], v[192:195], v[172:175], v[64:67]\n" \
"s_add_u32 s42, s42, 131072\n" \
"s_addc_u32 s43, s43, 0\n" \
"ds_read_b64_tr_b16 v[146:147], v6 offset:1024\n" \
"s_waitcnt lgkmcnt(9)\n" \
"v_mfma_f32_16x16x32_bf16 v[68:71], v[180:183], v[176:179], v[68:71]\n" \
"ds_read_b128 v[120:123], v0 offset:2048\n" \
"v_add_u32 v1, s52, v1\n" \
"v_mfma_f32_16x16x32_bf16 v[72:75], v[184:187], v[176:179], v[72:75]\n" \
"s_add_u32 s74, s74, 1\n" \
"s_cbranch_scc1 LWR%=_1\n" \
"LWS%=_1:\n" \
"ds_read_b128 v[124:127], v1 offset:0\n" \
"v_mfma_f32_16x16x32_bf16 v[76:79], v[188:191], v[176:179], v[76:79]\n" \
"ds_read_b128 v[128:131], v1 offset:2048\n" \
"v_add_u32 v2, s52, v2\n" \
"v_mfma_f32_16x16x32_bf16 v[80:83], v[192:195], v[176:179], v[80:83]\n" \
"s_mov_b32 s54, s55\n" \
"s_waitcnt vmcnt(12)\n" \
"s_barrier\n" \
"s_mov_b32 s66, 23\n" \
"LK2%=:\n" \
"s_waitcnt lgkmcnt(9)\n" \
"v_mfma_f32_16x16x32_bf16 v[20:23], v[132:135], v[116:119], v[20:23]\n" \
"s_add_u32 m0, s48, s54\n" \
"ds_read_b64_tr_b16 v[180:181], v3 offset:8192\n" \
"s_waitcnt lgkmcnt(8)\n" \
"v_mfma_f32_16x16x32_bf16 v[24:27], v[136:139], v[116:119], v[24:27]\n" \
"ds_read_b64_tr_b16 v[182:183], v3 offset:9216\n" \
"s_waitcnt lgkmcnt(7)\n" \
"v_mfma_f32_16x16x32_bf16 v[28:31], v[140:143], v[116:119], v[28:31]\n" \
"global_load_lds_dwordx4 v9, s[40:41] offset:0 sc1\n" \
"s_waitcnt lgkmcnt(5)\n" \
"v_mfma_f32_16x16x32_bf16 v[32:35], v[144:147], v[116:119], v[32:35]\n" \
"ds_read_b128 v[164:167], v0 offset:1024\n" \
"s_waitcnt lgkmcnt(5)\n" \
"v_mfma_f32_16x16x32_bf16 v[36:39], v[132:135], v[120:123], v[36:39]\n" \
"ds_read_b64_tr_b16 v[184:185], v4 offset:8192\n" \
"v_mfma_f32_16x16x32_bf16 v[40:43], v[136:139], v[120:123], v[40:43]\n" \
"global_load_lds_dwordx4 v9, s[40:41] offset:1024 sc1\n" \
"v_mfma_f32_16x16x32_bf16 v[44:47], v[140:143], v[120:123], v[44:47]\n" \
"ds_read_b64_tr_b16 v[186:187], v4 offset:9216\n" \
"v_mfma_f32_16x16x32_bf16 v[48:51], v[144:147], v[120:123], v[48:51]\n" \
"ds_read_b64_tr_b16 v[188:189], v5 offset:8192\n" \
"s_waitcnt lgkmcnt(7)\n" \
"v_mfma_f32_16x16x32_bf16 v[52:55], v[132:135], v[124:127], v[52:55]\n" \
"global_load_lds_dwordx4 v10, s[40:41] offset:2048 sc1\n" \
"ds_read_b64_tr_b16 v[190:191], v5 offset:9216\n" \
"v_mfma_f32_16x16x32_bf16 v[56:59], v[136:139], v[124:127], v[56:59]\n" \
"ds_read_b64_tr_b16 v[192:193], v6 offset:8192\n" \
"v_mfma_f32_16x16x32_bf16 v[60:63], v[140:143], v[124:127], v[60:63]\n" \
"global_load_lds_dwordx4 v10, s[40:41] offset:3072 sc1\n" \
"v_mfma_f32_16x16x32_bf16 v[64:67], v[144:147], v[124:127], v[64:67]\n" \
"ds_read_b64_tr_b16 v[194:195], v6 offset:9216\n" \
"s_waitcnt lgkmcnt(9)\n" \
"v_mfma_f32_16x16x32_bf16 v[68:71], v[132:135], v[128:131], v[68:71]\n" \
"ds_read_b128 v[168:171], v0 offset:3072\n" \
"v_mfma_f32_16x16x32_bf16 v[72:75], v[136:139], v[128:131], v[72:75]\n" \
"s_add_u32 s40, s40, 2048\n" \
"s_addc_u32 s41, s41, 0\n" \
"v_mfma_f32_16x16x32_bf16 v[76:79], v[140:143], v[128:131], v[76:79]\n" \
"ds_read_b128 v[172:175], v1 offset:1024\n" \
"v_mfma_f32_16x16x32_bf16 v[80:83], v[144:147], v[128:131], v[80:83]\n" \
"ds_read_b128 v[176:179], v1 offset:3072\n" \
"s_waitcnt lgkmcnt(9)\n" \
"v_mfma_f32_16x16x32_bf16 v[20:23], v[180:183], v[164:167], v[20:23]\n" \
"s_add_u32 m0, s50, s54\n" \
"s_cmp_eq_u32 s51, 131072\n" \
"s_cselect_b32 s52, s53, 32768\n" \
"s_waitcnt lgkmcnt(7)\n" \
"v_mfma_f32_16x16x32_bf16 v[24:27], v[184:187], v[164:167], v[24:27]\n" \
"s_mov_b32 s55, s51\n" \
"s_add_u32 s51, s51, s52\n" \
"s_waitcnt lgkmcnt(5)\n" \
"v_mfma_f32_16x16x32_bf16 v[28:31], v[188:191], v[164:167], v[28:31]\n" \
"v_add_u32 v3, s52, v3\n" \
"global_load_lds_dwordx4 v11, s[42:43] offset:0\n" \
"s_waitcnt lgkmcnt(3)\n" \
"v_mfma_f32_16x16x32_bf16 v[32:35], v[192:195], v[164:167], v[32:35]\n" \
"ds_read_b64_tr_b16 v[132:133], v3 offset:0\n" \
"ds_read_b64_tr_b16 v[134:135], v3 offset:1024\n" \
"s_waitcnt lgkmcnt(4)\n" \
"v_mfma_f32_16x16x32_bf16 v[36:39], v[180:183], v[168:171], v[36:39]\n" \
"v_add_u32 v0, s52, v0\n" \
"global_load_lds_dwordx4 v12, s[42:43] offset:1024\n" \
"v_mfma_f32_16x16x32_bf16 v[40:43], v[184:187], v[168:171], v[40:43]\n" \
"ds_read_b128 v[116:119], v0 offset:0\n" \
"v_add_u32 v4, s52, v4\n" \
"v_mfma_f32_16x16x32_bf16 v[44:47], v[188:191], v[168:171], v[44:47]\n" \
"ds_read_b64_tr_b16 v[136:137], v4 offset:0\n" \
"global_load_lds_dwordx4 v13, s[42:43] offset:2048\n" \
"v_mfma_f32_16x16x32_bf16 v[48:51], v[192:195], v[168:171], v[48:51]\n" \
"ds_read_b64_tr_b16 v[138:139], v4 offset:1024\n" \
"s_waitcnt lgkmcnt(6)\n" \
"v_mfma_f32_16x16x32_bf16 v[52:55], v[180:183], v[172:175], v[52:55]\n" \
"v_add_u32 v5, s52, v5\n" \
"ds_read_b64_tr_b16 v[140:141], v5 offset:0\n" \
"v_mfma_f32_16x16x32_bf16 v[56:59], v[184:187], v[172:175], v[56:59]\n" \
"global_load_lds_dwordx4 v14, s[42:43] offset:3072\n" \
"ds_read_b64_tr_b16 v[142:143], v5 offset:1024\n" \
"v_mfma_f32_16x16x32_bf16 v[60:63], v[188:191], v[172:175], v[60:63]\n" \
"v_add_u32 v6, s52, v6\n" \
"ds_read_b64_tr_b16 v[144:145], v6 offset:0\n" \
"v_mfma_f32_16x16x32_bf16 v[64:67], v[192:195], v[172:175], v[64:67]\n" \
"s_add_u32 s42, s42, 131072\n" \
"s_addc_u32 s43, s43, 0\n" \
"ds_read_b64_tr_b16 v[146:147], v6 offset:1024\n" \
"s_waitcnt lgkmcnt(9)\n" \
"v_mfma_f32_16x16x32_bf16 v[68:71], v[180:183], v[176:179], v[68:71]\n" \
"ds_read_b128 v[120:123], v0 offset:2048\n" \
"v_add_u32 v1, s52, v1\n" \
"v_mfma_f32_16x16x32_bf16 v[72:75], v[184:187], v[176:179], v[72:75]\n" \
"s_add_u32 s74, s74, 1\n" \
"s_cbranch_scc1 LWR%=_2\n" \
"LWS%=_2:\n" \
"ds_read_b128 v[124:127], v1 offset:0\n" \
"v_mfma_f32_16x16x32_bf16 v[76:79], v[188:191], v[176:179], v[76:79]\n" \
"ds_read_b128 v[128:131], v1 offset:2048\n" \
"v_add_u32 v2, s52, v2\n" \
"v_mfma_f32_16x16x32_bf16 v[80:83], v[192:195], v[176:179], v[80:83]\n" \
"s_mov_b32 s54, s55\n" \
"s_waitcnt vmcnt(16)\n" \
"s_barrier\n" \
"s_sub_u32 s66, s66, 1\n" \
"s_cmp_lg_u32 s66, 0\n" \
"s_cbranch_scc1 LK2%=\n" \
"s_waitcnt lgkmcnt(9)\n" \
"v_mfma_f32_16x16x32_bf16 v[20:23], v[132:135], v[116:119], v[20:23]\n" \
"s_add_u32 m0, s49, s54\n" \
"s_waitcnt lgkmcnt(7)\n" \
"v_mfma_f32_16x16x32_bf16 v[24:27], v[136:139], v[116:119], v[24:27]\n" \
"ds_read_b64_tr_b16 v[180:181], v3 offset:8192\n" \
"s_waitcnt lgkmcnt(6)\n" \
"v_mfma_f32_16x16x32_bf16 v[28:31], v[140:143], v[116:119], v[28:31]\n" \
"ds_read_b64_tr_b16 v[182:183], v3 offset:9216\n" \
"s_waitcnt lgkmcnt(5)\n" \
"v_mfma_f32_16x16x32_bf16 v[32:35], v[144:147], v[116:119], v[32:35]\n" \
"ds_read_b128 v[164:167], v0 offset:1024\n" \
"s_waitcnt lgkmcnt(5)\n" \
"v_mfma_f32_16x16x32_bf16 v[36:39], v[132:135], v[120:123], v[36:39]\n" \
"global_load_lds_dwordx4 v9, s[44:45] offset:0 sc1\n" \
"v_mfma_f32_16x16x32_bf16 v[40:43], v[136:139], v[120:123], v[40:43]\n" \
"ds_read_b64_tr_b16 v[184:185], v4 offset:8192\n" \
"v_mfma_f32_16x16x32_bf16 v[44:47], v[140:143], v[120:123], v[44:47]\n" \
"ds_read_b64_tr_b16 v[186:187], v4 offset:9216\n" \
"v_mfma_f32_16x16x32_bf16 v[48:51], v[144:147], v[120:123], v[48:51]\n" \
"ds_read_b64_tr_b16 v[188:189], v5 offset:8192\n" \
"s_waitcnt lgkmcnt(7)\n" \
"v_mfma_f32_16x16x32_bf16 v[52:55], v[132:135], v[124:127], v[52:55]\n" \
"global_load_lds_dwordx4 v9, s[44:45] offset:1024 sc1\n" \
"v_mfma_f32_16x16x32_bf16 v[56:59], v[136:139], v[124:127], v[56:59]\n" \
"ds_read_b64_tr_b16 v[190:191], v5 offset:9216\n" \
"v_mfma_f32_16x16x32_bf16 v[60:63], v[140:143], v[124:127], v[60:63]\n" \
"ds_read_b64_tr_b16 v[192:193], v6 offset:8192\n" \
"v_mfma_f32_16x16x32_bf16 v[64:67], v[144:147], v[124:127], v[64:67]\n" \
"ds_read_b64_tr_b16 v[194:195], v6 offset:9216\n" \
"s_waitcnt lgkmcnt(9)\n" \
"v_mfma_f32_16x16x32_bf16 v[68:71], v[132:135], v[128:131], v[68:71]\n" \
"s_add_u32 s44, s44, 2048\n" \
"s_addc_u32 s45, s45, 0\n" \
"v_mfma_f32_16x16x32_bf16 v[72:75], v[136:139], v[128:131], v[72:75]\n" \
"ds_read_b128 v[168:171], v0 offset:3072\n" \
"v_mfma_f32_16x16x32_bf16 v[76:79], v[140:143], v[128:131], v[76:79]\n" \
"ds_read_b128 v[172:175], v1 offset:1024\n" \
"v_mfma_f32_16x16x32_bf16 v[80:83], v[144:147], v[128:131], v[80:83]\n" \
"ds_read_b128 v[176:179], v1 offset:3072\n" \
"s_waitcnt lgkmcnt(9)\n" \
"v_mfma_f32_16x16x32_bf16 v[20:23], v[180:183], v[164:167], v[20:23]\n" \
"s_add_u32 m0, s50, s54\n" \
"s_cmp_eq_u32 s51, 131072\n" \
"s_cselect_b32 s52, s53, 32768\n" \
"s_waitcnt lgkmcnt(7)\n" \
"v_mfma_f32_16x16x32_bf16 v[24:27], v[184:187], v[164:167], v[24:27]\n" \
"s_mov_b32 s55, s51\n" \
"s_add_u32 s51, s51, s52\n" \
"s_waitcnt lgkmcnt(5)\n" \
"v_mfma_f32_16x16x32_bf16 v[28:31], v[188:191], v[164:167], v[28:31]\n" \
"v_add_u32 v3, s52, v3\n" \
"global_load_lds_dwordx4 v11, s[46:47] offset:0\n" \
"s_waitcnt lgkmcnt(3)\n" \
"v_mfma_f32_16x16x32_bf16 v[32:35], v[192:195], v[164:167], v[32:35]\n" \
"ds_read_b64_tr_b16 v[132:133], v3 offset:0\n" \
"ds_read_b64_tr_b16 v[134:135], v3 offset:1024\n" \
"s_waitcnt lgkmcnt(4)\n" \
"v_mfma_f32_16x16x32_bf16 v[36:39], v[180:183], v[168:171], v[36:39]\n" \
"v_add_u32 v0, s52, v0\n" \
"global_load_lds_dwordx4 v12, s[46:47] offset:1024\n" \
"v_mfma_f32_16x16x32_bf16 v[40:43], v[184:187], v[168:171], v[40:43]\n" \
"ds_read_b128 v[116:119], v0 offset:0\n" \
"v_add_u32 v4, s52, v4\n" \
"v_mfma_f32_16x16x32_bf16 v[44:47], v[188:191], v[168:171], v[44:47]\n" \
"ds_read_b64_tr_b16 v[136:137], v4 offset:0\n" \
"global_load_lds_dwordx4 v13, s[46:47] offset:2048\n" \
"v_mfma_f32_16x16x32_bf16 v[48:51], v[192:195], v[168:171], v[48:51]\n" \
"ds_read_b64_tr_b16 v[138:139], v4 offset:1024\n" \
"s_waitcnt lgkmcnt(6)\n" \
"v_mfma_f32_16x16x32_bf16 v[52:55], v[180:183], v[172:175], v[52:55]\n" \
"v_add_u32 v5, s52, v5\n" \
"ds_read_b64_tr_b16 v[140:141], v5 offset:0\n" \
"v_mfma_f32_16x16x32_bf16 v[56:59], v[184:187], v[172:175], v[56:59]\n" \
"global_load_lds_dwordx4 v14, s[46:47] offset:3072\n" \
"ds_read_b64_tr_b16 v[142:143], v5 offset:1024\n" \
"v_mfma_f32_16x16x32_bf16 v[60:63], v[188:191], v[172:175], v[60:63]\n" \
"v_add_u32 v6, s52, v6\n" \
"ds_read_b64_tr_b16 v[144:145], v6 offset:0\n" \
"v_mfma_f32_16x16x32_bf16 v[64:67], v[192:195], v[172:175], v[64:67]\n" \
"s_add_u32 s46, s46, 131072\n" \
"s_addc_u32 s47, s47, 0\n" \
"ds_read_b64_tr_b16 v[146:147], v6 offset:1024\n" \
"s_waitcnt lgkmcnt(9)\n" \
"v_mfma_f32_16x16x32_bf16 v[68:71], v[180:183], v[176:179], v[68:71]\n" \
"ds_read_b128 v[120:123], v0 offset:2048\n" \
"v_add_u32 v1, s52, v1\n" \
"v_mfma_f32_16x16x32_bf16 v[72:75], v[184:187], v[176:179], v[72:75]\n" \
"s_add_u32 s75, s75, 1\n" \
"s_cbranch_scc1 LWR%=_3\n" \
"LWS%=_3:\n" \
"ds_read_b128 v[124:127], v1 offset:0\n" \
"v_mfma_f32_16x16x32_bf16 v[76:79], v[188:191], v[176:179], v[76:79]\n" \
"ds_read_b128 v[128:131], v1 offset:2048\n" \
"v_add_u32 v2, s52, v2\n" \
"v_mfma_f32_16x16x32_bf16 v[80:83], v[192:195], v[176:179], v[80:83]\n" \
"s_mov_b32 s54, s55\n" \
"s_waitcnt vmcnt(14)\n" \
"s_barrier\n" \
"s_waitcnt lgkmcnt(9)\n" \
"v_mfma_f32_16x16x32_bf16 v[20:23], v[132:135], v[116:119], v[20:23]\n" \
"s_add_u32 m0, s49, s54\n" \
"s_waitcnt lgkmcnt(7)\n" \
"v_mfma_f32_16x16x32_bf16 v[24:27], v[136:139], v[116:119], v[24:27]\n" \
"ds_read_b64_tr_b16 v[180:181], v3 offset:8192\n" \
"s_waitcnt lgkmcnt(6)\n" \
"v_mfma_f32_16x16x32_bf16 v[28:31], v[140:143], v[116:119], v[28:31]\n" \
"ds_read_b64_tr_b16 v[182:183], v3 offset:9216\n" \
"s_waitcnt lgkmcnt(5)\n" \
"v_mfma_f32_16x16x32_bf16 v[32:35], v[144:147], v[116:119], v[32:35]\n" \
"ds_read_b128 v[164:167], v0 offset:1024\n" \
"s_waitcnt lgkmcnt(5)\n" \
"v_mfma_f32_16x16x32_bf16 v[36:39], v[132:135], v[120:123], v[36:39]\n" \
"global_load_lds_dwordx4 v9, s[44:45] offset:0 sc1\n" \
"v_mfma_f32_16x16x32_bf16 v[40:43], v[136:139], v[120:123], v[40:43]\n" \
"ds_read_b64_tr_b16 v[184:185], v4 offset:8192\n" \
"v_mfma_f32_16x16x32_bf16 v[44:47], v[140:143], v[120:123], v[44:47]\n" \
"ds_read_b64_tr_b16 v[186:187], v4 offset:9216\n" \
"v_mfma_f32_16x16x32_bf16 v[48:51], v[144:147], v[120:123], v[48:51]\n" \
"ds_read_b64_tr_b16 v[188:189], v5 offset:8192\n" \
"s_waitcnt lgkmcnt(7)\n" \
"v_mfma_f32_16x16x32_bf16 v[52:55], v[132:135], v[124:127], v[52:55]\n" \
"global_load_lds_dwordx4 v9, s[44:45] offset:1024 sc1\n" \
"v_mfma_f32_16x16x32_bf16 v[56:59], v[136:139], v[124:127], v[56:59]\n" \
"ds_read_b64_tr_b16 v[190:191], v5 offset:9216\n" \
"v_mfma_f32_16x16x32_bf16 v[60:63], v[140:143], v[124:127], v[60:63]\n" \
"ds_read_b64_tr_b16 v[192:193], v6 offset:8192\n" \
"v_mfma_f32_16x16x32_bf16 v[64:67], v[144:147], v[124:127], v[64:67]\n" \
"ds_read_b64_tr_b16 v[194:195], v6 offset:9216\n" \
"s_waitcnt lgkmcnt(9)\n" \
"v_mfma_f32_16x16x32_bf16 v[68:71], v[132:135], v[128:131], v[68:71]\n" \
"s_add_u32 s44, s44, 2048\n" \
"s_addc_u32 s45, s45, 0\n" \
"v_mfma_f32_16x16x32_bf16 v[72:75], v[136:139], v[128:131], v[72:75]\n" \
"ds_read_b128 v[168:171], v0 offset:3072\n" \
"v_mfma_f32_16x16x32_bf16 v[76:79], v[140:143], v[128:131], v[76:79]\n" \
"ds_read_b128 v[172:175], v1 offset:1024\n" \
"v_mfma_f32_16x16x32_bf16 v[80:83], v[144:147], v[128:131], v[80:83]\n" \
"ds_read_b128 v[176:179], v1 offset:3072\n" \
"s_waitcnt lgkmcnt(9)\n" \
"v_mfma_f32_16x16x32_bf16 v[20:23], v[180:183], v[164:167], v[20:23]\n" \
"s_add_u32 m0, s50, s54\n" \
"s_cmp_eq_u32 s51, 131072\n" \
"s_cselect_b32 s52, s53, 32768\n" \
"s_waitcnt lgkmcnt(7)\n" \
"v_mfma_f32_16x16x32_bf16 v[24:27], v[184:187], v[164:167], v[24:27]\n" \
"s_mov_b32 s55, s51\n" \
"s_add_u32 s51, s51, s52\n" \
"s_waitcnt lgkmcnt(5)\n" \
"v_mfma_f32_16x16x32_bf16 v[28:31], v[188:191], v[164:167], v[28:31]\n" \
"v_add_u32 v3, s52, v3\n" \
"global_load_lds_dwordx4 v11, s[46:47] offset:0\n" \
"s_waitcnt lgkmcnt(3)\n" \
"v_mfma_f32_16x16x32_bf16 v[32:35], v[192:195], v[164:167], v[32:35]\n" \
"ds_read_b64_tr_b16 v[132:133], v3 offset:0\n" \
"ds_read_b64_tr_b16 v[134:135], v3 offset:1024\n" \
"s_waitcnt lgkmcnt(4)\n" \
"v_mfma_f32_16x16x32_bf16 v[36:39], v[180:183], v[168:171], v[36:39]\n" \
"v_add_u32 v0, s52, v0\n" \
"global_load_lds_dwordx4 v12, s[46:47] offset:1024\n" \
"v_mfma_f32_16x16x32_bf16 v[40:43], v[184:187], v[168:171], v[40:43]\n" \
"ds_read_b128 v[116:119], v0 offset:0\n" \
"v_add_u32 v4, s52, v4\n" \
"v_mfma_f32_16x16x32_bf16 v[44:47], v[188:191], v[168:171], v[44:47]\n" \
"ds_read_b64_tr_b16 v[136:137], v4 offset:0\n" \
"global_load_lds_dwordx4 v13, s[46:47] offset:2048\n" \
"v_mfma_f32_16x16x32_bf16 v[48:51], v[192:195], v[168:171], v[48:51]\n" \
"ds_read_b64_tr_b16 v[138:139], v4 offset:1024\n" \
"s_waitcnt lgkmcnt(6)\n" \
"v_mfma_f32_16x16x32_bf16 v[52:55], v[180:183], v[172:175], v[52:55]\n" \
"v_add_u32 v5, s52, v5\n" \
"ds_read_b64_tr_b16 v[140:141], v5 offset:0\n" \
"v_mfma_f32_16x16x32_bf16 v[56:59], v[184:187], v[172:175], v[56:59]\n" \
"global_load_lds_dwordx4 v14, s[46:47] offset:3072\n" \
"ds_read_b64_tr_b16 v[142:143], v5 offset:1024\n" \
"v_mfma_f32_16x16x32_bf16 v[60:63], v[188:191], v[172:175], v[60:63]\n" \
"v_add_u32 v6, s52, v6\n" \
"ds_read_b64_tr_b16 v[144:145], v6 offset:0\n" \
"v_mfma_f32_16x16x32_bf16 v[64:67], v[192:195], v[172:175], v[64:67]\n" \
"s_add_u32 s46, s46, 131072\n" \
"s_addc_u32 s47, s47, 0\n" \
"ds_read_b64_tr_b16 v[146:147], v6 offset:1024\n" \
"s_waitcnt lgkmcnt(9)\n" \
"v_mfma_f32_16x16x32_bf16 v[68:71], v[180:183], v[176:179], v[68:71]\n" \
"ds_read_b128 v[120:123], v0 offset:2048\n" \
"v_add_u32 v1, s52, v1\n" \
"v_mfma_f32_16x16x32_bf16 v[72:75], v[184:187], v[176:179], v[72:75]\n" \
"s_add_u32 s75, s75, 1\n" \
"s_cbranch_scc1 LWR%=_4\n" \
"LWS%=_4:\n" \
"ds_read_b128 v[124:127], v1 offset:0\n" \
"v_mfma_f32_16x16x32_bf16 v[76:79], v[188:191], v[176:179], v[76:79]\n" \
"ds_read_b128 v[128:131], v1 offset:2048\n" \
"v_add_u32 v2, s52, v2\n" \
"v_mfma_f32_16x16x32_bf16 v[80:83], v[192:195], v[176:179], v[80:83]\n" \
"s_mov_b32 s54, s55\n" \
"s_waitcnt vmcnt(12)\n" \
"s_barrier\n" \
"s_waitcnt lgkmcnt(9)\n" \
"v_mfma_f32_16x16x32_bf16 v[20:23], v[132:135], v[116:119], v[20:23]\n" \
"s_add_u32 m0, s49, s54\n" \
"s_waitcnt lgkmcnt(7)\n" \
"v_mfma_f32_16x16x32_bf16 v[24:27], v[136:139], v[116:119], v[24:27]\n" \
"ds_read_b64_tr_b16 v[180:181], v3 offset:8192\n" \
"s_waitcnt lgkmcnt(6)\n" \
"v_mfma_f32_16x16x32_bf16 v[28:31], v[140:143], v[116:119], v[28:31]\n" \
"ds_read_b64_tr_b16 v[182:183], v3 offset:9216\n" \
"s_waitcnt lgkmcnt(5)\n" \
"v_mfma_f32_16x16x32_bf16 v[32:35], v[144:147], v[116:119], v[32:35]\n" \
"ds_read_b128 v[164:167], v0 offset:1024\n" \
"s_waitcnt lgkmcnt(5)\n" \
"v_mfma_f32_16x16x32_bf16 v[36:39], v[132:135], v[120:123], v[36:39]\n" \
"global_load_lds_dwordx4 v9, s[44:45] offset:0 sc1\n" \
"v_mfma_f32_16x16x32_bf16 v[40:43], v[136:139], v[120:123], v[40:43]\n" \
"ds_read_b64_tr_b16 v[184:185], v4 offset:8192\n" \
"v_mfma_f32_16x16x32_bf16 v[44:47], v[140:143], v[120:123], v[44:47]\n" \
"ds_read_b64_tr_b16 v[186:187], v4 offset:9216\n" \
"v_mfma_f32_16x16x32_bf16 v[48:51], v[144:147], v[120:123], v[48:51]\n" \
"ds_read_b64_tr_b16 v[188:189], v5 offset:8192\n" \
"s_waitcnt lgkmcnt(7)\n" \
"v_mfma_f32_16x16x32_bf16 v[52:55], v[132:135], v[124:127], v[52:55]\n" \
"global_load_lds_dwordx4 v9, s[44:45] offset:1024 sc1\n" \
"v_mfma_f32_16x16x32_bf16 v[56:59], v[136:139], v[124:127], v[56:59]\n" \
"ds_read_b64_tr_b16 v[190:191], v5 offset:9216\n" \
"v_mfma_f32_16x16x32_bf16 v[60:63], v[140:143], v[124:127], v[60:63]\n" \
"ds_read_b64_tr_b16 v[192:193], v6 offset:8192\n" \
"v_mfma_f32_16x16x32_bf16 v[64:67], v[144:147], v[124:127], v[64:67]\n" \
"ds_read_b64_tr_b16 v[194:195], v6 offset:9216\n" \
"s_waitcnt lgkmcnt(9)\n" \
"v_mfma_f32_16x16x32_bf16 v[68:71], v[132:135], v[128:131], v[68:71]\n" \
"s_add_u32 s44, s44, 2048\n" \
"s_addc_u32 s45, s45, 0\n" \
"v_mfma_f32_16x16x32_bf16 v[72:75], v[136:139], v[128:131], v[72:75]\n" \
"ds_read_b128 v[168:171], v0 offset:3072\n" \
"v_mfma_f32_16x16x32_bf16 v[76:79], v[140:143], v[128:131], v[76:79]\n" \
"ds_read_b128 v[172:175], v1 offset:1024\n" \
"v_mfma_f32_16x16x32_bf16 v[80:83], v[144:147], v[128:131], v[80:83]\n" \
"ds_read_b128 v[176:179], v1 offset:3072\n" \
"s_waitcnt lgkmcnt(9)\n" \
"v_mfma_f32_16x16x32_bf16 v[20:23], v[180:183], v[164:167], v[20:23]\n" \
"s_add_u32 m0, s50, s54\n" \
"s_cmp_eq_u32 s51, 131072\n" \
"s_cselect_b32 s52, s53, 32768\n" \
"s_waitcnt lgkmcnt(7)\n" \
"v_mfma_f32_16x16x32_bf16 v[24:27], v[184:187], v[164:167], v[24:27]\n" \
"s_mov_b32 s55, s51\n" \
"s_add_u32 s51, s51, s52\n" \
"s_waitcnt lgkmcnt(5)\n" \
"v_mfma_f32_16x16x32_bf16 v[28:31], v[188:191], v[164:167], v[28:31]\n" \
"v_add_u32 v3, s52, v3\n" \
"global_load_lds_dwordx4 v11, s[46:47] offset:0\n" \
"s_waitcnt lgkmcnt(3)\n" \
"v_mfma_f32_16x16x32_bf16 v[32:35], v[192:195], v[164:167], v[32:35]\n" \
"ds_read_b64_tr_b16 v[132:133], v3 offset:0\n" \
"ds_read_b64_tr_b16 v[134:135], v3 offset:1024\n" \
"s_waitcnt lgkmcnt(4)\n" \
"v_mfma_f32_16x16x32_bf16 v[36:39], v[180:183], v[168:171], v[36:39]\n" \
"v_add_u32 v0, s52, v0\n" \
"global_load_lds_dwordx4 v12, s[46:47] offset:1024\n" \
"v_mfma_f32_16x16x32_bf16 v[40:43], v[184:187], v[168:171], v[40:43]\n" \
"ds_read_b128 v[116:119], v0 offset:0\n" \
"v_add_u32 v4, s52, v4\n" \
"v_mfma_f32_16x16x32_bf16 v[44:47], v[188:191], v[168:171], v[44:47]\n" \
"ds_read_b64_tr_b16 v[136:137], v4 offset:0\n" \
"global_load_lds_dwordx4 v13, s[46:47] offset:2048\n" \
"v_mfma_f32_16x16x32_bf16 v[48:51], v[192:195], v[168:171], v[48:51]\n" \
"ds_read_b64_tr_b16 v[138:139], v4 offset:1024\n" \
"s_waitcnt lgkmcnt(6)\n" \
"v_mfma_f32_16x16x32_bf16 v[52:55], v[180:183], v[172:175], v[52:55]\n" \
"v_add_u32 v5, s52, v5\n" \
"ds_read_b64_tr_b16 v[140:141], v5 offset:0\n" \
"v_mfma_f32_16x16x32_bf16 v[56:59], v[184:187], v[172:175], v[56:59]\n" \
"global_load_lds_dwordx4 v14, s[46:47] offset:3072\n" \
"ds_read_b64_tr_b16 v[142:143], v5 offset:1024\n" \
"v_mfma_f32_16x16x32_bf16 v[60:63], v[188:191], v[172:175], v[60:63]\n" \
"v_add_u32 v6, s52, v6\n" \
"ds_read_b64_tr_b16 v[144:145], v6 offset:0\n" \
"v_mfma_f32_16x16x32_bf16 v[64:67], v[192:195], v[172:175], v[64:67]\n" \
"s_add_u32 s46, s46, 131072\n" \
"s_addc_u32 s47, s47, 0\n" \
"ds_read_b64_tr_b16 v[146:147], v6 offset:1024\n" \
"s_waitcnt lgkmcnt(9)\n" \
"v_mfma_f32_16x16x32_bf16 v[68:71], v[180:183], v[176:179], v[68:71]\n" \
"ds_read_b128 v[120:123], v0 offset:2048\n" \
"v_add_u32 v1, s52, v1\n" \
"v_mfma_f32_16x16x32_bf16 v[72:75], v[184:187], v[176:179], v[72:75]\n" \
"s_add_u32 s75, s75, 1\n" \
"s_cbranch_scc1 LWR%=_5\n" \
"LWS%=_5:\n" \
"ds_read_b128 v[124:127], v1 offset:0\n" \
"v_mfma_f32_16x16x32_bf16 v[76:79], v[188:191], v[176:179], v[76:79]\n" \
"ds_read_b128 v[128:131], v1 offset:2048\n" \
"v_add_u32 v2, s52, v2\n" \
"v_mfma_f32_16x16x32_bf16 v[80:83], v[192:195], v[176:179], v[80:83]\n" \
"s_mov_b32 s54, s55\n" \
"s_waitcnt vmcnt(12)\n" \
"s_barrier\n" \
"s_waitcnt lgkmcnt(9)\n" \
"v_mfma_f32_16x16x32_bf16 v[20:23], v[132:135], v[116:119], v[20:23]\n" \
"s_add_u32 m0, s49, s54\n" \
"s_waitcnt lgkmcnt(7)\n" \
"v_mfma_f32_16x16x32_bf16 v[24:27], v[136:139], v[116:119], v[24:27]\n" \
"ds_read_b64_tr_b16 v[180:181], v3 offset:8192\n" \
"s_waitcnt lgkmcnt(6)\n" \
"v_mfma_f32_16x16x32_bf16 v[28:31], v[140:143], v[116:119], v[28:31]\n" \
"ds_read_b64_tr_b16 v[182:183], v3 offset:9216\n" \
"s_waitcnt lgkmcnt(5)\n" \
"v_mfma_f32_16x16x32_bf16 v[32:35], v[144:147], v[116:119], v[32:35]\n" \
"ds_read_b128 v[164:167], v0 offset:1024\n" \
"s_waitcnt lgkmcnt(5)\n" \
"v_mfma_f32_16x16x32_bf16 v[36:39], v[132:135], v[120:123], v[36:39]\n" \
"global_load_lds_dwordx4 v9, s[44:45] offset:0 sc1\n" \
"v_mfma_f32_16x16x32_bf16 v[40:43], v[136:139], v[120:123], v[40:43]\n" \
"ds_read_b64_tr_b16 v[184:185], v4 offset:8192\n" \
"v_mfma_f32_16x16x32_bf16 v[44:47], v[140:143], v[120:123], v[44:47]\n" \
"ds_read_b64_tr_b16 v[186:187], v4 offset:9216\n" \
"v_mfma_f32_16x16x32_bf16 v[48:51], v[144:147], v[120:123], v[48:51]\n" \
"ds_read_b64_tr_b16 v[188:189], v5 offset:8192\n" \
"s_waitcnt lgkmcnt(7)\n" \
"v_mfma_f32_16x16x32_bf16 v[52:55], v[132:135], v[124:127], v[52:55]\n" \
"global_load_lds_dwordx4 v9, s[44:45] offset:1024 sc1\n" \
"v_mfma_f32_16x16x32_bf16 v[56:59], v[136:139], v[124:127], v[56:59]\n" \
"ds_read_b64_tr_b16 v[190:191], v5 offset:9216\n" \
"v_mfma_f32_16x16x32_bf16 v[60:63], v[140:143], v[124:127], v[60:63]\n" \
"ds_read_b64_tr_b16 v[192:193], v6 offset:8192\n" \
"v_mfma_f32_16x16x32_bf16 v[64:67], v[144:147], v[124:127], v[64:67]\n" \
"ds_read_b64_tr_b16 v[194:195], v6 offset:9216\n" \
"s_waitcnt lgkmcnt(9)\n" \
"v_mfma_f32_16x16x32_bf16 v[68:71], v[132:135], v[128:131], v[68:71]\n" \
"s_add_u32 s44, s44, 2048\n" \
"s_addc_u32 s45, s45, 0\n" \
"v_mfma_f32_16x16x32_bf16 v[72:75], v[136:139], v[128:131], v[72:75]\n" \
"ds_read_b128 v[168:171], v0 offset:3072\n" \
"v_mfma_f32_16x16x32_bf16 v[76:79], v[140:143], v[128:131], v[76:79]\n" \
"ds_read_b128 v[172:175], v1 offset:1024\n" \
"v_mfma_f32_16x16x32_bf16 v[80:83], v[144:147], v[128:131], v[80:83]\n" \
"ds_read_b128 v[176:179], v1 offset:3072\n" \
"s_waitcnt lgkmcnt(9)\n" \
"v_mfma_f32_16x16x32_bf16 v[20:23], v[180:183], v[164:167], v[20:23]\n" \
"s_add_u32 m0, s50, s54\n" \
"s_cmp_eq_u32 s51, 131072\n" \
"s_cselect_b32 s52, s53, 32768\n" \
"s_mov_b32 s55, s51\n" \
"s_waitcnt lgkmcnt(7)\n" \
"v_mfma_f32_16x16x32_bf16 v[24:27], v[184:187], v[164:167], v[24:27]\n" \
"s_add_u32 s51, s51, s52\n" \
"v_add_u32 v3, s52, v3\n" \
"s_waitcnt lgkmcnt(5)\n" \
"v_mfma_f32_16x16x32_bf16 v[28:31], v[188:191], v[164:167], v[28:31]\n" \
"global_load_lds_dwordx4 v11, s[46:47] offset:0\n" \
"ds_read_b64_tr_b16 v[132:133], v3 offset:0\n" \
"s_waitcnt lgkmcnt(4)\n" \
"v_mfma_f32_16x16x32_bf16 v[32:35], v[192:195], v[164:167], v[32:35]\n" \
"ds_read_b64_tr_b16 v[134:135], v3 offset:1024\n" \
"v_add_u32 v2, s52, v2\n" \
"s_waitcnt lgkmcnt(4)\n" \
"v_mfma_f32_16x16x32_bf16 v[36:39], v[180:183], v[168:171], v[36:39]\n" \
"ds_read_b128 v[116:119], v2 offset:0\n" \
"global_load_lds_dwordx4 v12, s[46:47] offset:1024\n" \
"v_add_u32 v4, s52, v4\n" \
"v_mfma_f32_16x16x32_bf16 v[40:43], v[184:187], v[168:171], v[40:43]\n" \
"ds_read_b64_tr_b16 v[136:137], v4 offset:0\n" \
"ds_read_b64_tr_b16 v[138:139], v4 offset:1024\n" \
"v_mfma_f32_16x16x32_bf16 v[44:47], v[188:191], v[168:171], v[44:47]\n" \
"v_add_u32 v5, s52, v5\n" \
"global_load_lds_dwordx4 v13, s[46:47] offset:2048\n" \
"v_mfma_f32_16x16x32_bf16 v[48:51], v[192:195], v[168:171], v[48:51]\n" \
"ds_read_b64_tr_b16 v[140:141], v5 offset:0\n" \
"ds_read_b64_tr_b16 v[142:143], v5 offset:1024\n" \
"s_waitcnt lgkmcnt(8)\n" \
"v_mfma_f32_16x16x32_bf16 v[52:55], v[180:183], v[172:175], v[52:55]\n" \
"v_add_u32 v6, s52, v6\n" \
"ds_read_b64_tr_b16 v[144:145], v6 offset:0\n" \
"global_load_lds_dwordx4 v14, s[46:47] offset:3072\n" \
"v_mfma_f32_16x16x32_bf16 v[56:59], v[184:187], v[172:175], v[56:59]\n" \
"ds_read_b64_tr_b16 v[146:147], v6 offset:1024\n" \
"ds_read_b128 v[120:123], v2 offset:2048\n" \
"v_mfma_f32_16x16x32_bf16 v[60:63], v[188:191], v[172:175], v[60:63]\n" \
"ds_read_b64_tr_b16 v[148:149], v3 offset:8192\n" \
"ds_read_b64_tr_b16 v[150:151], v3 offset:9216\n" \
"v_mfma_f32_16x16x32_bf16 v[64:67], v[192:195], v[172:175], v[64:67]\n" \
"s_add_u32 s46, s46, 131072\n" \
"s_addc_u32 s47, s47, 0\n" \
"ds_read_b128 v[124:127], v2 offset:1024\n" \
"s_waitcnt lgkmcnt(13)\n" \
"v_mfma_f32_16x16x32_bf16 v[68:71], v[180:183], v[176:179], v[68:71]\n" \
"ds_read_b64_tr_b16 v[152:153], v4 offset:8192\n" \
"ds_read_b64_tr_b16 v[154:155], v4 offset:9216\n" \
"ds_read_b64_tr_b16 v[156:157], v5 offset:8192\n" \
"v_mfma_f32_16x16x32_bf16 v[72:75], v[184:187], v[176:179], v[72:75]\n" \
"s_add_u32 s75, s75, 1\n" \
"s_cbranch_scc1 LWR%=_6\n" \
"LWS%=_6:\n" \
"ds_read_b64_tr_b16 v[158:159], v5 offset:9216\n" \
"v_mfma_f32_16x16x32_bf16 v[76:79], v[188:191], v[176:179], v[76:79]\n" \
"ds_read_b64_tr_b16 v[160:161], v6 offset:8192\n" \
"ds_read_b64_tr_b16 v[162:163], v6 offset:9216\n" \
"v_mfma_f32_16x16x32_bf16 v[80:83], v[192:195], v[176:179], v[80:83]\n" \
"ds_read_b128 v[128:131], v2 offset:3072\n" \
"s_mov_b32 s54, s55\n" \
"s_waitcnt vmcnt(12)\n" \
"s_barrier\n" \
K2_STAMP2 \
"s_nop 7\n" \
"global_store_dwordx4 v7, v[52:55], s[56:57] offset:0 sc1\n" \
"global_store_dwordx4 v7, v[56:59], s[56:57] offset:1024 sc1\n" \
"global_store_dwordx4 v7, v[60:63], s[56:57] offset:2048 sc1\n" \
"global_store_dwordx4 v7, v[64:67], s[56:57] offset:3072 sc1\n" \
"global_store_dwordx4 v8, v[68:71], s[56:57] offset:0 sc1\n" \
"global_store_dwordx4 v8, v[72:75], s[56:57] offset:1024 sc1\n" \
"global_store_dwordx4 v8, v[76:79], s[56:57] offset:2048 sc1\n" \
"global_store_dwordx4 v8, v[80:83], s[56:57] offset:3072 sc1\n" \
"s_nop 1\n" \
"s_waitcnt lgkmcnt(15)\n" \
"v_mfma_f32_16x16x32_bf16 v[84:87], v[132:135], v[116:119], v[84:87]\n" \
"s_add_u32 m0, s49, s54\n" \
"s_cmp_eq_u32 s51, 131072\n" \
"s_cselect_b32 s52, s53, 32768\n" \
"s_mov_b32 s55, s51\n" \
"s_waitcnt lgkmcnt(15)\n" \
"v_mfma_f32_16x16x32_bf16 v[88:91], v[136:139], v[116:119], v[88:91]\n" \
"s_add_u32 s51, s51, s52\n" \
"global_load_lds_dwordx4 v9, s[44:45] offset:0 sc1\n" \
"s_waitcnt lgkmcnt(13)\n" \
"v_mfma_f32_16x16x32_bf16 v[92:95], v[140:143], v[116:119], v[92:95]\n" \
"v_add_u32 v3, s52, v3\n" \
"ds_read_b64_tr_b16 v[180:181], v3 offset:0\n" \
"ds_read_b64_tr_b16 v[182:183], v3 offset:1024\n" \
"s_waitcnt lgkmcnt(13)\n" \
"v_mfma_f32_16x16x32_bf16 v[96:99], v[144:147], v[116:119], v[96:99]\n" \
"global_load_lds_dwordx4 v9, s[44:45] offset:1024 sc1\n" \
"v_add_u32 v2, s52, v2\n" \
"s_waitcnt lgkmcnt(12)\n" \
"v_mfma_f32_16x16x32_bf16 v[100:103], v[132:135], v[120:123], v[100:103]\n" \
"ds_read_b128 v[164:167], v2 offset:0\n" \
"s_add_u32 s44, s44, 2048\n" \
"s_addc_u32 s45, s45, 0\n" \
"v_add_u32 v4, s52, v4\n" \
"v_mfma_f32_16x16x32_bf16 v[104:107], v[136:139], v[120:123], v[104:107]\n" \
"ds_read_b64_tr_b16 v[184:185], v4 offset:0\n" \
"ds_read_b64_tr_b16 v[186:187], v4 offset:1024\n" \
"v_mfma_f32_16x16x32_bf16 v[108:111], v[140:143], v[120:123], v[108:111]\n" \
"s_add_u32 m0, s50, s54\n" \
"v_add_u32 v5, s52, v5\n" \
"ds_read_b64_tr_b16 v[188:189], v5 offset:0\n" \
"v_mfma_f32_16x16x32_bf16 v[112:115], v[144:147], v[120:123], v[112:115]\n" \
"global_load_lds_dwordx4 v11, s[46:47] offset:0\n" \
"ds_read_b64_tr_b16 v[190:191], v5 offset:1024\n" \
"s_waitcnt lgkmcnt(14)\n" \
"v_mfma_f32_16x16x32_bf16 v[84:87], v[148:151], v[124:127], v[84:87]\n" \
"v_add_u32 v6, s52, v6\n" \
"ds_read_b64_tr_b16 v[192:193], v6 offset:0\n" \
"global_load_lds_dwordx4 v12, s[46:47] offset:1024\n" \
"s_waitcnt lgkmcnt(13)\n" \
"v_mfma_f32_16x16x32_bf16 v[88:91], v[152:155], v[124:127], v[88:91]\n" \
"ds_read_b64_tr_b16 v[194:195], v6 offset:1024\n" \
"ds_read_b128 v[168:171], v2 offset:2048\n" \
"s_waitcnt lgkmcnt(13)\n" \
"v_mfma_f32_16x16x32_bf16 v[92:95], v[156:159], v[124:127], v[92:95]\n" \
"global_load_lds_dwordx4 v13, s[46:47] offset:2048\n" \
"ds_read_b64_tr_b16 v[196:197], v3 offset:8192\n" \
"ds_read_b64_tr_b16 v[198:199], v3 offset:9216\n" \
"s_waitcnt lgkmcnt(13)\n" \
"v_mfma_f32_16x16x32_bf16 v[96:99], v[160:163], v[124:127], v[96:99]\n" \
"ds_read_b128 v[172:175], v2 offset:1024\n" \
"global_load_lds_dwordx4 v14, s[46:47] offset:3072\n" \
"s_waitcnt lgkmcnt(13)\n" \
"v_mfma_f32_16x16x32_bf16 v[100:103], v[148:151], v[128:131], v[100:103]\n" \
"ds_read_b64_tr_b16 v[200:201], v4 offset:8192\n" \
"ds_read_b64_tr_b16 v[202:203], v4 offset:9216\n" \
"s_add_u32 s46, s46, 131072\n" \
"s_addc_u32 s47, s47, 0\n" \
"v_mfma_f32_16x16x32_bf16 v[104:107], v[152:155], v[128:131], v[104:107]\n" \
"ds_read_b64_tr_b16 v[204:205], v5 offset:8192\n" \
"ds_read_b64_tr_b16 v[206:207], v5 offset:9216\n" \
"v_mfma_f32_16x16x32_bf16 v[108:111], v[156:159], v[128:131], v[108:111]\n" \
"ds_read_b64_tr_b16 v[208:209], v6 offset:8192\n" \
"s_add_u32 s75, s75, 1\n" \
"s_cbranch_scc1 LWR%=_7\n" \
"LWS%=_7:\n" \
"ds_read_b64_tr_b16 v[210:211], v6 offset:9216\n" \
"v_mfma_f32_16x16x32_bf16 v[112:115], v[160:163], v[128:131], v[112:115]\n" \
"ds_read_b128 v[176:179], v2 offset:3072\n" \
"s_mov_b32 s54, s55\n" \
"s_waitcnt vmcnt(20)\n" \
"s_barrier\n" \
"s_waitcnt lgkmcnt(15)\n" \
"v_mfma_f32_16x16x32_bf16 v[84:87], v[180:183], v[164:167], v[84:87]\n" \
"s_add_u32 m0, s49, s54\n" \
"s_cmp_eq_u32 s51, 131072\n" \
"s_cselect_b32 s52, s53, 32768\n" \
"s_mov_b32 s55, s51\n" \
"s_waitcnt lgkmcnt(15)\n" \
"v_mfma_f32_16x16x32_bf16 v[88:91], v[184:187], v[164:167], v[88:91]\n" \
"s_add_u32 s51, s51, s52\n" \
"global_load_lds_dwordx4 v9, s[44:45] offset:0 sc1\n" \
"s_waitcnt lgkmcnt(13)\n" \
"v_mfma_f32_16x16x32_bf16 v[92:95], v[188:191], v[164:167], v[92:95]\n" \
"v_add_u32 v3, s52, v3\n" \
"ds_read_b64_tr_b16 v[132:133], v3 offset:0\n" \
"ds_read_b64_tr_b16 v[134:135], v3 offset:1024\n" \
"s_waitcnt lgkmcnt(13)\n" \
"v_mfma_f32_16x16x32_bf16 v[96:99], v[192:195], v[164:167], v[96:99]\n" \
"global_load_lds_dwordx4 v9, s[44:45] offset:1024 sc1\n" \
"v_add_u32 v2, s52, v2\n" \
"s_waitcnt lgkmcnt(12)\n" \
"v_mfma_f32_16x16x32_bf16 v[100:103], v[180:183], v[168:171], v[100:103]\n" \
"ds_read_b128 v[116:119], v2 offset:0\n" \
"s_add_u32 s44, s44, 2048\n" \
"s_addc_u32 s45, s45, 0\n" \
"v_add_u32 v4, s52, v4\n" \
"v_mfma_f32_16x16x32_bf16 v[104:107], v[184:187], v[168:171], v[104:107]\n" \
"ds_read_b64_tr_b16 v[136:137], v4 offset:0\n" \
"ds_read_b64_tr_b16 v[138:139], v4 offset:1024\n" \
"v_mfma_f32_16x16x32_bf16 v[108:111], v[188:191], v[168:171], v[108:111]\n" \
"s_add_u32 m0, s50, s54\n" \
"v_add_u32 v5, s52, v5\n" \
"ds_read_b64_tr_b16 v[140:141], v5 offset:0\n" \
"v_mfma_f32_16x16x32_bf16 v[112:115], v[192:195], v[168:171], v[112:115]\n" \
"global_load_lds_dwordx4 v11, s[46:47] offset:0\n" \
"ds_read_b64_tr_b16 v[142:143], v5 offset:1024\n" \
"s_waitcnt lgkmcnt(14)\n" \
"v_mfma_f32_16x16x32_bf16 v[84:87], v[196:199], v[172:175], v[84:87]\n" \
"v_add_u32 v6, s52, v6\n" \
"ds_read_b64_tr_b16 v[144:145], v6 offset:0\n" \
"global_load_lds_dwordx4 v12, s[46:47] offset:1024\n" \
"s_waitcnt lgkmcnt(13)\n" \
"v_mfma_f32_16x16x32_bf16 v[88:91], v[200:203], v[172:175], v[88:91]\n" \
"ds_read_b64_tr_b16 v[146:147], v6 offset:1024\n" \
"ds_read_b128 v[120:123], v2 offset:2048\n" \
"s_waitcnt lgkmcnt(13)\n" \
"v_mfma_f32_16x16x32_bf16 v[92:95], v[204:207], v[172:175], v[92:95]\n" \
"global_load_lds_dwordx4 v13, s[46:47] offset:2048\n" \
"ds_read_b64_tr_b16 v[148:149], v3 offset:8192\n" \
"ds_read_b64_tr_b16 v[150:151], v3 offset:9216\n" \
"s_waitcnt lgkmcnt(13)\n" \
"v_mfma_f32_16x16x32_bf16 v[96:99], v[208:211], v[172:175], v[96:99]\n" \
"ds_read_b128 v[124:127], v2 offset:1024\n" \
"global_load_lds_dwordx4 v14, s[46:47] offset:3072\n" \
"s_waitcnt lgkmcnt(13)\n" \
"v_mfma_f32_16x16x32_bf16 v[100:103], v[196:199], v[176:179], v[100:103]\n" \
"ds_read_b64_tr_b16 v[152:153], v4 offset:8192\n" \
"ds_read_b64_tr_b16 v[154:155], v4 offset:9216\n" \
"s_add_u32 s46, s46, 131072\n" \
"s_addc_u32 s47, s47, 0\n" \
"v_mfma_f32_16x16x32_bf16 v[104:107], v[200:203], v[176:179], v[104:107]\n" \
"ds_read_b64_tr_b16 v[156:157], v5 offset:8192\n" \
"ds_read_b64_tr_b16 v[158:159], v5 offset:9216\n" \
"v_mfma_f32_16x16x32_bf16 v[108:111], v[204:207], v[176:179], v[108:111]\n" \
"ds_read_b64_tr_b16 v[160:161], v6 offset:8192\n" \
"s_add_u32 s75, s75, 1\n" \
"s_cbranch_scc1 LWR%=_8\n" \
"LWS%=_8:\n" \
"ds_read_b64_tr_b16 v[162:163], v6 offset:9216\n" \
"v_mfma_f32_16x16x32_bf16 v[112:115], v[208:211], v[176:179], v[112:115]\n" \
"ds_read_b128 v[128:131], v2 offset:3072\n" \
"s_mov_b32 s54, s55\n" \
"s_waitcnt vmcnt(12)\n" \
"s_barrier\n" \
"s_waitcnt lgkmcnt(15)\n" \
"v_mfma_f32_16x16x32_bf16 v[84:87], v[132:135], v[116:119], v[84:87]\n" \
"s_add_u32 m0, s49, s54\n" \
"s_cmp_eq_u32 s51, 131072\n" \
"s_cselect_b32 s52, s53, 32768\n" \
"s_mov_b32 s55, s51\n" \
"s_waitcnt lgkmcnt(15)\n" \
"v_mfma_f32_16x16x32_bf16 v[88:91], v[136:139], v[116:119], v[88:91]\n" \
"s_add_u32 s51, s51, s52\n" \
"global_load_lds_dwordx4 v9, s[44:45] offset:0 sc1\n" \
"s_cmp_eq_u32 s65, 0\n" \
"s_cbranch_scc1 LF%=\n" \
"v_mov_b32 v16, s68\n" \
"v_mov_b32 v17, s69\n" \
"v_mov_b32 v19, s62\n" \
"s_mov_b64 exec, 1\n" \
"global_store_dwordx2 v19, v[16:17], s[60:61] sc1\n" \
"s_mov_b64 exec, -1\n" \
"LF%=:\n" \
"s_waitcnt lgkmcnt(13)\n" \
"v_mfma_f32_16x16x32_bf16 v[92:95], v[140:143], v[116:119], v[92:95]\n" \
"v_add_u32 v3, s52, v3\n" \
"ds_read_b64_tr_b16 v[180:181], v3 offset:0\n" \
"ds_read_b64_tr_b16 v[182:183], v3 offset:1024\n" \
"s_waitcnt lgkmcnt(13)\n" \
"v_mfma_f32_16x16x32_bf16 v[96:99], v[144:147], v[116:119], v[96:99]\n" \
"global_load_lds_dwordx4 v9, s[44:45] offset:1024 sc1\n" \
"v_add_u32 v2, s52, v2\n" \
"s_waitcnt lgkmcnt(12)\n" \
"v_mfma_f32_16x16x32_bf16 v[100:103], v[132:135], v[120:123], v[100:103]\n" \
"ds_read_b128 v[164:167], v2 offset:0\n" \
"s_add_u32 s44, s44, 2048\n" \
"s_addc_u32 s45, s45, 0\n" \
"v_add_u32 v4, s52, v4\n" \
"v_mfma_f32_16x16x32_bf16 v[104:107], v[136:139], v[120:123], v[104:107]\n" \
"ds_read_b64_tr_b16 v[184:185], v4 offset:0\n" \
"ds_read_b64_tr_b16 v[186:187], v4 offset:1024\n" \
"v_mfma_f32_16x16x32_bf16 v[108:111], v[140:143], v[120:123], v[108:111]\n" \
"s_add_u32 m0, s50, s54\n" \
"v_add_u32 v5, s52, v5\n" \
"ds_read_b64_tr_b16 v[188:189], v5 offset:0\n" \
"v_mfma_f32_16x16x32_bf16 v[112:115], v[144:147], v[120:123], v[112:115]\n" \
"global_load_lds_dwordx4 v11, s[46:47] offset:0\n" \
"ds_read_b64_tr_b16 v[190:191], v5 offset:1024\n" \
"s_waitcnt lgkmcnt(14)\n" \
"v_mfma_f32_16x16x32_bf16 v[84:87], v[148:151], v[124:127], v[84:87]\n" \
"v_add_u32 v6, s52, v6\n" \
"ds_read_b64_tr_b16 v[192:193], v6 offset:0\n" \
"global_load_lds_dwordx4 v12, s[46:47] offset:1024\n" \
"s_waitcnt lgkmcnt(13)\n" \
"v_mfma_f32_16x16x32_bf16 v[88:91], v[152:155], v[124:127], v[88:91]\n" \
"ds_read_b64_tr_b16 v[194:195], v6 offset:1024\n" \
"ds_read_b128 v[168:171], v2 offset:2048\n" \
"s_waitcnt lgkmcnt(13)\n" \
"v_mfma_f32_16x16x32_bf16 v[92:95], v[156:159], v[124:127], v[92:95]\n" \
"global_load_lds_dwordx4 v13, s[46:47] offset:2048\n" \
"ds_read_b64_tr_b16 v[196:197], v3 offset:8192\n" \
"ds_read_b64_tr_b16 v[198:199], v3 offset:9216\n" \
"s_waitcnt lgkmcnt(13)\n" \
"v_mfma_f32_16x16x32_bf16 v[96:99], v[160:163], v[124:127], v[96:99]\n" \
"ds_read_b128 v[172:175], v2 offset:1024\n" \
"global_load_lds_dwordx4 v14, s[46:47] offset:3072\n" \
"s_waitcnt lgkmcnt(13)\n" \
"v_mfma_f32_16x16x32_bf16 v[100:103], v[148:151], v[128:131], v[100:103]\n" \
"ds_read_b64_tr_b16 v[200:201], v4 offset:8192\n" \
"ds_read_b64_tr_b16 v[202:203], v4 offset:9216\n" \
"s_add_u32 s46, s46, 131072\n" \
"s_addc_u32 s47, s47, 0\n" \
"v_mfma_f32_16x16x32_bf16 v[104:107], v[152:155], v[128:131], v[104:107]\n" \
"ds_read_b64_tr_b16 v[204:205], v5 offset:8192\n" \
"ds_read_b64_tr_b16 v[206:207], v5 offset:9216\n" \
"v_mfma_f32_16x16x32_bf16 v[108:111], v[156:159], v[128:131], v[108:111]\n" \
"ds_read_b64_tr_b16 v[208:209], v6 offset:8192\n" \
"s_add_u32 s75, s75, 1\n" \
"s_cbranch_scc1 LWR%=_9\n" \
"LWS%=_9:\n" \
"ds_read_b64_tr_b16 v[210:211], v6 offset:9216\n" \
"v_mfma_f32_16x16x32_bf16 v[112:115], v[160:163], v[128:131], v[112:115]\n" \
"ds_read_b128 v[176:179], v2 offset:3072\n" \
"s_mov_b32 s54, s55\n" \
"s_waitcnt vmcnt(12)\n" \
"s_barrier\n" \
"s_waitcnt lgkmcnt(15)\n" \
"v_mfma_f32_16x16x32_bf16 v[84:87], v[180:183], v[164:167], v[84:87]\n" \
"s_add_u32 m0, s49, s54\n" \
"s_cmp_eq_u32 s51, 131072\n" \
"s_cselect_b32 s52, s53, 32768\n" \
"s_mov_b32 s55, s51\n" \
"s_waitcnt lgkmcnt(15)\n" \
"v_mfma_f32_16x16x32_bf16 v[88:91], v[184:187], v[164:167], v[88:91]\n" \
"s_add_u32 s51, s51, s52\n" \
"global_load_lds_dwordx4 v9, s[44:45] offset:0 sc1\n" \
"s_waitcnt lgkmcnt(13)\n" \
"v_mfma_f32_16x16x32_bf16 v[92:95], v[188:191], v[164:167], v[92:95]\n" \
"v_add_u32 v3, s52, v3\n" \
"ds_read_b64_tr_b16 v[132:133], v3 offset:0\n" \
"ds_read_b64_tr_b16 v[134:135], v3 offset:1024\n" \
"s_waitcnt lgkmcnt(13)\n" \
"v_mfma_f32_16x16x32_bf16 v[96:99], v[192:195], v[164:167], v[96:99]\n" \
"global_load_lds_dwordx4 v9, s[44:45] offset:1024 sc1\n" \
"v_add_u32 v2, s52, v2\n" \
"s_waitcnt lgkmcnt(12)\n" \
"v_mfma_f32_16x16x32_bf16 v[100:103], v[180:183], v[168:171], v[100:103]\n" \
"ds_read_b128 v[116:119], v2 offset:0\n" \
"s_add_u32 s44, s44, 2048\n" \
"s_addc_u32 s45, s45, 0\n" \
"v_add_u32 v4, s52, v4\n" \
"v_mfma_f32_16x16x32_bf16 v[104:107], v[184:187], v[168:171], v[104:107]\n" \
"ds_read_b64_tr_b16 v[136:137], v4 offset:0\n" \
"ds_read_b64_tr_b16 v[138:139], v4 offset:1024\n" \
"v_mfma_f32_16x16x32_bf16 v[108:111], v[188:191], v[168:171], v[108:111]\n" \
"s_add_u32 m0, s50, s54\n" \
"v_add_u32 v5, s52, v5\n" \
"ds_read_b64_tr_b16 v[140:141], v5 offset:0\n" \
"v_mfma_f32_16x16x32_bf16 v[112:115], v[192:195], v[168:171], v[112:115]\n" \
"global_load_lds_dwordx4 v11, s[46:47] offset:0\n" \
"ds_read_b64_tr_b16 v[142:143], v5 offset:1024\n" \
"s_waitcnt lgkmcnt(14)\n" \
"v_mfma_f32_16x16x32_bf16 v[84:87], v[196:199], v[172:175], v[84:87]\n" \
"v_add_u32 v6, s52, v6\n" \
"ds_read_b64_tr_b16 v[144:145], v6 offset:0\n" \
"global_load_lds_dwordx4 v12, s[46:47] offset:1024\n" \
"s_waitcnt lgkmcnt(13)\n" \
"v_mfma_f32_16x16x32_bf16 v[88:91], v[200:203], v[172:175], v[88:91]\n" \
"ds_read_b64_tr_b16 v[146:147], v6 offset:1024\n" \
"ds_read_b128 v[120:123], v2 offset:2048\n" \
"s_waitcnt lgkmcnt(13)\n" \
"v_mfma_f32_16x16x32_bf16 v[92:95], v[204:207], v[172:175], v[92:95]\n" \
"global_load_lds_dwordx4 v13, s[46:47] offset:2048\n" \
"ds_read_b64_tr_b16 v[148:149], v3 offset:8192\n" \
"ds_read_b64_tr_b16 v[150:151], v3 offset:9216\n" \
"s_waitcnt lgkmcnt(13)\n" \
"v_mfma_f32_16x16x32_bf16 v[96:99], v[208:211], v[172:175], v[96:99]\n" \
"ds_read_b128 v[124:127], v2 offset:1024\n" \
"global_load_lds_dwordx4 v14, s[46:47] offset:3072\n" \
"s_waitcnt lgkmcnt(13)\n" \
"v_mfma_f32_16x16x32_bf16 v[100:103], v[196:199], v[176:179], v[100:103]\n" \
"ds_read_b64_tr_b16 v[152:153], v4 offset:8192\n" \
"ds_read_b64_tr_b16 v[154:155], v4 offset:9216\n" \
"s_add_u32 s46, s46, 131072\n" \
"s_addc_u32 s47, s47, 0\n" \
"v_mfma_f32_16x16x32_bf16 v[104:107], v[200:203], v[176:179], v[104:107]\n" \
"ds_read_b64_tr_b16 v[156:157], v5 offset:8192\n" \
"ds_read_b64_tr_b16 v[158:159], v5 offset:9216\n" \
"v_mfma_f32_16x16x32_bf16 v[108:111], v[204:207], v[176:179], v[108:111]\n" \
"ds_read_b64_tr_b16 v[160:161], v6 offset:8192\n" \
"s_add_u32 s75, s75, 1\n" \
"s_cbranch_scc1 LWR%=_10\n" \
"LWS%=_10:\n" \
"ds_read_b64_tr_b16 v[162:163], v6 offset:9216\n" \
"v_mfma_f32_16x16x32_bf16 v[112:115], v[208:211], v[176:179], v[112:115]\n" \
"ds_read_b128 v[128:131], v2 offset:3072\n" \
"s_mov_b32 s54, s55\n" \
"s_waitcnt vmcnt(12)\n" \
"s_barrier\n" \
"s_waitcnt lgkmcnt(15)\n" \
"v_mfma_f32_16x16x32_bf16 v[84:87], v[132:135], v[116:119], v[84:87]\n" \
"s_add_u32 m0, s49, s54\n" \
"s_cmp_eq_u32 s51, 131072\n" \
"s_cselect_b32 s52, s53, 32768\n" \
"s_mov_b32 s55, s51\n" \
"s_waitcnt lgkmcnt(15)\n" \
"v_mfma_f32_16x16x32_bf16 v[88:91], v[136:139], v[116:119], v[88:91]\n" \
"s_add_u32 s51, s51, s52\n" \
"global_load_lds_dwordx4 v9, s[44:45] offset:0 sc1\n" \
"s_waitcnt lgkmcnt(13)\n" \
"v_mfma_f32_16x16x32_bf16 v[92:95], v[140:143], v[116:119], v[92:95]\n" \
"v_add_u32 v3, s52, v3\n" \
"ds_read_b64_tr_b16 v[180:181], v3 offset:0\n" \
"ds_read_b64_tr_b16 v[182:183], v3 offset:1024\n" \
"s_waitcnt lgkmcnt(13)\n" \
"v_mfma_f32_16x16x32_bf16 v[96:99], v[144:147], v[116:119], v[96:99]\n" \
"global_load_lds_dwordx4 v9, s[44:45] offset:1024 sc1\n" \
"v_add_u32 v2, s52, v2\n" \
"s_waitcnt lgkmcnt(12)\n" \
"v_mfma_f32_16x16x32_bf16 v[100:103], v[132:135], v[120:123], v[100:103]\n" \
"ds_read_b128 v[164:167], v2 offset:0\n" \
"s_add_u32 s44, s44, 2048\n" \
"s_addc_u32 s45, s45, 0\n" \
"v_add_u32 v4, s52, v4\n" \
"v_mfma_f32_16x16x32_bf16 v[104:107], v[136:139], v[120:123], v[104:107]\n" \
"ds_read_b64_tr_b16 v[184:185], v4 offset:0\n" \
"ds_read_b64_tr_b16 v[186:187], v4 offset:1024\n" \
"v_mfma_f32_16x16x32_bf16 v[108:111], v[140:143], v[120:123], v[108:111]\n" \
"s_add_u32 m0, s50, s54\n" \
"v_add_u32 v5, s52, v5\n" \
"ds_read_b64_tr_b16 v[188:189], v5 offset:0\n" \
"v_mfma_f32_16x16x32_bf16 v[112:115], v[144:147], v[120:123], v[112:115]\n" \
"global_load_lds_dwordx4 v11, s[46:47] offset:0\n" \
"ds_read_b64_tr_b16 v[190:191], v5 offset:1024\n" \
"s_waitcnt lgkmcnt(14)\n" \
"v_mfma_f32_16x16x32_bf16 v[84:87], v[148:151], v[124:127], v[84:87]\n" \
"v_add_u32 v6, s52, v6\n" \
"ds_read_b64_tr_b16 v[192:193], v6 offset:0\n" \
"global_load_lds_dwordx4 v12, s[46:47] offset:1024\n" \
"s_waitcnt lgkmcnt(13)\n" \
"v_mfma_f32_16x16x32_bf16 v[88:91], v[152:155], v[124:127], v[88:91]\n" \
"ds_read_b64_tr_b16 v[194:195], v6 offset:1024\n" \
"ds_read_b128 v[168:171], v2 offset:2048\n" \
"s_waitcnt lgkmcnt(13)\n" \
"v_mfma_f32_16x16x32_bf16 v[92:95], v[156:159], v[124:127], v[92:95]\n" \
"global_load_lds_dwordx4 v13, s[46:47] offset:2048\n" \
"ds_read_b64_tr_b16 v[196:197], v3 offset:8192\n" \
"ds_read_b64_tr_b16 v[198:199], v3 offset:9216\n" \
"s_waitcnt lgkmcnt(13)\n" \
"v_mfma_f32_16x16x32_bf16 v[96:99], v[160:163], v[124:127], v[96:99]\n" \
"ds_read_b128 v[172:175], v2 offset:1024\n" \
"global_load_lds_dwordx4 v14, s[46:47] offset:3072\n" \
"s_waitcnt lgkmcnt(13)\n" \
"v_mfma_f32_16x16x32_bf16 v[100:103], v[148:151], v[128:131], v[100:103]\n" \
"ds_read_b64_tr_b16 v[200:201], v4 offset:8192\n" \
"ds_read_b64_tr_b16 v[202:203], v4 offset:9216\n" \
"s_add_u32 s46, s46, 131072\n" \
"s_addc_u32 s47, s47, 0\n" \
"v_mfma_f32_16x16x32_bf16 v[104:107], v[152:155], v[128:131], v[104:107]\n" \
"ds_read_b64_tr_b16 v[204:205], v5 offset:8192\n" \
"ds_read_b64_tr_b16 v[206:207], v5 offset:9216\n" \
"v_mfma_f32_16x16x32_bf16 v[108:111], v[156:159], v[128:131], v[108:111]\n" \
"ds_read_b64_tr_b16 v[208:209], v6 offset:8192\n" \
"s_add_u32 s75, s75, 1\n" \
"s_cbranch_scc1 LWR%=_11\n" \
"LWS%=_11:\n" \
"ds_read_b64_tr_b16 v[210:211], v6 offset:9216\n" \
"v_mfma_f32_16x16x32_bf16 v[112:115], v[160:163], v[128:131], v[112:115]\n" \
"ds_read_b128 v[176:179], v2 offset:3072\n" \
"s_mov_b32 s54, s55\n" \
"s_waitcnt vmcnt(12)\n" \
"s_barrier\n" \
"s_waitcnt lgkmcnt(15)\n" \
"v_mfma_f32_16x16x32_bf16 v[84:87], v[180:183], v[164:167], v[84:87]\n" \
"s_add_u32 m0, s49, s54\n" \
"s_cmp_eq_u32 s51, 131072\n" \
"s_cselect_b32 s52, s53, 32768\n" \
"s_mov_b32 s55, s51\n" \
"s_waitcnt lgkmcnt(15)\n" \
"v_mfma_f32_16x16x32_bf16 v[88:91], v[184:187], v[164:167], v[88:91]\n" \
"s_add_u32 s51, s51, s52\n" \
"global_load_lds_dwordx4 v9, s[44:45] offset:0 sc1\n" \
"s_waitcnt lgkmcnt(13)\n" \
"v_mfma_f32_16x16x32_bf16 v[92:95], v[188:191], v[164:167], v[92:95]\n" \
"v_add_u32 v3, s52, v3\n" \
"ds_read_b64_tr_b16 v[132:133], v3 offset:0\n" \
"ds_read_b64_tr_b16 v[134:135], v3 offset:1024\n" \
"s_waitcnt lgkmcnt(13)\n" \
"v_mfma_f32_16x16x32_bf16 v[96:99], v[192:195], v[164:167], v[96:99]\n" \
"global_load_lds_dwordx4 v9, s[44:45] offset:1024 sc1\n" \
"v_add_u32 v2, s52, v2\n" \
"s_waitcnt lgkmcnt(12)\n" \
"v_mfma_f32_16x16x32_bf16 v[100:103], v[180:183], v[168:171], v[100:103]\n" \
"ds_read_b128 v[116:119], v2 offset:0\n" \
"s_add_u32 s44, s44, 2048\n" \
"s_addc_u32 s45, s45, 0\n" \
"v_add_u32 v4, s52, v4\n" \
"v_mfma_f32_16x16x32_bf16 v[104:107], v[184:187], v[168:171], v[104:107]\n" \
"ds_read_b64_tr_b16 v[136:137], v4 offset:0\n" \
"ds_read_b64_tr_b16 v[138:139], v4 offset:1024\n" \
"v_mfma_f32_16x16x32_bf16 v[108:111], v[188:191], v[168:171], v[108:111]\n" \
"s_add_u32 m0, s50, s54\n" \
"v_add_u32 v5, s52, v5\n" \
"ds_read_b64_tr_b16 v[140:141], v5 offset:0\n" \
"v_mfma_f32_16x16x32_bf16 v[112:115], v[192:195], v[168:171], v[112:115]\n" \
"global_load_lds_dwordx4 v11, s[46:47] offset:0\n" \
"ds_read_b64_tr_b16 v[142:143], v5 offset:1024\n" \
"s_waitcnt lgkmcnt(14)\n" \
"v_mfma_f32_16x16x32_bf16 v[84:87], v[196:199], v[172:175], v[84:87]\n" \
"v_add_u32 v6, s52, v6\n" \
"ds_read_b64_tr_b16 v[144:145], v6 offset:0\n" \
"global_load_lds_dwordx4 v12, s[46:47] offset:1024\n" \
"s_waitcnt lgkmcnt(13)\n" \
"v_mfma_f32_16x16x32_bf16 v[88:91], v[200:203], v[172:175], v[88:91]\n" \
"ds_read_b64_tr_b16 v[146:147], v6 offset:1024\n" \
"ds_read_b128 v[120:123], v2 offset:2048\n" \
"s_waitcnt lgkmcnt(13)\n" \
"v_mfma_f32_16x16x32_bf16 v[92:95], v[204:207], v[172:175], v[92:95]\n" \
"global_load_lds_dwordx4 v13, s[46:47] offset:2048\n" \
"ds_read_b64_tr_b16 v[148:149], v3 offset:8192\n" \
"ds_read_b64_tr_b16 v[150:151], v3 offset:9216\n" \
"s_waitcnt lgkmcnt(13)\n" \
"v_mfma_f32_16x16x32_bf16 v[96:99], v[208:211], v[172:175], v[96:99]\n" \
"ds_read_b128 v[124:127], v2 offset:1024\n" \
"global_load_lds_dwordx4 v14, s[46:47] offset:3072\n" \
"s_waitcnt lgkmcnt(13)\n" \
"v_mfma_f32_16x16x32_bf16 v[100:103], v[196:199], v[176:179], v[100:103]\n" \
"ds_read_b64_tr_b16 v[152:153], v4 offset:8192\n" \
"ds_read_b64_tr_b16 v[154:155], v4 offset:9216\n" \
"s_add_u32 s46, s46, 131072\n" \
"s_addc_u32 s47, s47, 0\n" \
"v_mfma_f32_16x16x32_bf16 v[104:107], v[200:203], v[176:179], v[104:107]\n" \
"ds_read_b64_tr_b16 v[156:157], v5 offset:8192\n" \
"ds_read_b64_tr_b16 v[158:159], v5 offset:9216\n" \
"v_mfma_f32_16x16x32_bf16 v[108:111], v[204:207], v[176:179], v[108:111]\n" \
"ds_read_b64_tr_b16 v[160:161], v6 offset:8192\n" \
"s_add_u32 s75, s75, 1\n" \
"s_cbranch_scc1 LWR%=_12\n" \
"LWS%=_12:\n" \
"ds_read_b64_tr_b16 v[162:163], v6 offset:9216\n" \
"v_mfma_f32_16x16x32_bf16 v[112:115], v[208:211], v[176:179], v[112:115]\n" \
"ds_read_b128 v[128:131], v2 offset:3072\n" \
"s_mov_b32 s54, s55\n" \
"s_waitcnt vmcnt(12)\n" \
"s_barrier\n" \
"s_waitcnt lgkmcnt(15)\n" \
"v_mfma_f32_16x16x32_bf16 v[84:87], v[132:135], v[116:119], v[84:87]\n" \
"s_add_u32 m0, s49, s54\n" \
"s_cmp_eq_u32 s51, 131072\n" \
"s_cselect_b32 s52, s53, 32768\n" \
"s_mov_b32 s55, s51\n" \
"s_waitcnt lgkmcnt(15)\n" \
"v_mfma_f32_16x16x32_bf16 v[88:91], v[136:139], v[116:119], v[88:91]\n" \
"s_add_u32 s51, s51, s52\n" \
"global_load_lds_dwordx4 v9, s[44:45] offset:0 sc1\n" \
"s_waitcnt lgkmcnt(13)\n" \
"v_mfma_f32_16x16x32_bf16 v[92:95], v[140:143], v[116:119], v[92:95]\n" \
"v_add_u32 v3, s52, v3\n" \
"ds_read_b64_tr_b16 v[180:181], v3 offset:0\n" \
"ds_read_b64_tr_b16 v[182:183], v3 offset:1024\n" \
"s_waitcnt lgkmcnt(13)\n" \
"v_mfma_f32_16x16x32_bf16 v[96:99], v[144:147], v[116:119], v[96:99]\n" \
"global_load_lds_dwordx4 v9, s[44:45] offset:1024 sc1\n" \
"v_add_u32 v2, s52, v2\n" \
"s_waitcnt lgkmcnt(12)\n" \
"v_mfma_f32_16x16x32_bf16 v[100:103], v[132:135], v[120:123], v[100:103]\n" \
"ds_read_b128 v[164:167], v2 offset:0\n" \
"s_add_u32 s44, s44, 2048\n" \
"s_addc_u32 s45, s45, 0\n" \
"v_add_u32 v4, s52, v4\n" \
"v_mfma_f32_16x16x32_bf16 v[104:107], v[136:139], v[120:123], v[104:107]\n" \
"ds_read_b64_tr_b16 v[184:185], v4 offset:0\n" \
"ds_read_b64_tr_b16 v[186:187], v4 offset:1024\n" \
"v_mfma_f32_16x16x32_bf16 v[108:111], v[140:143], v[120:123], v[108:111]\n" \
"s_add_u32 m0, s50, s54\n" \
"v_add_u32 v5, s52, v5\n" \
"ds_read_b64_tr_b16 v[188:189], v5 offset:0\n" \
"v_mfma_f32_16x16x32_bf16 v[112:115], v[144:147], v[120:123], v[112:115]\n" \
"global_load_lds_dwordx4 v11, s[46:47] offset:0\n" \
"ds_read_b64_tr_b16 v[190:191], v5 offset:1024\n" \
"s_waitcnt lgkmcnt(14)\n" \
"v_mfma_f32_16x16x32_bf16 v[84:87], v[148:151], v[124:127], v[84:87]\n" \
"v_add_u32 v6, s52, v6\n" \
"ds_read_b64_tr_b16 v[192:193], v6 offset:0\n" \
"global_load_lds_dwordx4 v12, s[46:47] offset:1024\n" \
"s_waitcnt lgkmcnt(13)\n" \
"v_mfma_f32_16x16x32_bf16 v[88:91], v[152:155], v[124:127], v[88:91]\n" \
"ds_read_b64_tr_b16 v[194:195], v6 offset:1024\n" \
"ds_read_b128 v[168:171], v2 offset:2048\n" \
"s_waitcnt lgkmcnt(13)\n" \
"v_mfma_f32_16x16x32_bf16 v[92:95], v[156:159], v[124:127], v[92:95]\n" \
"global_load_lds_dwordx4 v13, s[46:47] offset:2048\n" \
"ds_read_b64_tr_b16 v[196:197], v3 offset:8192\n" \
"ds_read_b64_tr_b16 v[198:199], v3 offset:9216\n" \
"s_waitcnt lgkmcnt(13)\n" \
"v_mfma_f32_16x16x32_bf16 v[96:99], v[160:163], v[124:127], v[96:99]\n" \
"ds_read_b128 v[172:175], v2 offset:1024\n" \
"global_load_lds_dwordx4 v14, s[46:47] offset:3072\n" \
"s_waitcnt lgkmcnt(13)\n" \
"v_mfma_f32_16x16x32_bf16 v[100:103], v[148:151], v[128:131], v[100:103]\n" \
"ds_read_b64_tr_b16 v[200:201], v4 offset:8192\n" \
"ds_read_b64_tr_b16 v[202:203], v4 offset:9216\n" \
"s_add_u32 s46, s46, 131072\n" \
"s_addc_u32 s47, s47, 0\n" \
"v_mfma_f32_16x16x32_bf16 v[104:107], v[152:155], v[128:131], v[104:107]\n" \
"ds_read_b64_tr_b16 v[204:205], v5 offset:8192\n" \
"ds_read_b64_tr_b16 v[206:207], v5 offset:9216\n" \
"v_mfma_f32_16x16x32_bf16 v[108:111], v[156:159], v[128:131], v[108:111]\n" \
"ds_read_b64_tr_b16 v[208:209], v6 offset:8192\n" \
"s_add_u32 s75, s75, 1\n" \
"s_cbranch_scc1 LWR%=_13\n" \
"LWS%=_13:\n" \
"ds_read_b64_tr_b16 v[210:211], v6 offset:9216\n" \
"v_mfma_f32_16x16x32_bf16 v[112:115], v[160:163], v[128:131], v[112:115]\n" \
"ds_read_b128 v[176:179], v2 offset:3072\n" \
"s_mov_b32 s54, s55\n" \
"s_waitcnt vmcnt(12)\n" \
"s_barrier\n" \
"s_waitcnt lgkmcnt(15)\n" \
"v_mfma_f32_16x16x32_bf16 v[84:87], v[180:183], v[164:167], v[84:87]\n" \
"s_add_u32 m0, s49, s54\n" \
"s_cmp_eq_u32 s51, 131072\n" \
"s_cselect_b32 s52, s53, 32768\n" \
"s_mov_b32 s55, s51\n" \
"s_waitcnt lgkmcnt(15)\n" \
"v_mfma_f32_16x16x32_bf16 v[88:91], v[184:187], v[164:167], v[88:91]\n" \
"s_add_u32 s51, s51, s52\n" \
"global_load_lds_dwordx4 v9, s[44:45] offset:0 sc1\n" \
"s_waitcnt lgkmcnt(13)\n" \
"v_mfma_f32_16x16x32_bf16 v[92:95], v[188:191], v[164:167], v[92:95]\n" \
"v_add_u32 v3, s52, v3\n" \
"ds_read_b64_tr_b16 v[132:133], v3 offset:0\n" \
"ds_read_b64_tr_b16 v[134:135], v3 offset:1024\n" \
"s_waitcnt lgkmcnt(13)\n" \
"v_mfma_f32_16x16x32_bf16 v[96:99], v[192:195], v[164:167], v[96:99]\n" \
"global_load_lds_dwordx4 v9, s[44:45] offset:1024 sc1\n" \
"v_add_u32 v2, s52, v2\n" \
"s_waitcnt lgkmcnt(12)\n" \
"v_mfma_f32_16x16x32_bf16 v[100:103], v[180:183], v[168:171], v[100:103]\n" \
"ds_read_b128 v[116:119], v2 offset:0\n" \
"s_add_u32 s44, s44, 2048\n" \
"s_addc_u32 s45, s45, 0\n" \
"v_add_u32 v4, s52, v4\n" \
"v_mfma_f32_16x16x32_bf16 v[104:107], v[184:187], v[168:171], v[104:107]\n" \
"ds_read_b64_tr_b16 v[136:137], v4 offset:0\n" \
"ds_read_b64_tr_b16 v[138:139], v4 offset:1024\n" \
"v_mfma_f32_16x16x32_bf16 v[108:111], v[188:191], v[168:171], v[108:111]\n" \
"s_add_u32 m0, s50, s54\n" \
"v_add_u32 v5, s52, v5\n" \
"ds_read_b64_tr_b16 v[140:141], v5 offset:0\n" \
"v_mfma_f32_16x16x32_bf16 v[112:115], v[192:195], v[168:171], v[112:115]\n" \
"global_load_lds_dwordx4 v11, s[46:47] offset:0\n" \
"ds_read_b64_tr_b16 v[142:143], v5 offset:1024\n" \
"s_waitcnt lgkmcnt(14)\n" \
"v_mfma_f32_16x16x32_bf16 v[84:87], v[196:199], v[172:175], v[84:87]\n" \
"v_add_u32 v6, s52, v6\n" \
"ds_read_b64_tr_b16 v[144:145], v6 offset:0\n" \
"global_load_lds_dwordx4 v12, s[46:47] offset:1024\n" \
"s_waitcnt lgkmcnt(13)\n" \
"v_mfma_f32_16x16x32_bf16 v[88:91], v[200:203], v[172:175], v[88:91]\n" \
"ds_read_b64_tr_b16 v[146:147], v6 offset:1024\n" \
"ds_read_b128 v[120:123], v2 offset:2048\n" \
"s_waitcnt lgkmcnt(13)\n" \
"v_mfma_f32_16x16x32_bf16 v[92:95], v[204:207], v[172:175], v[92:95]\n" \
"global_load_lds_dwordx4 v13, s[46:47] offset:2048\n" \
"ds_read_b64_tr_b16 v[148:149], v3 offset:8192\n" \
"ds_read_b64_tr_b16 v[150:151], v3 offset:9216\n" \
"s_waitcnt lgkmcnt(13)\n" \
"v_mfma_f32_16x16x32_bf16 v[96:99], v[208:211], v[172:175], v[96:99]\n" \
"ds_read_b128 v[124:127], v2 offset:1024\n" \
"global_load_lds_dwordx4 v14, s[46:47] offset:3072\n" \
"s_waitcnt lgkmcnt(13)\n" \
"v_mfma_f32_16x16x32_bf16 v[100:103], v[196:199], v[176:179], v[100:103]\n" \
"ds_read_b64_tr_b16 v[152:153], v4 offset:8192\n" \
"ds_read_b64_tr_b16 v[154:155], v4 offset:9216\n" \
"s_add_u32 s46, s46, 131072\n" \
"s_addc_u32 s47, s47, 0\n" \
"v_mfma_f32_16x16x32_bf16 v[104:107], v[200:203], v[176:179], v[104:107]\n" \
"ds_read_b64_tr_b16 v[156:157], v5 offset:8192\n" \
"ds_read_b64_tr_b16 v[158:159], v5 offset:9216\n" \
"v_mfma_f32_16x16x32_bf16 v[108:111], v[204:207], v[176:179], v[108:111]\n" \
"ds_read_b64_tr_b16 v[160:161], v6 offset:8192\n" \
"s_add_u32 s75, s75, 1\n" \
"s_cbranch_scc1 LWR%=_14\n" \
"LWS%=_14:\n" \
"ds_read_b64_tr_b16 v[162:163], v6 offset:9216\n" \
"v_mfma_f32_16x16x32_bf16 v[112:115], v[208:211], v[176:179], v[112:115]\n" \
"ds_read_b128 v[128:131], v2 offset:3072\n" \
"s_mov_b32 s54, s55\n" \
"s_waitcnt vmcnt(12)\n" \
"s_barrier\n" \
"s_waitcnt lgkmcnt(15)\n" \
"v_mfma_f32_16x16x32_bf16 v[84:87], v[132:135], v[116:119], v[84:87]\n" \
"s_add_u32 m0, s49, s54\n" \
"s_cmp_eq_u32 s51, 131072\n" \
"s_cselect_b32 s52, s53, 32768\n" \
"s_mov_b32 s55, s51\n" \
"s_waitcnt lgkmcnt(15)\n" \
"v_mfma_f32_16x16x32_bf16 v[88:91], v[136:139], v[116:119], v[88:91]\n" \
"s_add_u32 s51, s51, s52\n" \
"global_load_lds_dwordx4 v9, s[44:45] offset:0 sc1\n" \
"s_waitcnt lgkmcnt(13)\n" \
"v_mfma_f32_16x16x32_bf16 v[92:95], v[140:143], v[116:119], v[92:95]\n" \
"v_add_u32 v3, s52, v3\n" \
"ds_read_b64_tr_b16 v[180:181], v3 offset:0\n" \
"ds_read_b64_tr_b16 v[182:183], v3 offset:1024\n" \
"s_waitcnt lgkmcnt(13)\n" \
"v_mfma_f32_16x16x32_bf16 v[96:99], v[144:147], v[116:119], v[96:99]\n" \
"global_load_lds_dwordx4 v9, s[44:45] offset:1024 sc1\n" \
"v_add_u32 v2, s52, v2\n" \
"s_waitcnt lgkmcnt(12)\n" \
"v_mfma_f32_16x16x32_bf16 v[100:103], v[132:135], v[120:123], v[100:103]\n" \
"ds_read_b128 v[164:167], v2 offset:0\n" \
"s_add_u32 s44, s44, 2048\n" \
"s_addc_u32 s45, s45, 0\n" \
"v_add_u32 v4, s52, v4\n" \
"v_mfma_f32_16x16x32_bf16 v[104:107], v[136:139], v[120:123], v[104:107]\n" \
"ds_read_b64_tr_b16 v[184:185], v4 offset:0\n" \
"ds_read_b64_tr_b16 v[186:187], v4 offset:1024\n" \
"v_mfma_f32_16x16x32_bf16 v[108:111], v[140:143], v[120:123], v[108:111]\n" \
"s_add_u32 m0, s50, s54\n" \
"v_add_u32 v5, s52, v5\n" \
"ds_read_b64_tr_b16 v[188:189], v5 offset:0\n" \
"v_mfma_f32_16x16x32_bf16 v[112:115], v[144:147], v[120:123], v[112:115]\n" \
"global_load_lds_dwordx4 v11, s[46:47] offset:0\n" \
"ds_read_b64_tr_b16 v[190:191], v5 offset:1024\n" \
"s_waitcnt lgkmcnt(14)\n" \
"v_mfma_f32_16x16x32_bf16 v[84:87], v[148:151], v[124:127], v[84:87]\n" \
"v_add_u32 v6, s52, v6\n" \
"ds_read_b64_tr_b16 v[192:193], v6 offset:0\n" \
"global_load_lds_dwordx4 v12, s[46:47] offset:1024\n" \
"s_waitcnt lgkmcnt(13)\n" \
"v_mfma_f32_16x16x32_bf16 v[88:91], v[152:155], v[124:127], v[88:91]\n" \
"ds_read_b64_tr_b16 v[194:195], v6 offset:1024\n" \
"ds_read_b128 v[168:171], v2 offset:2048\n" \
"s_waitcnt lgkmcnt(13)\n" \
"v_mfma_f32_16x16x32_bf16 v[92:95], v[156:159], v[124:127], v[92:95]\n" \
"global_load_lds_dwordx4 v13, s[46:47] offset:2048\n" \
"ds_read_b64_tr_b16 v[196:197], v3 offset:8192\n" \
"ds_read_b64_tr_b16 v[198:199], v3 offset:9216\n" \
"s_waitcnt lgkmcnt(13)\n" \
"v_mfma_f32_16x16x32_bf16 v[96:99], v[160:163], v[124:127], v[96:99]\n" \
"ds_read_b128 v[172:175], v2 offset:1024\n" \
"global_load_lds_dwordx4 v14, s[46:47] offset:3072\n" \
"s_waitcnt lgkmcnt(13)\n" \
"v_mfma_f32_16x16x32_bf16 v[100:103], v[148:151], v[128:131], v[100:103]\n" \
"ds_read_b64_tr_b16 v[200:201], v4 offset:8192\n" \
"ds_read_b64_tr_b16 v[202:203], v4 offset:9216\n" \
"s_add_u32 s46, s46, 131072\n" \
"s_addc_u32 s47, s47, 0\n" \
"v_mfma_f32_16x16x32_bf16 v[104:107], v[152:155], v[128:131], v[104:107]\n" \
"ds_read_b64_tr_b16 v[204:205], v5 offset:8192\n" \
"ds_read_b64_tr_b16 v[206:207], v5 offset:9216\n" \
"v_mfma_f32_16x16x32_bf16 v[108:111], v[156:159], v[128:131], v[108:111]\n" \
"ds_read_b64_tr_b16 v[208:209], v6 offset:8192\n" \
"s_add_u32 s75, s75, 1\n" \
"s_cbranch_scc1 LWR%=_15\n" \
"LWS%=_15:\n" \
"ds_read_b64_tr_b16 v[210:211], v6 offset:9216\n" \
"v_mfma_f32_16x16x32_bf16 v[112:115], v[160:163], v[128:131], v[112:115]\n" \
"ds_read_b128 v[176:179], v2 offset:3072\n" \
"s_mov_b32 s54, s55\n" \
"s_waitcnt vmcnt(12)\n" \
"s_barrier\n" \
"s_waitcnt lgkmcnt(15)\n" \
"v_mfma_f32_16x16x32_bf16 v[84:87], v[180:183], v[164:167], v[84:87]\n" \
"s_add_u32 m0, s49, s54\n" \
"s_cmp_eq_u32 s51, 131072\n" \
"s_cselect_b32 s52, s53, 32768\n" \
"s_mov_b32 s55, s51\n" \
"v_mov_b32 v19, s63\n" \
"s_waitcnt lgkmcnt(15)\n" \
"v_mfma_f32_16x16x32_bf16 v[88:91], v[184:187], v[164:167], v[88:91]\n" \
"s_add_u32 s51, s51, s52\n" \
"global_load_lds_dwordx4 v9, s[44:45] offset:0 sc1\n" \
"s_waitcnt lgkmcnt(13)\n" \
"v_mfma_f32_16x16x32_bf16 v[92:95], v[188:191], v[164:167], v[92:95]\n" \
"v_add_u32 v3, s52, v3\n" \
"ds_read_b64_tr_b16 v[132:133], v3 offset:0\n" \
"ds_read_b64_tr_b16 v[134:135], v3 offset:1024\n" \
"global_load_dwordx2 v[16:17], v19, s[60:61] sc1\n" \
"s_waitcnt lgkmcnt(13)\n" \
"v_mfma_f32_16x16x32_bf16 v[96:99], v[192:195], v[164:167], v[96:99]\n" \
"global_load_lds_dwordx4 v9, s[44:45] offset:1024 sc1\n" \
"v_add_u32 v2, s52, v2\n" \
"s_waitcnt lgkmcnt(12)\n" \
"v_mfma_f32_16x16x32_bf16 v[100:103], v[180:183], v[168:171], v[100:103]\n" \
"ds_read_b128 v[116:119], v2 offset:0\n" \
"s_add_u32 s44, s44, 2048\n" \
"s_addc_u32 s45, s45, 0\n" \
"v_add_u32 v4, s52, v4\n" \
"v_mfma_f32_16x16x32_bf16 v[104:107], v[184:187], v[168:171], v[104:107]\n" \
"ds_read_b64_tr_b16 v[136:137], v4 offset:0\n" \
"ds_read_b64_tr_b16 v[138:139], v4 offset:1024\n" \
"v_mfma_f32_16x16x32_bf16 v[108:111], v[188:191], v[168:171], v[108:111]\n" \
"s_add_u32 m0, s50, s54\n" \
"v_add_u32 v5, s52, v5\n" \
"ds_read_b64_tr_b16 v[140:141], v5 offset:0\n" \
"v_mfma_f32_16x16x32_bf16 v[112:115], v[192:195], v[168:171], v[112:115]\n" \
"global_load_lds_dwordx4 v11, s[46:47] offset:0\n" \
"ds_read_b64_tr_b16 v[142:143], v5 offset:1024\n" \
"s_waitcnt lgkmcnt(14)\n" \
"v_mfma_f32_16x16x32_bf16 v[84:87], v[196:199], v[172:175], v[84:87]\n" \
"v_add_u32 v6, s52, v6\n" \
"ds_read_b64_tr_b16 v[144:145], v6 offset:0\n" \
"global_load_lds_dwordx4 v12, s[46:47] offset:1024\n" \
"s_waitcnt lgkmcnt(13)\n" \
"v_mfma_f32_16x16x32_bf16 v[88:91], v[200:203], v[172:175], v[88:91]\n" \
"ds_read_b64_tr_b16 v[146:147], v6 offset:1024\n" \
"ds_read_b128 v[120:123], v2 offset:2048\n" \
"s_waitcnt lgkmcnt(13)\n" \
"v_mfma_f32_16x16x32_bf16 v[92:95], v[204:207], v[172:175], v[92:95]\n" \
"global_load_lds_dwordx4 v13, s[46:47] offset:2048\n" \
"ds_read_b64_tr_b16 v[148:149], v3 offset:8192\n" \
"ds_read_b64_tr_b16 v[150:151], v3 offset:9216\n" \
"s_waitcnt lgkmcnt(13)\n" \
"v_mfma_f32_16x16x32_bf16 v[96:99], v[208:211], v[172:175], v[96:99]\n" \
"ds_read_b128 v[124:127], v2 offset:1024\n" \
"global_load_lds_dwordx4 v14, s[46:47] offset:3072\n" \
"s_waitcnt lgkmcnt(13)\n" \
"v_mfma_f32_16x16x32_bf16 v[100:103], v[196:199], v[176:179], v[100:103]\n" \
"ds_read_b64_tr_b16 v[152:153], v4 offset:8192\n" \
"ds_read_b64_tr_b16 v[154:155], v4 offset:9216\n" \
"s_add_u32 s46, s46, 131072\n" \
"s_addc_u32 s47, s47, 0\n" \
"v_mfma_f32_16x16x32_bf16 v[104:107], v[200:203], v[176:179], v[104:107]\n" \
"ds_read_b64_tr_b16 v[156:157], v5 offset:8192\n" \
"ds_read_b64_tr_b16 v[158:159], v5 offset:9216\n" \
"v_mfma_f32_16x16x32_bf16 v[108:111], v[204:207], v[176:179], v[108:111]\n" \
"ds_read_b64_tr_b16 v[160:161], v6 offset:8192\n" \
"s_add_u32 s75, s75, 1\n" \
"s_cbranch_scc1 LWR%=_16\n" \
"LWS%=_16:\n" \
"ds_read_b64_tr_b16 v[162:163], v6 offset:9216\n" \
"v_mfma_f32_16x16x32_bf16 v[112:115], v[208:211], v[176:179], v[112:115]\n" \
"ds_read_b128 v[128:131], v2 offset:3072\n" \
"s_mov_b32 s54, s55\n" \
"s_waitcnt vmcnt(13)\n" \
"s_barrier\n" \
"s_waitcnt lgkmcnt(15)\n" \
"v_mfma_f32_16x16x32_bf16 v[84:87], v[132:135], v[116:119], v[84:87]\n" \
"s_add_u32 m0, s49, s54\n" \
"s_cmp_eq_u32 s51, 131072\n" \
"s_cselect_b32 s52, s53, 32768\n" \
"s_mov_b32 s55, s51\n" \
"s_waitcnt lgkmcnt(15)\n" \
"v_mfma_f32_16x16x32_bf16 v[88:91], v[136:139], v[116:119], v[88:91]\n" \
"s_add_u32 s51, s51, s52\n" \
"global_load_lds_dwordx4 v9, s[44:45] offset:0 sc1\n" \
"s_waitcnt lgkmcnt(13)\n" \
"v_mfma_f32_16x16x32_bf16 v[92:95], v[140:143], v[116:119], v[92:95]\n" \
"v_add_u32 v3, s52, v3\n" \
"ds_read_b64_tr_b16 v[180:181], v3 offset:0\n" \
"ds_read_b64_tr_b16 v[182:183], v3 offset:1024\n" \
"s_waitcnt lgkmcnt(13)\n" \
"v_mfma_f32_16x16x32_bf16 v[96:99], v[144:147], v[116:119], v[96:99]\n" \
"global_load_lds_dwordx4 v9, s[44:45] offset:1024 sc1\n" \
"v_add_u32 v2, s52, v2\n" \
"s_waitcnt lgkmcnt(12)\n" \
"v_mfma_f32_16x16x32_bf16 v[100:103], v[132:135], v[120:123], v[100:103]\n" \
"ds_read_b128 v[164:167], v2 offset:0\n" \
"s_add_u32 s44, s44, 2048\n" \
"s_addc_u32 s45, s45, 0\n" \
"v_add_u32 v4, s52, v4\n" \
"v_mfma_f32_16x16x32_bf16 v[104:107], v[136:139], v[120:123], v[104:107]\n" \
"ds_read_b64_tr_b16 v[184:185], v4 offset:0\n" \
"ds_read_b64_tr_b16 v[186:187], v4 offset:1024\n" \
"v_mfma_f32_16x16x32_bf16 v[108:111], v[140:143], v[120:123], v[108:111]\n" \
"s_add_u32 m0, s50, s54\n" \
"v_add_u32 v5, s52, v5\n" \
"ds_read_b64_tr_b16 v[188:189], v5 offset:0\n" \
"v_mfma_f32_16x16x32_bf16 v[112:115], v[144:147], v[120:123], v[112:115]\n" \
"global_load_lds_dwordx4 v11, s[46:47] offset:0\n" \
"ds_read_b64_tr_b16 v[190:191], v5 offset:1024\n" \
"s_waitcnt lgkmcnt(14)\n" \
"v_mfma_f32_16x16x32_bf16 v[84:87], v[148:151], v[124:127], v[84:87]\n" \
"v_add_u32 v6, s52, v6\n" \
"ds_read_b64_tr_b16 v[192:193], v6 offset:0\n" \
"global_load_lds_dwordx4 v12, s[46:47] offset:1024\n" \
"s_waitcnt lgkmcnt(13)\n" \
"v_mfma_f32_16x16x32_bf16 v[88:91], v[152:155], v[124:127], v[88:91]\n" \
"ds_read_b64_tr_b16 v[194:195], v6 offset:1024\n" \
"ds_read_b128 v[168:171], v2 offset:2048\n" \
"s_waitcnt lgkmcnt(13)\n" \
"v_mfma_f32_16x16x32_bf16 v[92:95], v[156:159], v[124:127], v[92:95]\n" \
"global_load_lds_dwordx4 v13, s[46:47] offset:2048\n" \
"ds_read_b64_tr_b16 v[196:197], v3 offset:8192\n" \
"ds_read_b64_tr_b16 v[198:199], v3 offset:9216\n" \
"s_waitcnt lgkmcnt(13)\n" \
"v_mfma_f32_16x16x32_bf16 v[96:99], v[160:163], v[124:127], v[96:99]\n" \
"ds_read_b128 v[172:175], v2 offset:1024\n" \
"global_load_lds_dwordx4 v14, s[46:47] offset:3072\n" \
"s_waitcnt lgkmcnt(13)\n" \
"v_mfma_f32_16x16x32_bf16 v[100:103], v[148:151], v[128:131], v[100:103]\n" \
"ds_read_b64_tr_b16 v[200:201], v4 offset:8192\n" \
"ds_read_b64_tr_b16 v[202:203], v4 offset:9216\n" \
"s_add_u32 s46, s46, 131072\n" \
"s_addc_u32 s47, s47, 0\n" \
"v_mfma_f32_16x16x32_bf16 v[104:107], v[152:155], v[128:131], v[104:107]\n" \
"ds_read_b64_tr_b16 v[204:205], v5 offset:8192\n" \
"ds_read_b64_tr_b16 v[206:207], v5 offset:9216\n" \
"v_mfma_f32_16x16x32_bf16 v[108:111], v[156:159], v[128:131], v[108:111]\n" \
"ds_read_b64_tr_b16 v[208:209], v6 offset:8192\n" \
"s_add_u32 s75, s75, 1\n" \
"s_cbranch_scc1 LWR%=_17\n" \
"LWS%=_17:\n" \
"ds_read_b64_tr_b16 v[210:211], v6 offset:9216\n" \
"v_mfma_f32_16x16x32_bf16 v[112:115], v[160:163], v[128:131], v[112:115]\n" \
"ds_read_b128 v[176:179], v2 offset:3072\n" \
"s_mov_b32 s54, s55\n" \
"s_waitcnt vmcnt(13)\n" \
"s_barrier\n" \
"s_waitcnt lgkmcnt(15)\n" \
"v_mfma_f32_16x16x32_bf16 v[84:87], v[180:183], v[164:167], v[84:87]\n" \
"s_add_u32 m0, s49, s54\n" \
"s_cmp_eq_u32 s51, 131072\n" \
"s_cselect_b32 s52, s53, 32768\n" \
"s_mov_b32 s55, s51\n" \
"s_waitcnt lgkmcnt(15)\n" \
"v_mfma_f32_16x16x32_bf16 v[88:91], v[184:187], v[164:167], v[88:91]\n" \
"s_add_u32 s51, s51, s52\n" \
"global_load_lds_dwordx4 v9, s[44:45] offset:0 sc1\n" \
"s_waitcnt lgkmcnt(13)\n" \
"v_mfma_f32_16x16x32_bf16 v[92:95], v[188:191], v[164:167], v[92:95]\n" \
"v_add_u32 v3, s52, v3\n" \
"ds_read_b64_tr_b16 v[132:133], v3 offset:0\n" \
"ds_read_b64_tr_b16 v[134:135], v3 offset:1024\n" \
"s_waitcnt lgkmcnt(13)\n" \
"v_mfma_f32_16x16x32_bf16 v[96:99], v[192:195], v[164:167], v[96:99]\n" \
"global_load_lds_dwordx4 v9, s[44:45] offset:1024 sc1\n" \
"v_add_u32 v2, s52, v2\n" \
"s_waitcnt lgkmcnt(12)\n" \
"v_mfma_f32_16x16x32_bf16 v[100:103], v[180:183], v[168:171], v[100:103]\n" \
"ds_read_b128 v[116:119], v2 offset:0\n" \
"s_add_u32 s44, s44, 2048\n" \
"s_addc_u32 s45, s45, 0\n" \
"v_add_u32 v4, s52, v4\n" \
"v_mfma_f32_16x16x32_bf16 v[104:107], v[184:187], v[168:171], v[104:107]\n" \
"ds_read_b64_tr_b16 v[136:137], v4 offset:0\n" \
"ds_read_b64_tr_b16 v[138:139], v4 offset:1024\n" \
"v_mfma_f32_16x16x32_bf16 v[108:111], v[188:191], v[168:171], v[108:111]\n" \
"s_add_u32 m0, s50, s54\n" \
"v_add_u32 v5, s52, v5\n" \
"ds_read_b64_tr_b16 v[140:141], v5 offset:0\n" \
"v_mfma_f32_16x16x32_bf16 v[112:115], v[192:195], v[168:171], v[112:115]\n" \
"global_load_lds_dwordx4 v11, s[46:47] offset:0\n" \
"ds_read_b64_tr_b16 v[142:143], v5 offset:1024\n" \
"s_waitcnt lgkmcnt(14)\n" \
"v_mfma_f32_16x16x32_bf16 v[84:87], v[196:199], v[172:175], v[84:87]\n" \
"v_add_u32 v6, s52, v6\n" \
"ds_read_b64_tr_b16 v[144:145], v6 offset:0\n" \
"global_load_lds_dwordx4 v12, s[46:47] offset:1024\n" \
"s_waitcnt lgkmcnt(13)\n" \
"v_mfma_f32_16x16x32_bf16 v[88:91], v[200:203], v[172:175], v[88:91]\n" \
"ds_read_b64_tr_b16 v[146:147], v6 offset:1024\n" \
"ds_read_b128 v[120:123], v2 offset:2048\n" \
"s_waitcnt lgkmcnt(13)\n" \
"v_mfma_f32_16x16x32_bf16 v[92:95], v[204:207], v[172:175], v[92:95]\n" \
"global_load_lds_dwordx4 v13, s[46:47] offset:2048\n" \
"ds_read_b64_tr_b16 v[148:149], v3 offset:8192\n" \
"ds_read_b64_tr_b16 v[150:151], v3 offset:9216\n" \
"s_waitcnt lgkmcnt(13)\n" \
"v_mfma_f32_16x16x32_bf16 v[96:99], v[208:211], v[172:175], v[96:99]\n" \
"ds_read_b128 v[124:127], v2 offset:1024\n" \
"global_load_lds_dwordx4 v14, s[46:47] offset:3072\n" \
"s_waitcnt lgkmcnt(13)\n" \
"v_mfma_f32_16x16x32_bf16 v[100:103], v[196:199], v[176:179], v[100:103]\n" \
"ds_read_b64_tr_b16 v[152:153], v4 offset:8192\n" \
"ds_read_b64_tr_b16 v[154:155], v4 offset:9216\n" \
"s_add_u32 s46, s46, 131072\n" \
"s_addc_u32 s47, s47, 0\n" \
"v_mfma_f32_16x16x32_bf16 v[104:107], v[200:203], v[176:179], v[104:107]\n" \
"ds_read_b64_tr_b16 v[156:157], v5 offset:8192\n" \
"ds_read_b64_tr_b16 v[158:159], v5 offset:9216\n" \
"v_mfma_f32_16x16x32_bf16 v[108:111], v[204:207], v[176:179], v[108:111]\n" \
"ds_read_b64_tr_b16 v[160:161], v6 offset:8192\n" \
"s_add_u32 s75, s75, 1\n" \
"s_cbranch_scc1 LWR%=_18\n" \
"LWS%=_18:\n" \
"ds_read_b64_tr_b16 v[162:163], v6 offset:9216\n" \
"v_mfma_f32_16x16x32_bf16 v[112:115], v[208:211], v[176:179], v[112:115]\n" \
"ds_read_b128 v[128:131], v2 offset:3072\n" \
"s_mov_b32 s54, s55\n" \
"s_waitcnt vmcnt(12)\n" \
"s_cmp_eq_u32 s65, 0\n" \
"s_cbranch_scc1 LQ%=\n" \
"s_mov_b32 s67, 0\n" \
"s_waitcnt vmcnt(17)\n" \
"LP%=:\n" \
"v_cmp_ne_u64 vcc, s[68:69], v[16:17]\n" \
"s_nop 4\n" \
"s_cbranch_vccz LQ%=\n" \
"s_add_u32 s67, s67, 1\n" \
"s_cmp_gt_u32 s67, 0x100000\n" \
"s_cbranch_scc1 LQ%=\n" \
"s_sleep 1\n" \
"global_load_dwordx2 v[16:17], v19, s[60:61] sc1\n" \
"s_waitcnt vmcnt(0)\n" \
"s_branch LP%=\n" \
"LQ%=:\n" \
"s_barrier\n" \
"s_waitcnt lgkmcnt(15)\n" \
"v_mfma_f32_16x16x32_bf16 v[84:87], v[132:135], v[116:119], v[84:87]\n" \
"s_cmp_eq_u32 s51, 131072\n" \
"s_cselect_b32 s52, s53, 32768\n" \
"s_mov_b32 s55, s51\n" \
"global_load_dwordx4 v[52:55], v7, s[58:59] offset:0 sc1\n" \
"s_waitcnt lgkmcnt(15)\n" \
"v_mfma_f32_16x16x32_bf16 v[88:91], v[136:139], v[116:119], v[88:91]\n" \
"s_add_u32 s51, s51, s52\n" \
"v_add_u32 v3, s52, v3\n" \
"s_waitcnt lgkmcnt(13)\n" \
"v_mfma_f32_16x16x32_bf16 v[92:95], v[140:143], v[116:119], v[92:95]\n" \
"ds_read_b64_tr_b16 v[180:181], v3 offset:0\n" \
"ds_read_b64_tr_b16 v[182:183], v3 offset:1024\n" \
"global_load_dwordx4 v[56:59], v7, s[58:59] offset:1024 sc1\n" \
"s_waitcnt lgkmcnt(13)\n" \
"v_mfma_f32_16x16x32_bf16 v[96:99], v[144:147], v[116:119], v[96:99]\n" \
"v_add_u32 v2, s52, v2\n" \
"ds_read_b128 v[164:167], v2 offset:0\n" \
"s_waitcnt lgkmcnt(13)\n" \
"v_mfma_f32_16x16x32_bf16 v[100:103], v[132:135], v[120:123], v[100:103]\n" \
"v_add_u32 v4, s52, v4\n" \
"ds_read_b64_tr_b16 v[184:185], v4 offset:0\n" \
"global_load_dwordx4 v[60:63], v7, s[58:59] offset:2048 sc1\n" \
"v_mfma_f32_16x16x32_bf16 v[104:107], v[136:139], v[120:123], v[104:107]\n" \
"ds_read_b64_tr_b16 v[186:187], v4 offset:1024\n" \
"v_mfma_f32_16x16x32_bf16 v[108:111], v[140:143], v[120:123], v[108:111]\n" \
"v_add_u32 v5, s52, v5\n" \
"ds_read_b64_tr_b16 v[188:189], v5 offset:0\n" \
"global_load_dwordx4 v[64:67], v7, s[58:59] offset:3072 sc1\n" \
"v_mfma_f32_16x16x32_bf16 v[112:115], v[144:147], v[120:123], v[112:115]\n" \
"ds_read_b64_tr_b16 v[190:191], v5 offset:1024\n" \
"v_add_u32 v6, s52, v6\n" \
"s_waitcnt lgkmcnt(14)\n" \
"v_mfma_f32_16x16x32_bf16 v[84:87], v[148:151], v[124:127], v[84:87]\n" \
"ds_read_b64_tr_b16 v[192:193], v6 offset:0\n" \
"ds_read_b64_tr_b16 v[194:195], v6 offset:1024\n" \
"global_load_dwordx4 v[68:71], v8, s[58:59] offset:0 sc1\n" \
"s_waitcnt lgkmcnt(14)\n" \
"v_mfma_f32_16x16x32_bf16 v[88:91], v[152:155], v[124:127], v[88:91]\n" \
"ds_read_b128 v[168:171], v2 offset:2048\n" \
"ds_read_b64_tr_b16 v[196:197], v3 offset:8192\n" \
"s_waitcnt lgkmcnt(14)\n" \
"v_mfma_f32_16x16x32_bf16 v[92:95], v[156:159], v[124:127], v[92:95]\n" \
"ds_read_b64_tr_b16 v[198:199], v3 offset:9216\n" \
"global_load_dwordx4 v[72:75], v8, s[58:59] offset:1024 sc1\n" \
"s_waitcnt lgkmcnt(13)\n" \
"v_mfma_f32_16x16x32_bf16 v[96:99], v[160:163], v[124:127], v[96:99]\n" \
"ds_read_b128 v[172:175], v2 offset:1024\n" \
"ds_read_b64_tr_b16 v[200:201], v4 offset:8192\n" \
"s_waitcnt lgkmcnt(14)\n" \
"v_mfma_f32_16x16x32_bf16 v[100:103], v[148:151], v[128:131], v[100:103]\n" \
"ds_read_b64_tr_b16 v[202:203], v4 offset:9216\n" \
"ds_read_b64_tr_b16 v[204:205], v5 offset:8192\n" \
"global_load_dwordx4 v[76:79], v8, s[58:59] offset:2048 sc1\n" \
"v_mfma_f32_16x16x32_bf16 v[104:107], v[152:155], v[128:131], v[104:107]\n" \
"ds_read_b64_tr_b16 v[206:207], v5 offset:9216\n" \
"ds_read_b64_tr_b16 v[208:209], v6 offset:8192\n" \
"v_mfma_f32_16x16x32_bf16 v[108:111], v[156:159], v[128:131], v[108:111]\n" \
"ds_read_b64_tr_b16 v[210:211], v6 offset:9216\n" \
"ds_read_b128 v[176:179], v2 offset:3072\n" \
"global_load_dwordx4 v[80:83], v8, s[58:59] offset:3072 sc1\n" \
"v_mfma_f32_16x16x32_bf16 v[112:115], v[160:163], v[128:131], v[112:115]\n" \
"s_mov_b32 s54, s55\n" \
"s_waitcnt vmcnt(14)\n" \
"s_barrier\n" \
"s_waitcnt lgkmcnt(15)\n" \
"v_mfma_f32_16x16x32_bf16 v[84:87], v[180:183], v[164:167], v[84:87]\n" \
"s_cmp_eq_u32 s51, 131072\n" \
"s_cselect_b32 s52, s53, 32768\n" \
"s_mov_b32 s55, s51\n" \
"s_waitcnt lgkmcnt(15)\n" \
"v_mfma_f32_16x16x32_bf16 v[88:91], v[184:187], v[164:167], v[88:91]\n" \
"s_add_u32 s51, s51, s52\n" \
"v_add_u32 v3, s52, v3\n" \
"s_waitcnt lgkmcnt(13)\n" \
"v_mfma_f32_16x16x32_bf16 v[92:95], v[188:191], v[164:167], v[92:95]\n" \
"ds_read_b64_tr_b16 v[132:133], v3 offset:0\n" \
"ds_read_b64_tr_b16 v[134:135], v3 offset:1024\n" \
"s_waitcnt lgkmcnt(13)\n" \
"v_mfma_f32_16x16x32_bf16 v[96:99], v[192:195], v[164:167], v[96:99]\n" \
"v_add_u32 v2, s52, v2\n" \
"ds_read_b128 v[116:119], v2 offset:0\n" \
"s_waitcnt lgkmcnt(13)\n" \
"v_mfma_f32_16x16x32_bf16 v[100:103], v[180:183], v[168:171], v[100:103]\n" \
"v_add_u32 v4, s52, v4\n" \
"ds_read_b64_tr_b16 v[136:137], v4 offset:0\n" \
"v_mfma_f32_16x16x32_bf16 v[104:107], v[184:187], v[168:171], v[104:107]\n" \
"ds_read_b64_tr_b16 v[138:139], v4 offset:1024\n" \
"v_mfma_f32_16x16x32_bf16 v[108:111], v[188:191], v[168:171], v[108:111]\n" \
"v_add_u32 v5, s52, v5\n" \
"ds_read_b64_tr_b16 v[140:141], v5 offset:0\n" \
"v_mfma_f32_16x16x32_bf16 v[112:115], v[192:195], v[168:171], v[112:115]\n" \
"ds_read_b64_tr_b16 v[142:143], v5 offset:1024\n" \
"v_add_u32 v6, s52, v6\n" \
"s_waitcnt lgkmcnt(14)\n" \
"v_mfma_f32_16x16x32_bf16 v[84:87], v[196:199], v[172:175], v[84:87]\n" \
"ds_read_b64_tr_b16 v[144:145], v6 offset:0\n" \
"ds_read_b64_tr_b16 v[146:147], v6 offset:1024\n" \
"s_waitcnt lgkmcnt(14)\n" \
"v_mfma_f32_16x16x32_bf16 v[88:91], v[200:203], v[172:175], v[88:91]\n" \
"ds_read_b128 v[120:123], v2 offset:2048\n" \
"ds_read_b64_tr_b16 v[148:149], v3 offset:8192\n" \
"s_waitcnt lgkmcnt(14)\n" \
"v_mfma_f32_16x16x32_bf16 v[92:95], v[204:207], v[172:175], v[92:95]\n" \
"ds_read_b64_tr_b16 v[150:151], v3 offset:9216\n" \
"s_waitcnt lgkmcnt(13)\n" \
"v_mfma_f32_16x16x32_bf16 v[96:99], v[208:211], v[172:175], v[96:99]\n" \
"ds_read_b128 v[124:127], v2 offset:1024\n" \
"ds_read_b64_tr_b16 v[152:153], v4 offset:8192\n" \
"s_waitcnt lgkmcnt(14)\n" \
"v_mfma_f32_16x16x32_bf16 v[100:103], v[196:199], v[176:179], v[100:103]\n" \
"ds_read_b64_tr_b16 v[154:155], v4 offset:9216\n" \
"ds_read_b64_tr_b16 v[156:157], v5 offset:8192\n" \
"v_mfma_f32_16x16x32_bf16 v[104:107], v[200:203], v[176:179], v[104:107]\n" \
"ds_read_b64_tr_b16 v[158:159], v5 offset:9216\n" \
"ds_read_b64_tr_b16 v[160:161], v6 offset:8192\n" \
"v_mfma_f32_16x16x32_bf16 v[108:111], v[204:207], v[176:179], v[108:111]\n" \
"ds_read_b64_tr_b16 v[162:163], v6 offset:9216\n" \
"ds_read_b128 v[128:131], v2 offset:3072\n" \
"v_mfma_f32_16x16x32_bf16 v[112:115], v[208:211], v[176:179], v[112:115]\n" \
"s_mov_b32 s54, s55\n" \
"s_waitcnt vmcnt(8)\n" \
"s_barrier\n" \
"s_waitcnt lgkmcnt(15)\n" \
"v_mfma_f32_16x16x32_bf16 v[84:87], v[132:135], v[116:119], v[84:87]\n" \
"s_cmp_eq_u32 s51, 131072\n" \
"s_cselect_b32 s52, s53, 32768\n" \
"s_mov_b32 s55, s51\n" \
"s_waitcnt lgkmcnt(15)\n" \
"v_mfma_f32_16x16x32_bf16 v[88:91], v[136:139], v[116:119], v[88:91]\n" \
"s_add_u32 s51, s51, s52\n" \
"v_add_u32 v3, s52, v3\n" \
"s_waitcnt lgkmcnt(13)\n" \
"v_mfma_f32_16x16x32_bf16 v[92:95], v[140:143], v[116:119], v[92:95]\n" \
"ds_read_b64_tr_b16 v[180:181], v3 offset:0\n" \
"ds_read_b64_tr_b16 v[182:183], v3 offset:1024\n" \
"s_waitcnt lgkmcnt(13)\n" \
"v_mfma_f32_16x16x32_bf16 v[96:99], v[144:147], v[116:119], v[96:99]\n" \
"v_add_u32 v2, s52, v2\n" \
"ds_read_b128 v[164:167], v2 offset:0\n" \
"s_waitcnt lgkmcnt(13)\n" \
"v_mfma_f32_16x16x32_bf16 v[100:103], v[132:135], v[120:123], v[100:103]\n" \
"v_add_u32 v4, s52, v4\n" \
"ds_read_b64_tr_b16 v[184:185], v4 offset:0\n" \
"v_mfma_f32_16x16x32_bf16 v[104:107], v[136:139], v[120:123], v[104:107]\n" \
"ds_read_b64_tr_b16 v[186:187], v4 offset:1024\n" \
"v_mfma_f32_16x16x32_bf16 v[108:111], v[140:143], v[120:123], v[108:111]\n" \
"v_add_u32 v5, s52, v5\n" \
"ds_read_b64_tr_b16 v[188:189], v5 offset:0\n" \
"v_mfma_f32_16x16x32_bf16 v[112:115], v[144:147], v[120:123], v[112:115]\n" \
"ds_read_b64_tr_b16 v[190:191], v5 offset:1024\n" \
"v_add_u32 v6, s52, v6\n" \
"s_waitcnt lgkmcnt(14)\n" \
"v_mfma_f32_16x16x32_bf16 v[84:87], v[148:151], v[124:127], v[84:87]\n" \
"ds_read_b64_tr_b16 v[192:193], v6 offset:0\n" \
"ds_read_b64_tr_b16 v[194:195], v6 offset:1024\n" \
"s_waitcnt lgkmcnt(14)\n" \
"v_mfma_f32_16x16x32_bf16 v[88:91], v[152:155], v[124:127], v[88:91]\n" \
"ds_read_b128 v[168:171], v2 offset:2048\n" \
"ds_read_b64_tr_b16 v[196:197], v3 offset:8192\n" \
"s_waitcnt lgkmcnt(14)\n" \
"v_mfma_f32_16x16x32_bf16 v[92:95], v[156:159], v[124:127], v[92:95]\n" \
"ds_read_b64_tr_b16 v[198:199], v3 offset:9216\n" \
"s_waitcnt lgkmcnt(13)\n" \
"v_mfma_f32_16x16x32_bf16 v[96:99], v[160:163], v[124:127], v[96:99]\n" \
"ds_read_b128 v[172:175], v2 offset:1024\n" \
"ds_read_b64_tr_b16 v[200:201], v4 offset:8192\n" \
"s_waitcnt lgkmcnt(14)\n" \
"v_mfma_f32_16x16x32_bf16 v[100:103], v[148:151], v[128:131], v[100:103]\n" \
"ds_read_b64_tr_b16 v[202:203], v4 offset:9216\n" \
"ds_read_b64_tr_b16 v[204:205], v5 offset:8192\n" \
"v_mfma_f32_16x16x32_bf16 v[104:107], v[152:155], v[128:131], v[104:107]\n" \
"ds_read_b64_tr_b16 v[206:207], v5 offset:9216\n" \
"ds_read_b64_tr_b16 v[208:209], v6 offset:8192\n" \
"v_mfma_f32_16x16x32_bf16 v[108:111], v[156:159], v[128:131], v[108:111]\n" \
"ds_read_b64_tr_b16 v[210:211], v6 offset:9216\n" \
"ds_read_b128 v[176:179], v2 offset:3072\n" \
"v_mfma_f32_16x16x32_bf16 v[112:115], v[160:163], v[128:131], v[112:115]\n" \
"s_mov_b32 s54, s55\n" \
"s_barrier\n" \
"s_waitcnt lgkmcnt(15)\n" \
"v_mfma_f32_16x16x32_bf16 v[84:87], v[180:183], v[164:167], v[84:87]\n" \
"s_waitcnt lgkmcnt(15)\n" \
"v_mfma_f32_16x16x32_bf16 v[88:91], v[184:187], v[164:167], v[88:91]\n" \
"s_waitcnt lgkmcnt(13)\n" \
"v_mfma_f32_16x16x32_bf16 v[92:95], v[188:191], v[164:167], v[92:95]\n" \
"s_waitcnt lgkmcnt(11)\n" \
"v_mfma_f32_16x16x32_bf16 v[96:99], v[192:195], v[164:167], v[96:99]\n" \
"s_waitcnt lgkmcnt(10)\n" \
"v_mfma_f32_16x16x32_bf16 v[100:103], v[180:183], v[168:171], v[100:103]\n" \
"v_mfma_f32_16x16x32_bf16 v[104:107], v[184:187], v[168:171], v[104:107]\n" \
"v_mfma_f32_16x16x32_bf16 v[108:111], v[188:191], v[168:171], v[108:111]\n" \
"v_mfma_f32_16x16x32_bf16 v[112:115], v[192:195], v[168:171], v[112:115]\n" \
"s_waitcnt lgkmcnt(7)\n" \
"v_mfma_f32_16x16x32_bf16 v[84:87], v[196:199], v[172:175], v[84:87]\n" \
"s_waitcnt lgkmcnt(5)\n" \
"v_mfma_f32_16x16x32_bf16 v[88:91], v[200:203], v[172:175], v[88:91]\n" \
"s_waitcnt lgkmcnt(3)\n" \
"v_mfma_f32_16x16x32_bf16 v[92:95], v[204:207], v[172:175], v[92:95]\n" \
"s_waitcnt lgkmcnt(1)\n" \
"v_mfma_f32_16x16x32_bf16 v[96:99], v[208:211], v[172:175], v[96:99]\n" \
"s_waitcnt lgkmcnt(0)\n" \
"v_mfma_f32_16x16x32_bf16 v[100:103], v[196:199], v[176:179], v[100:103]\n" \
"v_mfma_f32_16x16x32_bf16 v[104:107], v[200:203], v[176:179], v[104:107]\n" \
"v_mfma_f32_16x16x32_bf16 v[108:111], v[204:207], v[176:179], v[108:111]\n" \
"v_mfma_f32_16x16x32_bf16 v[112:115], v[208:211], v[176:179], v[112:115]\n" \
"s_barrier\n" \
"s_waitcnt vmcnt(0)\n" \
K2_STAMP4 \
"v_mbcnt_lo_u32_b32 v0, -1, 0\n" \
"v_mbcnt_hi_u32_b32 v0, -1, v0\n" \
"v_and_b32 v1, 15, v0\n" \
"v_lshlrev_b32 v1, 11, v1\n" \
"v_bfe_u32 v3, v0, 4, 1\n" \
"v_lshl_add_u32 v1, v3, 5, v1\n" \
"v_lshrrev_b32 v3, 5, v0\n" \
"v_lshl_add_u32 v1, v3, 4, v1\n" \
"v_mov_b32 v2, 0xbfb8aa3b\n" \
"v_add_u32 v4, 32768, v1\n" \
"s_nop 1\n" \
"v_pk_add_f32 v[20:21], v[20:21], v[52:53]\n" \
"v_pk_add_f32 v[28:29], v[28:29], v[60:61]\n" \
"v_pk_add_f32 v[24:25], v[24:25], v[56:57]\n" \
"v_pk_add_f32 v[32:33], v[32:33], v[64:65]\n" \
"v_pk_add_f32 v[22:23], v[22:23], v[54:55]\n" \
"v_pk_add_f32 v[30:31], v[30:31], v[62:63]\n" \
"v_pk_add_f32 v[26:27], v[26:27], v[58:59]\n" \
"v_pk_add_f32 v[34:35], v[34:35], v[66:67]\n" \
"v_pk_mul_f32 v[116:117], v[20:21], v[2:3] op_sel_hi:[1,0]\n" \
"v_pk_mul_f32 v[132:133], v[28:29], v[2:3] op_sel_hi:[1,0]\n" \
"v_pk_mul_f32 v[120:121], v[24:25], v[2:3] op_sel_hi:[1,0]\n" \
"v_pk_mul_f32 v[136:137], v[32:33], v[2:3] op_sel_hi:[1,0]\n" \
"v_pk_mul_f32 v[118:119], v[22:23], v[2:3] op_sel_hi:[1,0]\n" \
"v_pk_mul_f32 v[134:135], v[30:31], v[2:3] op_sel_hi:[1,0]\n" \
"v_pk_mul_f32 v[122:123], v[26:27], v[2:3] op_sel_hi:[1,0]\n" \
"v_pk_mul_f32 v[138:139], v[34:35], v[2:3] op_sel_hi:[1,0]\n" \
"v_exp_f32 v116, v116\n" \
"v_exp_f32 v132, v132\n" \
"v_exp_f32 v120, v120\n" \
"v_exp_f32 v136, v136\n" \
"v_exp_f32 v117, v117\n" \
"v_exp_f32 v133, v133\n" \
"v_exp_f32 v121, v121\n" \
"v_exp_f32 v137, v137\n" \
"v_exp_f32 v118, v118\n" \
"v_exp_f32 v134, v134\n" \
"v_exp_f32 v122, v122\n" \
"v_exp_f32 v138, v138\n" \
"v_exp_f32 v119, v119\n" \
"v_exp_f32 v135, v135\n" \
"v_exp_f32 v123, v123\n" \
"v_exp_f32 v139, v139\n" \
"v_pk_mul_f32 v[20:21], v[20:21], v[84:85]\n" \
"v_pk_mul_f32 v[28:29], v[28:29], v[92:93]\n" \
"v_pk_mul_f32 v[24:25], v[24:25], v[88:89]\n" \
"v_pk_mul_f32 v[32:33], v[32:33], v[96:97]\n" \
"v_pk_mul_f32 v[22:23], v[22:23], v[86:87]\n" \
"v_pk_mul_f32 v[30:31], v[30:31], v[94:95]\n" \
"v_pk_mul_f32 v[26:27], v[26:27], v[90:91]\n" \
"v_pk_mul_f32 v[34:35], v[34:35], v[98:99]\n" \
"v_pk_add_f32 v[116:117], v[116:117], 1.0 op_sel_hi:[1,0]\n" \
"v_pk_add_f32 v[132:133], v[132:133], 1.0 op_sel_hi:[1,0]\n" \
"v_pk_add_f32 v[120:121], v[120:121], 1.0 op_sel_hi:[1,0]\n" \
"v_pk_add_f32 v[136:137], v[136:137], 1.0 op_sel_hi:[1,0]\n" \
"v_pk_add_f32 v[118:119], v[118:119], 1.0 op_sel_hi:[1,0]\n" \
"v_pk_add_f32 v[134:135], v[134:135], 1.0 op_sel_hi:[1,0]\n" \
"v_pk_add_f32 v[122:123], v[122:123], 1.0 op_sel_hi:[1,0]\n" \
"v_pk_add_f32 v[138:139], v[138:139], 1.0 op_sel_hi:[1,0]\n" \
"v_rcp_f32 v116, v116\n" \
"v_rcp_f32 v132, v132\n" \
"v_rcp_f32 v120, v120\n" \
"v_rcp_f32 v136, v136\n" \
"v_rcp_f32 v117, v117\n" \
"v_rcp_f32 v133, v133\n" \
"v_rcp_f32 v121, v121\n" \
"v_rcp_f32 v137, v137\n" \
"v_rcp_f32 v118, v118\n" \
"v_rcp_f32 v134, v134\n" \
"v_rcp_f32 v122, v122\n" \
"v_rcp_f32 v138, v138\n" \
"v_rcp_f32 v119, v119\n" \
"v_rcp_f32 v135, v135\n" \
"v_rcp_f32 v123, v123\n" \
"v_rcp_f32 v139, v139\n" \
"v_lshlrev_b32 v52, 16, v212\n" \
"v_lshlrev_b32 v60, 16, v216\n" \
"v_lshlrev_b32 v56, 16, v214\n" \
"v_lshlrev_b32 v64, 16, v218\n" \
"v_and_b32 v53, 0xffff0000, v212\n" \
"v_and_b32 v61, 0xffff0000, v216\n" \
"v_and_b32 v57, 0xffff0000, v214\n" \
"v_and_b32 v65, 0xffff0000, v218\n" \
"v_lshlrev_b32 v54, 16, v213\n" \
"v_lshlrev_b32 v62, 16, v217\n" \
"v_lshlrev_b32 v58, 16, v215\n" \
"v_lshlrev_b32 v66, 16, v219\n" \
"v_and_b32 v55, 0xffff0000, v213\n" \
"v_and_b32 v63, 0xffff0000, v217\n" \
"v_and_b32 v59, 0xffff0000, v215\n" \
"v_and_b32 v67, 0xffff0000, v219\n" \
"v_pk_fma_f32 v[20:21], v[20:21], v[116:117], v[52:53]\n" \
"v_pk_fma_f32 v[28:29], v[28:29], v[132:133], v[60:61]\n" \
"v_pk_fma_f32 v[24:25], v[24:25], v[120:121], v[56:57]\n" \
"v_pk_fma_f32 v[32:33], v[32:33], v[136:137], v[64:65]\n" \
"v_pk_fma_f32 v[22:23], v[22:23], v[118:119], v[54:55]\n" \
"v_pk_fma_f32 v[30:31], v[30:31], v[134:135], v[62:63]\n" \
"v_pk_fma_f32 v[26:27], v[26:27], v[122:123], v[58:59]\n" \
"v_pk_fma_f32 v[34:35], v[34:35], v[138:139], v[66:67]\n" \
"v_cvt_pk_bf16_f32 v124, v20, v21\n" \
"v_cvt_pk_bf16_f32 v140, v28, v29\n" \
"v_cvt_pk_bf16_f32 v126, v24, v25\n" \
"v_cvt_pk_bf16_f32 v142, v32, v33\n" \
"v_cvt_pk_bf16_f32 v125, v22, v23\n" \
"v_cvt_pk_bf16_f32 v141, v30, v31\n" \
"v_cvt_pk_bf16_f32 v127, v26, v27\n" \
"v_cvt_pk_bf16_f32 v143, v34, v35\n" \
"v_permlane16_swap_b32 v124, v126\n" \
"v_permlane16_swap_b32 v140, v142\n" \
"v_permlane16_swap_b32 v125, v127\n" \
"v_permlane16_swap_b32 v141, v143\n" \
"global_store_dwordx4 v1, v[124:127], s[72:73] offset:0\n" \
"global_store_dwordx4 v1, v[140:143], s[72:73] offset:64\n" \
"v_pk_add_f32 v[36:37], v[36:37], v[68:69]\n" \
"v_pk_add_f32 v[44:45], v[44:45], v[76:77]\n" \
"v_pk_add_f32 v[40:41], v[40:41], v[72:73]\n" \
"v_pk_add_f32 v[48:49], v[48:49], v[80:81]\n" \
"v_pk_add_f32 v[38:39], v[38:39], v[70:71]\n" \
"v_pk_add_f32 v[46:47], v[46:47], v[78:79]\n" \
"v_pk_add_f32 v[42:43], v[42:43], v[74:75]\n" \
"v_pk_add_f32 v[50:51], v[50:51], v[82:83]\n" \
"v_pk_mul_f32 v[148:149], v[36:37], v[2:3] op_sel_hi:[1,0]\n" \
"v_pk_mul_f32 v[164:165], v[44:45], v[2:3] op_sel_hi:[1,0]\n" \
"v_pk_mul_f32 v[152:153], v[40:41], v[2:3] op_sel_hi:[1,0]\n" \
"v_pk_mul_f32 v[168:169], v[48:49], v[2:3] op_sel_hi:[1,0]\n" \
"v_pk_mul_f32 v[150:151], v[38:39], v[2:3] op_sel_hi:[1,0]\n" \
"v_pk_mul_f32 v[166:167], v[46:47], v[2:3] op_sel_hi:[1,0]\n" \
"v_pk_mul_f32 v[154:155], v[42:43], v[2:3] op_sel_hi:[1,0]\n" \
"v_pk_mul_f32 v[170:171], v[50:51], v[2:3] op_sel_hi:[1,0]\n" \
"v_exp_f32 v148, v148\n" \
"v_exp_f32 v164, v164\n" \
"v_exp_f32 v152, v152\n" \
"v_exp_f32 v168, v168\n" \
"v_exp_f32 v149, v149\n" \
"v_exp_f32 v165, v165\n" \
"v_exp_f32 v153, v153\n" \
"v_exp_f32 v169, v169\n" \
"v_exp_f32 v150, v150\n" \
"v_exp_f32 v166, v166\n" \
"v_exp_f32 v154, v154\n" \
"v_exp_f32 v170, v170\n" \
"v_exp_f32 v151, v151\n" \
"v_exp_f32 v167, v167\n" \
"v_exp_f32 v155, v155\n" \
"v_exp_f32 v171, v171\n" \
"v_pk_mul_f32 v[36:37], v[36:37], v[100:101]\n" \
"v_pk_mul_f32 v[44:45], v[44:45], v[108:109]\n" \
"v_pk_mul_f32 v[40:41], v[40:41], v[104:105]\n" \
"v_pk_mul_f32 v[48:49], v[48:49], v[112:113]\n" \
"v_pk_mul_f32 v[38:39], v[38:39], v[102:103]\n" \
"v_pk_mul_f32 v[46:47], v[46:47], v[110:111]\n" \
"v_pk_mul_f32 v[42:43], v[42:43], v[106:107]\n" \
"v_pk_mul_f32 v[50:51], v[50:51], v[114:115]\n" \
"v_pk_add_f32 v[148:149], v[148:149], 1.0 op_sel_hi:[1,0]\n" \
"v_pk_add_f32 v[164:165], v[164:165], 1.0 op_sel_hi:[1,0]\n" \
"v_pk_add_f32 v[152:153], v[152:153], 1.0 op_sel_hi:[1,0]\n" \
"v_pk_add_f32 v[168:169], v[168:169], 1.0 op_sel_hi:[1,0]\n" \
"v_pk_add_f32 v[150:151], v[150:151], 1.0 op_sel_hi:[1,0]\n" \
"v_pk_add_f32 v[166:167], v[166:167], 1.0 op_sel_hi:[1,0]\n" \
"v_pk_add_f32 v[154:155], v[154:155], 1.0 op_sel_hi:[1,0]\n" \
"v_pk_add_f32 v[170:171], v[170:171], 1.0 op_sel_hi:[1,0]\n" \
"v_rcp_f32 v148, v148\n" \
"v_rcp_f32 v164, v164\n" \
"v_rcp_f32 v152, v152\n" \
"v_rcp_f32 v168, v168\n" \
"v_rcp_f32 v149, v149\n" \
"v_rcp_f32 v165, v165\n" \
"v_rcp_f32 v153, v153\n" \
"v_rcp_f32 v169, v169\n" \
"v_rcp_f32 v150, v150\n" \
"v_rcp_f32 v166, v166\n" \
"v_rcp_f32 v154, v154\n" \
"v_rcp_f32 v170, v170\n" \
"v_rcp_f32 v151, v151\n" \
"v_rcp_f32 v167, v167\n" \
"v_rcp_f32 v155, v155\n" \
"v_rcp_f32 v171, v171\n" \
"v_lshlrev_b32 v68, 16, v220\n" \
"v_lshlrev_b32 v76, 16, v224\n" \
"v_lshlrev_b32 v72, 16, v222\n" \
"v_lshlrev_b32 v80, 16, v226\n" \
"v_and_b32 v69, 0xffff0000, v220\n" \
"v_and_b32 v77, 0xffff0000, v224\n" \
"v_and_b32 v73, 0xffff0000, v222\n" \
"v_and_b32 v81, 0xffff0000, v226\n" \
"v_lshlrev_b32 v70, 16, v221\n" \
"v_lshlrev_b32 v78, 16, v225\n" \
"v_lshlrev_b32 v74, 16, v223\n" \
"v_lshlrev_b32 v82, 16, v227\n" \
"v_and_b32 v71, 0xffff0000, v221\n" \
"v_and_b32 v79, 0xffff0000, v225\n" \
"v_and_b32 v75, 0xffff0000, v223\n" \
"v_and_b32 v83, 0xffff0000, v227\n" \
"v_pk_fma_f32 v[36:37], v[36:37], v[148:149], v[68:69]\n" \
"v_pk_fma_f32 v[44:45], v[44:45], v[164:165], v[76:77]\n" \
"v_pk_fma_f32 v[40:41], v[40:41], v[152:153], v[72:73]\n" \
"v_pk_fma_f32 v[48:49], v[48:49], v[168:169], v[80:81]\n" \
"v_pk_fma_f32 v[38:39], v[38:39], v[150:151], v[70:71]\n" \
"v_pk_fma_f32 v[46:47], v[46:47], v[166:167], v[78:79]\n" \
"v_pk_fma_f32 v[42:43], v[42:43], v[154:155], v[74:75]\n" \
"v_pk_fma_f32 v[50:51], v[50:51], v[170:171], v[82:83]\n" \
"v_cvt_pk_bf16_f32 v156, v36, v37\n" \
"v_cvt_pk_bf16_f32 v172, v44, v45\n" \
"v_cvt_pk_bf16_f32 v158, v40, v41\n" \
"v_cvt_pk_bf16_f32 v174, v48, v49\n" \
"v_cvt_pk_bf16_f32 v157, v38, v39\n" \
"v_cvt_pk_bf16_f32 v173, v46, v47\n" \
"v_cvt_pk_bf16_f32 v159, v42, v43\n" \
"v_cvt_pk_bf16_f32 v175, v50, v51\n" \
"v_permlane16_swap_b32 v156, v158\n" \
"v_permlane16_swap_b32 v172, v174\n" \
"v_permlane16_swap_b32 v157, v159\n" \
"v_permlane16_swap_b32 v173, v175\n" \
"global_store_dwordx4 v4, v[156:159], s[72:73] offset:0\n" \
"global_store_dwordx4 v4, v[172:175], s[72:73] offset:64\n" \
"s_branch LWE%=\n" \
"LWR%=_1:\n" \
"s_sub_u32 s40, s40, 57344\n" \
"s_subb_u32 s41, s41, 0\n" \
"s_sub_u32 s42, s42, 3670016\n" \
"s_subb_u32 s43, s43, 0\n" \
"s_branch LWS%=_1\n" \
"LWR%=_2:\n" \
"s_sub_u32 s40, s40, 57344\n" \
"s_subb_u32 s41, s41, 0\n" \
"s_sub_u32 s42, s42, 3670016\n" \
"s_subb_u32 s43, s43, 0\n" \
"s_branch LWS%=_2\n" \
"LWR%=_3:\n" \
"s_sub_u32 s44, s44, 32768\n" \
"s_subb_u32 s45, s45, 0\n" \
"s_sub_u32 s46, s46, 2097152\n" \
"s_subb_u32 s47, s47, 0\n" \
"s_branch LWS%=_3\n" \
"LWR%=_4:\n" \
"s_sub_u32 s44, s44, 32768\n" \
"s_subb_u32 s45, s45, 0\n" \
"s_sub_u32 s46, s46, 2097152\n" \
"s_subb_u32 s47, s47, 0\n" \
"s_branch LWS%=_4\n" \
"LWR%=_5:\n" \
"s_sub_u32 s44, s44, 32768\n" \
"s_subb_u32 s45, s45, 0\n" \
"s_sub_u32 s46, s46, 2097152\n" \
"s_subb_u32 s47, s47, 0\n" \
"s_branch LWS%=_5\n" \
"LWR%=_6:\n" \
"s_sub_u32 s44, s44, 32768\n" \
"s_subb_u32 s45, s45, 0\n" \
"s_sub_u32 s46, s46, 2097152\n" \
"s_subb_u32 s47, s47, 0\n" \
"s_branch LWS%=_6\n" \
"LWR%=_7:\n" \
"s_sub_u32 s44, s44, 32768\n" \
"s_subb_u32 s45, s45, 0\n" \
"s_sub_u32 s46, s46, 2097152\n" \
"s_subb_u32 s47, s47, 0\n" \
"s_branch LWS%=_7\n" \
"LWR%=_8:\n" \
"s_sub_u32 s44, s44, 32768\n" \
"s_subb_u32 s45, s45, 0\n" \
"s_sub_u32 s46, s46, 2097152\n" \
"s_subb_u32 s47, s47, 0\n" \
"s_branch LWS%=_8\n" \
"LWR%=_9:\n" \
"s_sub_u32 s44, s44, 32768\n" \
"s_subb_u32 s45, s45, 0\n" \
"s_sub_u32 s46, s46, 2097152\n" \
"s_subb_u32 s47, s47, 0\n" \
"s_branch LWS%=_9\n" \
"LWR%=_10:\n" \
"s_sub_u32 s44, s44, 32768\n" \
"s_subb_u32 s45, s45, 0\n" \
"s_sub_u32 s46, s46, 2097152\n" \
"s_subb_u32 s47, s47, 0\n" \
"s_branch LWS%=_10\n" \
"LWR%=_11:\n" \
"s_sub_u32 s44, s44, 32768\n" \
"s_subb_u32 s45, s45, 0\n" \
"s_sub_u32 s46, s46, 2097152\n" \
"s_subb_u32 s47, s47, 0\n" \
"s_branch LWS%=_11\n" \
"LWR%=_12:\n" \
"s_sub_u32 s44, s44, 32768\n" \
"s_subb_u32 s45, s45, 0\n" \
"s_sub_u32 s46, s46, 2097152\n" \
"s_subb_u32 s47, s47, 0\n" \
"s_branch LWS%=_12\n" \
"LWR%=_13:\n" \
"s_sub_u32 s44, s44, 32768\n" \
"s_subb_u32 s45, s45, 0\n" \
"s_sub_u32 s46, s46, 2097152\n" \
"s_subb_u32 s47, s47, 0\n" \
"s_branch LWS%=_13\n" \
"LWR%=_14:\n" \
"s_sub_u32 s44, s44, 32768\n" \
"s_subb_u32 s45, s45, 0\n" \
"s_sub_u32 s46, s46, 2097152\n" \
"s_subb_u32 s47, s47, 0\n" \
"s_branch LWS%=_14\n" \
"LWR%=_15:\n" \
"s_sub_u32 s44, s44, 32768\n" \
"s_subb_u32 s45, s45, 0\n" \
"s_sub_u32 s46, s46, 2097152\n" \
"s_subb_u32 s47, s47, 0\n" \
"s_branch LWS%=_15\n" \
"LWR%=_16:\n" \
"s_sub_u32 s44, s44, 32768\n" \
"s_subb_u32 s45, s45, 0\n" \
"s_sub_u32 s46, s46, 2097152\n" \
"s_subb_u32 s47, s47, 0\n" \
"s_branch LWS%=_16\n" \
"LWR%=_17:\n" \
"s_sub_u32 s44, s44, 32768\n" \
"s_subb_u32 s45, s45, 0\n" \
"s_sub_u32 s46, s46, 2097152\n" \
"s_subb_u32 s47, s47, 0\n" \
"s_branch LWS%=_17\n" \
"LWR%=_18:\n" \
"s_sub_u32 s44, s44, 32768\n" \
"s_subb_u32 s45, s45, 0\n" \
"s_sub_u32 s46, s46, 2097152\n" \
"s_subb_u32 s47, s47, 0\n" \
"s_branch LWS%=_18\n" \
"LWE%=:\n"
#define K2_ASM_CLOB "v20", "v21", "v22", "v23", "v24", "v25", "v26", "v27", "v28", "v29", "v30", "v31", "v32", "v33", "v34", "v35", "v36", "v37", "v38", "v39", "v40", "v41", "v42", "v43", "v44", "v45", "v46", "v47", "v48", "v49", "v50", "v51", "v52", "v53", "v54", "v55", "v56", "v57", "v58", "v59", "v60", "v61", "v62", "v63", "v64", "v65", "v66", "v67", "v68", "v69", "v70", "v71", "v72", "v73", "v74", "v75", "v76", "v77", "v78", "v79", "v80", "v81", "v82", "v83", "v84", "v85", "v86", "v87", "v88", "v89", "v90", "v91", "v92", "v93", "v94", "v95", "v96", "v97", "v98", "v99", "v100", "v101", "v102", "v103", "v104", "v105", "v106", "v107", "v108", "v109", "v110", "v111", "v112", "v113", "v114", "v115", "v116", "v117", "v118", "v119", "v120", "v121", "v122", "v123", "v124", "v125", "v126", "v127", "v128", "v129", "v130", "v131", "v132", "v133", "v134", "v135", "v136", "v137", "v138", "v139", "v140", "v141", "v142", "v143", "v144", "v145", "v146", "v147", "v148", "v149", "v150", "v151", "v152", "v153", "v154", "v155", "v156", "v157", "v158", "v159", "v160", "v161", "v162", "v163", "v164", "v165", "v166", "v167", "v168", "v169", "v170", "v171", "v172", "v173", "v174", "v175", "v176", "v177", "v178", "v179", "v180", "v181", "v182", "v183", "v184", "v185", "v186", "v187", "v188", "v189", "v190", "v191", "v192", "v193", "v194", "v195", "v196", "v197", "v198", "v199", "v200", "v201", "v202", "v203", "v204", "v205", "v206", "v207", "v208", "v209", "v210", "v211", "v212", "v213", "v214", "v215", "v216", "v217", "v218", "v219", "v220", "v221", "v222", "v223", "v224", "v225", "v226", "v227", "vcc", "scc", "memory"

#define K2_STAMP4
#define K2_STAMP3
#define K2_STAMP0
#define K2_STAMP1
#define K2_STAMP2
#define ROT_D 0
#define ROT_S (2 * (pm2 & 7))
constexpr int A2 = 0, B2 = 16384;
constexpr int LDS_ALL = 163840;

__global__ __launch_bounds__(256, 1) void k_fused(const u16* __restrict__ x, const u16* __restrict__ wqkv, const u16* __restrict__ wg, const u16* __restrict__ wu,
                                                  const u16* __restrict__ wo, const u16* __restrict__ wd, char* __restrict__ ws, u16* __restrict__ out) {
#define LO(v) rfl((unsigned)(v))
#define HI(v) rfl((unsigned)((v) >> 32))
  const int wid0 = rfl(threadIdx.x >> 6), wid = wid0, wr = wid & 1, wc = wid >> 1;
  const int b = blockIdx.x;
  const int pn = (b & 7) * 4 + ((b >> 3) & 3), pm = b >> 5;
  STAMP(1, 0);
  u32x8 vdma; u32x16 sA; u32x4 sC0; u32x16 rows; u32x2 vtl; int lane;
  {
    const int l = threadIdx.x & 63, g = l >> 4, r = l & 15;
    const unsigned xlane = (l >> 2) * 2048 + (((l & 3) ^ (((l >> 5) & 1) << 1)) * 16);
#pragma unroll
    for (int i = 0; i < 4; ++i) { int blk = 4 * wid + i; if (pm == 0 && blk == 0) blk = 1; vdma[i] = blk * 32768 + xlane - i * 1024; }
#pragma unroll
    for (int i = 0; i < 2; ++i) { const int row = 8 * wid + 4 * i + g, mask = ((row & 3) << 2) | (((row >> 3) & 1) << 1); int ch = r ^ mask; if (ch > 13) ch = 13;
      vdma[4 + i] = row * 7168 + pn * 224 + ch * 16 - i * 1024; }
    const char* xb = (const char*)x + ((ptrdiff_t)256 * pm - 16) * 2048 + pm * 256;
    const char* zb; unsigned incz, lz, z9;
    if (wid < 3) {
#pragma unroll
      for (int i = 0; i < 2; ++i) { const int c = 64 * (2 * wid + i) + l, row = c / 12, pos = c % 12, ch = pos ^ (((row >> 3) & 1) << 1);
        vdma[6 + i] = row * 6144 + (ch >> 2) * 2048 + pn * 64 + (ch & 3) * 16 - i * 1024; }
      zb = (const char*)wqkv + (size_t)pm * 128 * 6144; incz = 32 * 6144; lz = ZOFF + 2 * wid * 1024; z9 = 1;
    } else { vdma[6] = 16 * 32768 + xlane; vdma[7] = vdma[6]; zb = xb; incz = 64; lz = XOFF + 16 * 1024; z9 = 0; }
    const uint64_t pX = (uint64_t)xb, pG = (uint64_t)wg + (uint64_t)pm * 128 * 7168, pU = (uint64_t)wu + (uint64_t)pm * 128 * 7168, pZ = (uint64_t)zb;
        sA = (u32x16){LO(pX), HI(pX), LO(pG), HI(pG), LO(pU), HI(pU), LO(pZ), HI(pZ), rfl(incz), rfl(XOFF + wid * 4096), rfl(GOFF + wid * 2048), rfl(UOFF + wid * 2048), rfl(lz), rfl(z9), rfl(wid == 0 ? 3u : 0u), 0u};
    const uint64_t pR = (uint64_t)((const char*)x + (size_t)(256 * pm + 8 * pn + 2 * wid) * 2048), pS = (uint64_t)(ws + WS_SYNC);
    sC0 = (u32x4){LO(pR), HI(pR), LO(pS), HI(pS)};
    SCHED();
    asm volatile(K1_ASM_A
      : "+{v[10:17]}"(vdma), "+{s[40:55]}"(sA), "={v[20:35]}"(rows), "={v[112:113]}"(vtl), [lane] "=v"(lane)
      : "{s[72:75]}"(sC0)
      : K1_ASM_CLOB_A
      );
  }
  SCHED();
  const int g = lane >> 4, r = lane & 15, q = (lane & 15) >> 2, p = lane & 3;
  u32x8 vrd; u32x2 vrd2; u32x2 vmisc; u32x8 vep;
  const unsigned xrd = r * 64 + ((g ^ (((r >> 3) & 1) << 1)) * 16);
  vrd[0] = XOFF + wr * 8192 + xrd;
  const int maskG = (q << 2) | ((g & 1) << 1), rowb = 8 * g + q;
  unsigned aG[4];
#pragma unroll
  for (int j = 0; j < 4; ++j) aG[j] = GOFF + 256 * rowb + 16 * ((8 * wc + 2 * j + (p >> 1)) ^ maskG) + 8 * (p & 1);
  auto aZ = [&](int z) -> unsigned { return ZOFF + 192 * rowb + 16 * ((2 * z + (p >> 1)) ^ ((g & 1) << 1)) + 8 * (p & 1); };
  vrd[1] = aG[0]; vrd[2] = aG[1]; vrd[3] = aG[2];
  if (wc == 0) { vrd[4] = aG[3]; vrd[5] = aG[3] + 1024; vrd[6] = aG[3] + 8192; vrd[7] = aG[3] + 9216; }
  else { vrd[4] = aZ(0); vrd[5] = aZ(0) + 768; vrd[6] = aZ(1); vrd[7] = aZ(1) + 768; }
  const int zb0 = wc ? 4 : 2;
  vrd2[0] = aZ(zb0 + wr); vrd2[1] = aZ(zb0 + 1 - wr);
  vmisc[0] = XOFF + xrd;
  vmisc[1] = (lane >> 3) * 2048 + (lane & 7) * 128;
  constexpr int QS = 65536, KS = QS + 16384, VS = KS + 17408;
  { const unsigned wofs = r * 32 + g * 8, kv = (wc ? VS : KS) + wofs;
    vep[0] = kv + wr * 8704 + 8 * wr * 512; vep[1] = kv + (1 - wr) * 8704 + 8 * wr * 512; vep[2] = kv + wr * 8704; vep[3] = QS + 8 * wr * 1024 + wofs;
    vep[4] = (7 * pn + 4 * wc + (g & 1)) * 512 + r * 32 + (g >> 1) * 16; vep[5] = (7 * pn + 6) * 512 + (g & 1) * (224 * 512) + r * 32 + (g >> 1) * 16;
    auto bidx = [](int pm_, int pn_) { return (pm_ << 5) | ((pn_ & 3) << 3) | (pn_ >> 2); };
    int fo = SY_F1 + bidx(pm, lane & 31) * 64;
    if (lane == 32 || lane == 33) fo = SY_F1 + bidx(pm > 0 ? pm - 1 : 0, 30 + (lane & 1)) * 64;
    if (lane >= 34 && lane < 42) fo = SY_F2 + bidx(lane - 34, pn) * 64;
    vep[6] = fo; vep[7] = fo; }
  const int xc = b & 7, ib = b >> 3, ai0 = wc ? 6 + 5 * wr : 3 * wr;
  const uint64_t pH = (uint64_t)(ws + WS_H + (size_t)(16 * pm + 8 * wr) * (224 * 512)), pO = (uint64_t)(ws + WS_O);
  const uint64_t pD = (uint64_t)((const char*)wd + (size_t)(1792 * (xc & 1) + 56 * ib + 8 * wid) * 2048 + (xc >> 2) * 1024);
  const uint64_t pW = (uint64_t)((const char*)wo + (size_t)(32 * ib + 8 * wid) * 2048 + (xc >> 2) * 1024);
  const unsigned pub = 0;
  u32x16 sB = {LO(pH), HI(pH), LO(pO), HI(pO), LO(pD), HI(pD), LO(pW), HI(pW), rfl(wid < 3 ? 32 * 2048 : 0), rfl(wc), rfl(wr),
               rfl(QS + ai0 * 1024), rfl(KS + ai0 * 512), rfl(VS + ai0 * 512), rfl(((16 * pm + ai0) * 64 + 2 * pn) * 512), rfl(pub)};
  u32x8 sC1 = {rfl(SY_CSP + pn * 8 * 2048), rfl(pm == 0 ? 64u : 0u), rfl(SY_F1 + b * 64), rfl(SY_ST + (256 * pm + 8 * pn + 2 * wid) * 4),
               rfl((pm == 0 && wid == 0) ? 0u : ~0u), rfl((b == 0 && wid == 0) ? 1u : 0u), rfl(SY_CSP + (pn * 8 + pm) * 2048 + wc * 1024 + wr * 512), rfl(SY_ST + (256 * pm - 16) * 4)};
  const unsigned scr = rfl(SY_SCR + (b * 4 + wid) * 512), wrapit = rfl(7 - pm), wrapz = rfl(wid < 3 ? 1024 * 6144 : 2048);
  u32x2 epoch;
  SCHED();
  asm volatile(K1_ASM_BODY
      : "+{v[0:7]}"(vrd), "+{v[8:9]}"(vrd2), "+{v[10:17]}"(vdma), "+{v[18:19]}"(vmisc), "+{v[20:35]}"(rows), "+{v[248:255]}"(vep), "+{v[112:113]}"(vtl),
        "+{s[40:55]}"(sA), "+{s[56:71]}"(sB), "+{s[72:75]}"(sC0), "+{s[76:83]}"(sC1), "={s[84:85]}"(epoch)
      : [scr] "s"(scr), [wrapit] "s"(wrapit), [wrapz] "s"(wrapz)
      : K1_ASM_CLOB
      );
  SCHED();
  {
    typedef __attribute__((address_space(4))) const uint64_t* kaptr_t;
    kaptr_t ka = (kaptr_t)__builtin_amdgcn_kernarg_segment_ptr();
    int b = blockIdx.x, wid = wid0;
    asm volatile("" : "+s"(ka), "+s"(b), "+s"(wid));
    const u16* x = (const u16*)ka[0]; const u16* wo = (const u16*)ka[4]; const u16* wd = (const u16*)ka[5]; char* ws = (char*)ka[6]; u16* out = (u16*)ka[7];
    const int xc = b & 7, ib = b >> 3;
    const int lane = lane_id();
    const int g = lane >> 4, r = lane & 15, q = (lane & 15) >> 2, p = lane & 3;
    const int kh = xc & 1, pm2 = ((xc >> 1) & 1) * 8 + (ib & 7), pn2 = (xc >> 2) * 4 + (ib >> 3);
    const int wr2 = wid >> 1, wc2 = wid & 1, partner = b ^ 1;
    u32x16 v2; u32x4 v2b;
    const unsigned aA = A2 + (g >> 1) * 512 + r * 32 + (g & 1) * 16;
    const int maskG2 = (q << 2) | ((g & 1) << 1);
    v2[0] = aA + (4 * kh + 2 * wr2) * 2048; v2[1] = aA + (4 * (kh ^ 1) + 2 * wr2) * 2048; v2[2] = aA + (2 * wr2) * 2048;
#pragma unroll
    for (int n = 0; n < 4; ++n) v2[3 + n] = B2 + 256 * (8 * g + q) + 16 * ((8 * wc2 + 2 * n + (p >> 1)) ^ maskG2) + 8 * (p & 1);
    v2[7] = lane * 16; v2[8] = lane * 16 + 4096;
    v2[9] = lane * 16; v2[10] = lane * 16 + 224 * 512 - 2048;
#pragma unroll
    for (int c = 0; c < 4; ++c) v2[11 + c] = (4 * c + g) * 2048 + ((r ^ ((g << 2) | (c & 2))) * 16) - 1024 * c;
    v2[15] = r * 2048 + g * 8;
    auto bidx = [](int pm_, int pn_) { return (pm_ << 5) | ((pn_ & 3) << 3) | (pn_ >> 2); };
    v2b[0] = lane; v2b[1] = lane; v2b[2] = lane; v2b[3] = SY_F3 + bidx(pm2 >> 1, lane & 31) * 64;
    const int offd = ROT_D, offs = ROT_S;
    const uint64_t ha = (uint64_t)(ws + WS_H + ((size_t)(8 * pm2 + 2 * wid) * 224 + 112 * kh + 4 * offd) * 512), wb = (uint64_t)((const char*)wd + ((size_t)1792 * kh + 64 * offd + 16 * wid) * 2048 + pn2 * 256);
    const uint64_t oa = (uint64_t)(ws + WS_O + ((size_t)(8 * pm2 + 4 * kh + wid) * 64 + 4 * offs) * 512), ob = (uint64_t)((const char*)wo + (size_t)(64 * offs + 16 * wid) * 2048 + pn2 * 256);
    u32x16 s2 = {LO(ha), HI(ha), LO(wb), HI(wb), LO(oa), HI(oa), LO(ob), HI(ob), rfl(A2 + 2 * wid * 2048), rfl(A2 + wid * 2048), rfl(B2 + 16 * wid * 256), 0u, 0u, 0u, 0u, 0u};
    const uint64_t exs = (uint64_t)(ws + WS_EX + (size_t)partner * 32768 + wid * 8192), exr = (uint64_t)(ws + WS_EX + (size_t)b * 32768 + wid * 8192), sy = (uint64_t)(ws + WS_SYNC);
    const int row0 = 128 * pm2 + 64 * kh + 32 * wr2, col0 = 128 * pn2 + 64 * wc2;
    const uint64_t xb2 = (uint64_t)(x + (size_t)row0 * D_ + col0);
    u32x16 s2b = {LO(exs), HI(exs), LO(exr), HI(exr), LO(sy), HI(sy), rfl(SY_F4 + partner * 64), rfl(SY_F4 + b * 64), rfl(SY_F3 + b * 64), rfl(wid == 0 ? 1u : 0u), 0u, 0u,
                  rfl(epoch[0]), rfl(epoch[1]), LO(xb2), HI(xb2)};
    const uint64_t ob2 = (uint64_t)(out + (size_t)row0 * D_ + col0);
    u32x4 s2c = {LO(ob2), HI(ob2), rfl(offd ? 0u - (unsigned)(28 - offd - 4) : 1u), rfl(offs ? 0u - (unsigned)(16 - offs) : 1u)};
    SCHED();
    asm volatile(K2_ASM_BODY
      : "+{v[0:15]}"(v2), "+{v[16:19]}"(v2b), "+{s[40:55]}"(s2), "+{s[56:71]}"(s2b), "+{s[72:75]}"(s2c)
      :
      : K2_ASM_CLOB
      );
    STAMP(2, 4);
  }
#undef LO
#undef HI
}

extern "C" __global__ void k_code_end() {}

extern "C" void kernel_launch(void* const* d_in, const int* in_sizes, int n_in, void* d_out, int out_size, void* d_ws, size_t ws_size, hipStream_t stream) {
  const u16* x = (const u16*)d_in[0]; const u16* wqkv = (const u16*)d_in[1]; const u16* wo = (const u16*)d_in[2];
  const u16* wg = (const u16*)d_in[4]; const u16* wu = (const u16*)d_in[5]; const u16* wd = (const u16*)d_in[6];
  char* ws = (char*)d_ws;
  hipFuncSetAttribute((const void*)k_fused, hipFuncAttributeMaxDynamicSharedMemorySize, LDS_ALL);
  k_fused<<<256, 256, LDS_ALL, stream>>>(x, wqkv, wg, wu, wo, wd, ws, (u16*)d_out);
}
```
